```python
import torch

def _softmax(x, axis=-1):
    return torch.softmax(x, dim=axis)
def _gelu(x):
    return torch.nn.functional.gelu(x, approximate="tanh")
def _silu(x):
    return torch.nn.functional.silu(x)
def _sigmoid(x):
    return torch.sigmoid(x)
def _layernorm(x, axis=-1, eps=1e-5):
    m = x.mean(dim=axis, keepdim=True)
    v = x.var(dim=axis, keepdim=True, unbiased=False)
    return (x - m) / torch.sqrt(v + eps)
def _scatter_add(tgt, idx, src, axis=0):
    return tgt.clone().index_add_(axis, idx, src)
def _segment_sum(src, seg, n):
    out = torch.zeros((n,) + src.shape[1:], dtype=src.dtype)
    return out.index_add_(0, seg, src)
def _pad(x, pw):
    flat = tuple(v for pair in reversed(pw) for v in pair)
    return torch.nn.functional.pad(x, flat)

kernel_name = 'parallel_sum__sliding_block__dense_activation_relu__prompt'

def setup_inputs():

    _w = (lambda a: (lambda t: t.to(torch.bfloat16) if t.is_floating_point() else t)(torch.as_tensor(a)))
    def _A_setup():
        B, S, D = 1, 2048, 1024
        return {
            "x": _w((lambda *s: torch.randn(*s, dtype=torch.bfloat16) * 0.05)(B, S, D)),
            "wqkv": _w((lambda *s: torch.randn(*s, dtype=torch.bfloat16) * 0.05)(D, 3 * D)),
            "wo": _w((lambda *s: torch.randn(*s, dtype=torch.bfloat16) * 0.05)(D, D)),
        }
    _A_in = _A_setup()

    def _B_setup():
        B, S, D, Dff = 1, 2048, 1024, 3584
        return {
            "x": _w((lambda *s: torch.randn(*s, dtype=torch.bfloat16) * 0.05)(B, S, D)),
            "w1": _w((lambda *s: torch.randn(*s, dtype=torch.bfloat16) * 0.05)(D, Dff)),
            "w2": _w((lambda *s: torch.randn(*s, dtype=torch.bfloat16) * 0.05)(Dff, D)),
        }
    _B_in = _B_setup()

    return {
        "A_x": _A_in["x"],
        "A_wqkv": _A_in["wqkv"],
        "A_wo": _A_in["wo"],
        "B_x": _B_in["x"],
        "B_w1": _B_in["w1"],
        "B_w2": _B_in["w2"],
    }

def reference(A_x, A_wqkv, A_wo, B_x, B_w1, B_w2):
    def _A_ref(x, wqkv, wo):
        B, S, D = x.shape
        H = 32; Dh = D // H; W = 256
        qkv = (lambda x, *d: x.permute(*d))((x @ wqkv).reshape(B, S, 3, H, Dh), 2, 0, 3, 1, 4)
        q, k, v = qkv[0], qkv[1], qkv[2]
        scores = torch.einsum("bhqd,bhkd->bhqk", q, k) * (Dh ** -0.5)
        idx = torch.arange(S)
        mask = (idx[:, None] - idx[None, :] >= 0) & (idx[:, None] - idx[None, :] < W)
        scores = torch.where(mask[None, None], scores, -1e9)
        p = _softmax(scores, axis=-1)
        o = torch.einsum("bhqk,bhkd->bhqd", p, v)
        return (lambda x, *d: x.permute(*d))(o, 0, 2, 1, 3).reshape(B, S, D) @ wo

    def _B_ref(x, w1, w2):
        h = torch.einsum("bsd,df->bsf", x, w1)
        zero = torch.zeros_like(h)
        h = torch.maximum(h, zero)
        return torch.einsum("bsf,fd->bsd", h, w2)

    x = A_x
    xn = _layernorm(x, axis=-1)
    return x + _A_ref(xn, A_wqkv, A_wo) + _B_ref(xn, B_w1, B_w2)
```

```cpp
#include <hip/hip_runtime.h>
#include <hip/hip_bf16.h>
#include <stdint.h>

typedef unsigned short u16;
typedef short bf16x8 __attribute__((ext_vector_type(8)));
typedef short s16x4 __attribute__((ext_vector_type(4)));
typedef float f32x4 __attribute__((ext_vector_type(4)));
typedef unsigned u32x4 __attribute__((ext_vector_type(4)));
typedef unsigned u32x2 __attribute__((ext_vector_type(2)));
#define LAS __attribute__((address_space(3)))

constexpr int S = 2048, D = 1024, H = 32, DH = 32, W = 256, FF = 3584;

__device__ __forceinline__ float bf2f(u16 v) { return __uint_as_float(((unsigned)v) << 16); }
__device__ __forceinline__ unsigned pk2(float lo, float hi) {
  typedef float f2 __attribute__((ext_vector_type(2)));
  typedef __bf16 b2 __attribute__((ext_vector_type(2)));
  f2 v = {lo, hi}; b2 b = __builtin_convertvector(v, b2); return __builtin_bit_cast(unsigned, b);
}

__global__ __launch_bounds__(256) void ln_kernel(const u16* __restrict__ x, u16* __restrict__ xn) {
  const int lane = threadIdx.x & 63, row = blockIdx.x * 4 + (threadIdx.x >> 6);
  const u32x4* p = (const u32x4*)(x + (size_t)row * D);
  u32x4 v[2] = {p[lane], p[64 + lane]};
  float f[16]; float s = 0.f;
#pragma unroll
  for (int i = 0; i < 2; ++i)
#pragma unroll
    for (int j = 0; j < 4; ++j) { f[i * 8 + 2 * j] = __uint_as_float(v[i][j] << 16); f[i * 8 + 2 * j + 1] = __uint_as_float(v[i][j] & 0xffff0000u); }
#pragma unroll
  for (int i = 0; i < 16; ++i) s += f[i];
#pragma unroll
  for (int o = 1; o < 64; o <<= 1) s += __shfl_xor(s, o);
  const float mean = s * (1.f / D); float q = 0.f;
#pragma unroll
  for (int i = 0; i < 16; ++i) { f[i] -= mean; q += f[i] * f[i]; }
#pragma unroll
  for (int o = 1; o < 64; o <<= 1) q += __shfl_xor(q, o);
  const float rstd = 1.f / sqrtf(q * (1.f / D) + 1e-5f);
  u32x4* o = (u32x4*)(xn + (size_t)row * D);
#pragma unroll
  for (int i = 0; i < 2; ++i) { u32x4 w;
#pragma unroll
    for (int j = 0; j < 4; ++j) w[j] = pk2(f[i * 8 + 2 * j] * rstd, f[i * 8 + 2 * j + 1] * rstd);
    o[i * 64 + lane] = w; }
}

__device__ __forceinline__ void gemm_acc(f32x4 (&acc)[4][4], const u16* __restrict__ A, int lda, const u16* __restrict__ B, int ldb, int K, int m0, int n0, char* lds) {
  const int tid = threadIdx.x, lane = tid & 63, wid = tid >> 6, wr = wid >> 1, wc = wid & 1;
  char* sA = lds; char* sB = lds + 128 * 80;
  for (int k0 = 0; k0 < K; k0 += 32) {
    u32x4 ra[2], rb[2];
#pragma unroll
    for (int i = 0; i < 2; ++i) { int c = tid + i * 256; int r = c >> 2, ch = c & 3; ra[i] = *(const u32x4*)(A + (size_t)(m0 + r) * lda + k0 + ch * 8); }
#pragma unroll
    for (int i = 0; i < 2; ++i) { int c = tid + i * 256; int k = c >> 4, ch = c & 15; rb[i] = *(const u32x4*)(B + (size_t)(k0 + k) * ldb + n0 + ch * 8); }
    __syncthreads();
#pragma unroll
    for (int i = 0; i < 2; ++i) { int c = tid + i * 256; int r = c >> 2, ch = c & 3; *(u32x4*)(sA + r * 80 + ch * 16) = ra[i]; }
#pragma unroll
    for (int i = 0; i < 2; ++i) { int c = tid + i * 256; int k = c >> 4, ch = c & 15; *(u32x4*)(sB + k * 272 + ch * 16) = rb[i]; }
    __syncthreads();
    bf16x8 af[4], bfr[4];
#pragma unroll
    for (int m = 0; m < 4; ++m) af[m] = *(const bf16x8*)(sA + (wr * 64 + m * 16 + (lane & 15)) * 80 + (lane >> 4) * 16);
#pragma unroll
    for (int n = 0; n < 4; ++n) {
      const int g = lane >> 4, q = (lane >> 2) & 3, p = lane & 3;
      LAS char* b0 = (LAS char*)sB + (8 * g + q) * 272 + (wc * 64 + n * 16 + 4 * p) * 2;
      s16x4 lo = __builtin_amdgcn_ds_read_tr16_b64_v4i16((LAS s16x4*)b0);
      s16x4 hi = __builtin_amdgcn_ds_read_tr16_b64_v4i16((LAS s16x4*)(b0 + 4 * 272));
      bfr[n] = (bf16x8){lo[0], lo[1], lo[2], lo[3], hi[0], hi[1], hi[2], hi[3]};
    }
#pragma unroll
    for (int m = 0; m < 4; ++m)
#pragma unroll
      for (int n = 0; n < 4; ++n) acc[m][n] = __builtin_amdgcn_mfma_f32_16x16x32_bf16(bfr[n], af[m], acc[m][n], 0, 0, 0);
  }
}

__global__ __launch_bounds__(256) void gemm1_kernel(const u16* __restrict__ xn, const u16* __restrict__ wqkv, const u16* __restrict__ w1, u16* __restrict__ qkv, u16* __restrict__ h) {
  __shared__ __attribute__((aligned(16))) char lds[128 * 80 + 32 * 272];
  const int nb = blockIdx.x, mb = blockIdx.y;
  const bool isq = nb < 24;
  const int n0 = isq ? nb * 128 : (nb - 24) * 128, m0 = mb * 128;
  f32x4 acc[4][4] = {};
  gemm_acc(acc, xn, D, isq ? wqkv : w1, isq ? 3 * D : FF, D, m0, n0, lds);
  const int lane = threadIdx.x & 63, wid = threadIdx.x >> 6, wr = wid >> 1, wc = wid & 1;
  u16* out = isq ? qkv : h; const int ldo = isq ? 3 * D : FF;
#pragma unroll
  for (int m = 0; m < 4; ++m)
#pragma unroll
    for (int n = 0; n < 4; ++n) {
      f32x4 v = acc[m][n];
      if (!isq) { v[0] = fmaxf(v[0], 0.f); v[1] = fmaxf(v[1], 0.f); v[2] = fmaxf(v[2], 0.f); v[3] = fmaxf(v[3], 0.f); }
      u32x2 w = {pk2(v[0], v[1]), pk2(v[2], v[3])};
      *(u32x2*)(out + (size_t)(m0 + wr * 64 + m * 16 + (lane & 15)) * ldo + n0 + wc * 64 + n * 16 + 4 * (lane >> 4)) = w;
    }
}

__global__ __launch_bounds__(256) void gemm2_kernel(const u16* __restrict__ o, const u16* __restrict__ wo, const u16* __restrict__ h, const u16* __restrict__ w2, const u16* __restrict__ x, u16* __restrict__ out) {
  __shared__ __attribute__((aligned(16))) char lds[128 * 80 + 32 * 272];
  const int n0 = blockIdx.x * 128, m0 = blockIdx.y * 128;
  f32x4 acc[4][4] = {};
  gemm_acc(acc, o, D, wo, D, D, m0, n0, lds);
  gemm_acc(acc, h, FF, w2, D, FF, m0, n0, lds);
  const int lane = threadIdx.x & 63, wid = threadIdx.x >> 6, wr = wid >> 1, wc = wid & 1;
#pragma unroll
  for (int m = 0; m < 4; ++m)
#pragma unroll
    for (int n = 0; n < 4; ++n) {
      const size_t off = (size_t)(m0 + wr * 64 + m * 16 + (lane & 15)) * D + n0 + wc * 64 + n * 16 + 4 * (lane >> 4);
      u32x2 xv = *(const u32x2*)(x + off);
      f32x4 v = acc[m][n];
      v[0] += __uint_as_float(xv[0] << 16); v[1] += __uint_as_float(xv[0] & 0xffff0000u); v[2] += __uint_as_float(xv[1] << 16); v[3] += __uint_as_float(xv[1] & 0xffff0000u);
      u32x2 w = {pk2(v[0], v[1]), pk2(v[2], v[3])};
      *(u32x2*)(out + off) = w;
    }
}

__global__ __launch_bounds__(64) void attn_kernel(const u16* __restrict__ qkv, u16* __restrict__ o) {
  const int qi = blockIdx.x * 64 + threadIdx.x, hd = blockIdx.y;
  float q[DH], acc[DH];
#pragma unroll
  for (int d = 0; d < DH; ++d) { q[d] = bf2f(qkv[(size_t)qi * 3 * D + hd * DH + d]); acc[d] = 0.f; }
  float m = -1e30f, l = 0.f;
  const int k_lo = qi - (W - 1) < 0 ? 0 : qi - (W - 1);
  for (int k = k_lo; k <= qi; ++k) {
    const u16* kp = qkv + (size_t)k * 3 * D + D + hd * DH; const u16* vp = kp + D;
    float s = 0.f;
#pragma unroll
    for (int d = 0; d < DH; ++d) s += q[d] * bf2f(kp[d]);
    s *= 0.17677669529663687f;
    const float mn = fmaxf(m, s), a = __expf(m - mn), p = __expf(s - mn);
    l = l * a + p; m = mn;
#pragma unroll
    for (int d = 0; d < DH; ++d) acc[d] = acc[d] * a + p * bf2f(vp[d]);
  }
  const float r = 1.f / l;
#pragma unroll
  for (int d = 0; d < DH; d += 2) *(unsigned*)(o + (size_t)qi * D + hd * DH + d) = pk2(acc[d] * r, acc[d + 1] * r);
}

extern "C" void kernel_launch(void* const* d_in, const int* in_sizes, int n_in, void* d_out, int out_size, void* d_ws, size_t ws_size, hipStream_t stream) {
  const u16* x = (const u16*)d_in[0]; const u16* wqkv = (const u16*)d_in[1]; const u16* wo = (const u16*)d_in[2];
  const u16* w1 = (const u16*)d_in[4]; const u16* w2 = (const u16*)d_in[5];
  u16* xn = (u16*)d_ws; u16* qkv = xn + (size_t)S * D; u16* h = qkv + (size_t)S * 3 * D; u16* o = h + (size_t)S * FF;
  ln_kernel<<<S / 4, 256, 0, stream>>>(x, xn);
  gemm1_kernel<<<dim3(52, 16), 256, 0, stream>>>(xn, wqkv, w1, qkv, h);
  attn_kernel<<<dim3(S / 64, H), 64, 0, stream>>>(qkv, o);
  gemm2_kernel<<<dim3(8, 16), 256, 0, stream>>>(o, wo, h, w2, x, (u16*)d_out);
}
```

```cpp
#include <hip/hip_runtime.h>
#include <hip/hip_bf16.h>

typedef unsigned short u16;
constexpr int MAIN_LDS = 163840;

__device__ __forceinline__ unsigned lo(const void* p) { return (unsigned)(size_t)p; }
__device__ __forceinline__ unsigned hi(const void* p) { return (unsigned)((size_t)p >> 32); }

__global__ __launch_bounds__(256, 1) void k1_main(const u16* __restrict__ wqkv, const u16* __restrict__ w1, const u16* __restrict__ w2, const u16* __restrict__ wo,
                                                  const u16* __restrict__ x, u16* __restrict__ out, char* __restrict__ ws) {
  extern __shared__ __attribute__((aligned(16))) char lds[];
  const unsigned bid = blockIdx.x, w = __builtin_amdgcn_readfirstlane(threadIdx.x >> 6);
  const unsigned grp = bid & 7, idx = bid >> 3, kap = grp & 3;
  const unsigned tm = (grp & 4) + (idx & 3), cb = idx >> 2, hd = 8 * kap + cb, ldsb = lo(lds);
  const unsigned pb = 448 * (hd >> 1), odd = hd & 1, sh = (hd >> 1) & 1;
  const unsigned cf = pb + 256 * odd + 64 * sh, ch = pb + 256 * odd + 128 * (sh ^ 1), cq = pb + 192 + 32 * odd;
  const unsigned rb = ((bid & 4) << 5) | (idx << 2) | kap;
  asm volatile(
      "km_code_begin:\n\t"
      "s_nop 4\n\t"
      "v_mbcnt_lo_u32_b32 v0, -1, 0\n\t"
      "v_mbcnt_hi_u32_b32 v0, -1, v0\n\t"
      "v_lshlrev_b32 v1, 4, v0\n\t"
      "s_mov_b32 s44, %0\n\t"
      "s_mov_b32 s45, %1\n\t"
      "s_mov_b32 s46, 53477376\n\t"
      "s_mov_b32 s47, 0x20000\n\t"
      "s_lshl_b32 s64, %24, 2\n\t"
      "s_add_u32 s64, s64, %14\n\t"
      "s_lshl_b32 s64, s64, 12\n\t"
      "s_add_u32 s48, %10, s64\n\t"
      "s_addc_u32 s49, %11, 0\n\t"
      "s_mov_b32 s64, 52428672\n\t"
      "v_mov_b32 v25, 0\n\t"
      "v_and_b32 v40, 31, v0\n\t"
      "v_lshrrev_b32 v41, 5, v0\n\t"
      "v_lshlrev_b32 v42, 4, v40\n\t"
      "v_lshl_add_u32 v42, v41, 11, v42\n\t"
      "buffer_load_dwordx2 v[46:47], v25, s[44:47], s64 offen sc1\n\t"
      "global_load_dwordx4 v[48:51], v42, s[48:49] offset:0\n\t"
      "global_load_dwordx4 v[52:55], v42, s[48:49] offset:512\n\t"
      "global_load_dwordx4 v[56:59], v42, s[48:49] offset:1024\n\t"
      "global_load_dwordx4 v[60:63], v42, s[48:49] offset:1536\n\t"
      "s_getpc_b64 s[48:49]\n\t"
      ".Lkm_pc31_%=:\n\t"
      "s_sub_u32 s48, s48, .Lkm_pc31_%=-km_code_begin\n\t"
      "s_subb_u32 s49, s49, 0\n\t"
      "s_mov_b32 s50, km_code_end-km_code_begin\n\t"
      "s_mov_b32 s51, 0x20000\n\t"
      "s_lshr_b32 s64, %15, 3\n\t"
      "s_lshl_b32 s64, s64, 11\n\t"
      "v_and_b32 v30, 15, v0\n\t"
      "v_lshlrev_b32 v30, 7, v30\n\t"
      "v_add_u32 v30, s64, v30\n\t"
      "buffer_load_dword v31, v30, s[48:51], 0 offen\n\t"
      "s_lshl_b32 s64, %17, 2\n\t"
      "s_add_u32 s64, s64, %14\n\t"
      "s_lshl_b32 s64, s64, 17\n\t"
      "s_add_u32 s64, s64, 4096\n\t"
      "s_add_u32 s36, %0, s64\n\t"
      "s_addc_u32 s37, %1, 0\n\t"
      "s_mov_b32 s40, %2\n\t"
      "s_mov_b32 s41, %3\n\t"
      "s_mov_b32 s42, %2\n\t"
      "s_mov_b32 s43, %3\n\t"
      "s_mov_b32 s52, %4\n\t"
      "s_mov_b32 s53, %5\n\t"
      "s_mov_b32 s60, 3\n\t"
      "s_lshl_b32 s62, %14, 11\n\t"
      "s_add_u32 s62, s62, %16\n\t"
      "s_lshl_b32 s63, %14, 10\n\t"
      "s_add_u32 s63, s63, %16\n\t"
      "s_lshl_b32 s64, %14, 4\n\t"
      "v_lshrrev_b32 v24, 2, v0\n\t"
      "v_add_u32 v24, s64, v24\n\t"
      "v_and_b32 v25, 3, v0\n\t"
      "v_lshrrev_b32 v26, 4, v0\n\t"
      "v_and_b32 v26, 2, v26\n\t"
      "v_xor_b32 v25, v25, v26\n\t"
      "v_lshlrev_b32 v25, 4, v25\n\t"
      "s_lshl_b32 s65, %18, 6\n\t"
      "v_mul_u32_u24 v26, 6144, v24\n\t"
      "v_add3_u32 v4, v26, v25, s65\n\t"
      "v_mul_u32_u24 v26, 7168, v24\n\t"
      "v_add3_u32 v7, v26, v25, %22\n\t"
      "v_lshrrev_b32 v24, 3, v0\n\t"
      "v_and_b32 v26, 2, v24\n\t"
      "v_add_u32 v24, s64, v24\n\t"
      "v_mul_u32_u24 v27, 6144, v24\n\t"
      "v_mul_u32_u24 v24, 7168, v24\n\t"
      "v_add_u32 v24, %21, v24\n\t"
      "v_and_b32 v25, 7, v0\n\t"
      "v_xor_b32 v25, v25, v26\n\t"
      "v_lshl_add_u32 v5, v25, 4, v24\n\t"
      "v_xor_b32 v26, 4, v25\n\t"
      "v_lshl_add_u32 v6, v26, 4, v24\n\t"
      "v_add_u32 v6, 57344, v6\n\t"
      "s_and_b32 s66, %18, 1\n\t"
      "s_mul_i32 s67, s66, 1984\n\t"
      "s_add_u32 s65, s65, s67\n\t"
      "s_add_u32 s65, s65, 2048\n\t"
      "s_lshl_b32 s66, s66, 2\n\t"
      "v_add_u32 v27, s65, v27\n\t"
      "v_xor_b32 v25, s66, v25\n\t"
      "v_lshl_add_u32 v2, v25, 4, v27\n\t"
      "v_xor_b32 v25, 4, v25\n\t"
      "v_lshl_add_u32 v3, v25, 4, v27\n\t"
      "v_add_u32 v3, 49152, v3\n\t"
      "v_bfe_u32 v24, v0, 1, 2\n\t"
      "v_bfe_u32 v25, v0, 3, 1\n\t"
      "v_lshl_add_u32 v24, v25, 3, v24\n\t"
      "v_bfe_u32 v25, v0, 4, 1\n\t"
      "v_lshl_add_u32 v24, v25, 2, v24\n\t"
      "v_add_u32 v24, s64, v24\n\t"
      "v_mul_u32_u24 v24, 7168, v24\n\t"
      "v_and_b32 v25, 1, v0\n\t"
      "v_lshlrev_b32 v25, 4, v25\n\t"
      "v_add3_u32 v8, v24, v25, %23\n\t"
      "s_lshr_b32 s64, %15, 3\n\t"
      "s_lshl_b32 s64, s64, 2\n\t"
      "s_add_u32 s78, s64, %14\n\t"
      "s_lshr_b32 s66, %17, 2\n\t"
      "s_lshl_b32 s64, s66, 7\n\t"
      "s_add_u32 s64, s64, s78\n\t"
      "s_mul_i32 s65, s64, 6144\n\t"
      "s_lshl_b32 s61, %19, 9\n\t"
      "s_add_u32 s65, s65, s61\n\t"
      "v_lshrrev_b32 v24, 2, v0\n\t"
      "v_and_b32 v25, 3, v0\n\t"
      "v_lshlrev_b32 v24, 11, v24\n\t"
      "v_lshl_add_u32 v24, v25, 7, v24\n\t"
      "v_add_u32 v24, s65, v24\n\t"
      "v_mov_b32 v28, %2\n\t"
      "v_mov_b32 v29, %3\n\t"
      "v_mov_b32 v30, 1572864\n\t"
      "v_lshlrev_b32 v26, 7, v0\n\t"
      "s_mul_i32 s65, s64, 7168\n\t"
      "s_mul_i32 s61, %19, 1792\n\t"
      "s_add_u32 s65, s65, s61\n\t"
      "v_cmp_le_u32 vcc, 12, v0\n\t"
      "s_sub_u32 s65, s65, 1536\n\t"
      "v_add_u32 v25, s65, v26\n\t"
      "v_cndmask_b32 v24, v24, v25, vcc\n\t"
      "v_mov_b32 v25, %4\n\t"
      "v_cndmask_b32 v28, v28, v25, vcc\n\t"
      "v_mov_b32 v25, %5\n\t"
      "v_cndmask_b32 v29, v29, v25, vcc\n\t"
      "v_mov_b32 v25, 1835008\n\t"
      "v_cndmask_b32 v30, v30, v25, vcc\n\t"
      "s_lshl_b32 s64, %19, 1\n\t"
      "s_add_u32 s64, s64, s66\n\t"
      "s_mul_i32 s65, s64, 917504\n\t"
      "s_mul_i32 s61, s78, 1792\n\t"
      "s_add_u32 s65, s65, s61\n\t"
      "v_cmp_le_u32 vcc, 26, v0\n\t"
      "s_sub_u32 s65, s65, 3328\n\t"
      "v_add_u32 v25, s65, v26\n\t"
      "v_cndmask_b32 v24, v24, v25, vcc\n\t"
      "v_mov_b32 v25, %6\n\t"
      "v_cndmask_b32 v28, v28, v25, vcc\n\t"
      "v_mov_b32 v25, %7\n\t"
      "v_cndmask_b32 v29, v29, v25, vcc\n\t"
      "v_mov_b32 v25, 229376\n\t"
      "v_cndmask_b32 v30, v30, v25, vcc\n\t"
      "s_lshl_b32 s65, s64, 18\n\t"
      "s_lshl_b32 s61, s78, 9\n\t"
      "s_add_u32 s65, s65, s61\n\t"
      "v_cmp_le_u32 vcc, 40, v0\n\t"
      "s_sub_u32 s65, s65, 5120\n\t"
      "v_add_u32 v25, s65, v26\n\t"
      "v_cndmask_b32 v24, v24, v25, vcc\n\t"
      "v_mov_b32 v25, %8\n\t"
      "v_cndmask_b32 v28, v28, v25, vcc\n\t"
      "v_mov_b32 v25, %9\n\t"
      "v_cndmask_b32 v29, v29, v25, vcc\n\t"
      "v_mov_b32 v25, 65536\n\t"
      "v_cndmask_b32 v30, v30, v25, vcc\n\t"
      "v_add_co_u32 v28, vcc, v28, v24\n\t"
      "v_addc_co_u32 v29, vcc, 0, v29, vcc\n\t"
      "s_mov_b32 s57, 4095\n\t"
      "v_mov_b32 v25, 0\n\t"
      "s_mov_b64 exec, 0x3ffffff\n\t"
      "global_load_dword v31, v[28:29], off\n\t"
      "v_add_co_u32 v28, vcc, v28, v30\n\t"
      "v_addc_co_u32 v29, vcc, 0, v29, vcc\n\t"
      "s_mov_b64 exec, -1\n\t"
      "s_lshl_b32 s64, %14, 1\n\t"
      "v_add_u32 v41, s64, v41\n\t"
      "v_and_b32 v43, 7, v0\n\t"
      "v_xor_b32 v41, v41, v43\n\t"
      "v_lshlrev_b32 v41, 4, v41\n\t"
      "v_lshl_add_u32 v41, v40, 7, v41\n\t"
      "v_add_u32 v41, %16, v41\n\t"
      "v_lshrrev_b32 v40, 3, v0\n\t"
      "v_xor_b32 v42, v40, v43\n\t"
      "v_lshlrev_b32 v42, 4, v42\n\t"
      "v_lshl_add_u32 v42, v40, 7, v42\n\t"
      "s_lshl_b32 s64, %14, 12\n\t"
      "s_add_u32 s64, s64, %16\n\t"
      "v_add_u32 v42, s64, v42\n\t"
      "v_lshrrev_b32 v44, 2, v40\n\t"
      "v_and_b32 v40, 3, v40\n\t"
      "v_lshlrev_b32 v40, 8, v40\n\t"
      "v_lshl_add_u32 v40, v44, 12, v40\n\t"
      "v_lshl_add_u32 v40, v43, 4, v40\n\t"
      "s_lshr_b32 s64, %24, 3\n\t"
      "s_lshl_b32 s64, s64, 17\n\t"
      "s_and_b32 s65, %24, 6\n\t"
      "s_lshl_b32 s65, s65, 9\n\t"
      "s_add_u32 s64, s64, s65\n\t"
      "s_and_b32 s65, %24, 1\n\t"
      "s_lshl_b32 s65, s65, 7\n\t"
      "s_add_u32 s64, s64, s65\n\t"
      "s_lshl_b32 s65, %14, 15\n\t"
      "s_add_u32 s64, s64, s65\n\t"
      "s_waitcnt vmcnt(5)\n\t"
      "v_lshlrev_b32 v64, 16, v48\n\t"
      "v_and_b32 v65, 0xffff0000, v48\n\t"
      "v_lshlrev_b32 v66, 16, v49\n\t"
      "v_and_b32 v67, 0xffff0000, v49\n\t"
      "v_lshlrev_b32 v68, 16, v50\n\t"
      "v_and_b32 v69, 0xffff0000, v50\n\t"
      "v_lshlrev_b32 v70, 16, v51\n\t"
      "v_and_b32 v71, 0xffff0000, v51\n\t"
      "s_waitcnt vmcnt(4)\n\t"
      "v_lshlrev_b32 v72, 16, v52\n\t"
      "v_and_b32 v73, 0xffff0000, v52\n\t"
      "v_lshlrev_b32 v74, 16, v53\n\t"
      "v_and_b32 v75, 0xffff0000, v53\n\t"
      "v_lshlrev_b32 v76, 16, v54\n\t"
      "v_and_b32 v77, 0xffff0000, v54\n\t"
      "v_lshlrev_b32 v78, 16, v55\n\t"
      "v_and_b32 v79, 0xffff0000, v55\n\t"
      "s_waitcnt vmcnt(3)\n\t"
      "v_lshlrev_b32 v80, 16, v56\n\t"
      "v_and_b32 v81, 0xffff0000, v56\n\t"
      "v_lshlrev_b32 v82, 16, v57\n\t"
      "v_and_b32 v83, 0xffff0000, v57\n\t"
      "v_lshlrev_b32 v84, 16, v58\n\t"
      "v_and_b32 v85, 0xffff0000, v58\n\t"
      "v_lshlrev_b32 v86, 16, v59\n\t"
      "v_and_b32 v87, 0xffff0000, v59\n\t"
      "s_waitcnt vmcnt(2)\n\t"
      "v_lshlrev_b32 v88, 16, v60\n\t"
      "v_and_b32 v89, 0xffff0000, v60\n\t"
      "v_lshlrev_b32 v90, 16, v61\n\t"
      "v_and_b32 v91, 0xffff0000, v61\n\t"
      "v_lshlrev_b32 v92, 16, v62\n\t"
      "v_and_b32 v93, 0xffff0000, v62\n\t"
      "v_lshlrev_b32 v94, 16, v63\n\t"
      "v_and_b32 v95, 0xffff0000, v63\n\t"
      "v_readfirstlane_b32 s58, v46\n\t"
      "v_readfirstlane_b32 s59, v47\n\t"
      "v_pk_add_f32 v[32:33], v[64:65], v[66:67]\n\t"
      "v_pk_add_f32 v[34:35], v[68:69], v[70:71]\n\t"
      "s_add_u32 s58, s58, 0x7f4a7c15\n\t"
      "s_addc_u32 s59, s59, 0x9e3779b9\n\t"
      "v_pk_add_f32 v[32:33], v[32:33], v[72:73]\n\t"
      "v_pk_add_f32 v[34:35], v[34:35], v[74:75]\n\t"
      "v_pk_add_f32 v[32:33], v[32:33], v[76:77]\n\t"
      "v_pk_add_f32 v[34:35], v[34:35], v[78:79]\n\t"
      "v_pk_add_f32 v[32:33], v[32:33], v[80:81]\n\t"
      "v_pk_add_f32 v[34:35], v[34:35], v[82:83]\n\t"
      "v_pk_add_f32 v[32:33], v[32:33], v[84:85]\n\t"
      "v_pk_add_f32 v[34:35], v[34:35], v[86:87]\n\t"
      "v_pk_add_f32 v[32:33], v[32:33], v[88:89]\n\t"
      "v_pk_add_f32 v[34:35], v[34:35], v[90:91]\n\t"
      "v_pk_add_f32 v[32:33], v[32:33], v[92:93]\n\t"
      "v_pk_add_f32 v[34:35], v[34:35], v[94:95]\n\t"
      "v_pk_add_f32 v[32:33], v[32:33], v[34:35]\n\t"
      "v_add_f32 v32, v32, v33\n\t"
      "s_nop 1\n\t"
      "v_add_f32_dpp v32, v32, v32 quad_perm:[1,0,3,2] row_mask:0xf bank_mask:0xf\n\t"
      "s_nop 1\n\t"
      "v_add_f32_dpp v32, v32, v32 quad_perm:[2,3,0,1] row_mask:0xf bank_mask:0xf\n\t"
      "s_nop 1\n\t"
      "v_add_f32_dpp v32, v32, v32 row_half_mirror row_mask:0xf bank_mask:0xf\n\t"
      "s_nop 1\n\t"
      "v_add_f32_dpp v32, v32, v32 row_mirror row_mask:0xf bank_mask:0xf\n\t"
      "v_mov_b32 v33, v32\n\t"
      "s_nop 1\n\t"
      "v_permlane16_swap_b32 v32, v33\n\t"
      "v_add_f32 v32, v32, v33\n\t"
      "v_mul_f32 v32, 0xba800000, v32\n\t"
      "v_pk_add_f32 v[64:65], v[64:65], v[32:33] op_sel_hi:[1,0]\n\t"
      "v_pk_add_f32 v[66:67], v[66:67], v[32:33] op_sel_hi:[1,0]\n\t"
      "v_pk_add_f32 v[68:69], v[68:69], v[32:33] op_sel_hi:[1,0]\n\t"
      "v_pk_add_f32 v[70:71], v[70:71], v[32:33] op_sel_hi:[1,0]\n\t"
      "v_pk_add_f32 v[72:73], v[72:73], v[32:33] op_sel_hi:[1,0]\n\t"
      "v_pk_add_f32 v[74:75], v[74:75], v[32:33] op_sel_hi:[1,0]\n\t"
      "v_pk_add_f32 v[76:77], v[76:77], v[32:33] op_sel_hi:[1,0]\n\t"
      "v_pk_add_f32 v[78:79], v[78:79], v[32:33] op_sel_hi:[1,0]\n\t"
      "v_pk_add_f32 v[80:81], v[80:81], v[32:33] op_sel_hi:[1,0]\n\t"
      "v_pk_add_f32 v[82:83], v[82:83], v[32:33] op_sel_hi:[1,0]\n\t"
      "v_pk_add_f32 v[84:85], v[84:85], v[32:33] op_sel_hi:[1,0]\n\t"
      "v_pk_add_f32 v[86:87], v[86:87], v[32:33] op_sel_hi:[1,0]\n\t"
      "v_pk_add_f32 v[88:89], v[88:89], v[32:33] op_sel_hi:[1,0]\n\t"
      "v_pk_add_f32 v[90:91], v[90:91], v[32:33] op_sel_hi:[1,0]\n\t"
      "v_pk_add_f32 v[92:93], v[92:93], v[32:33] op_sel_hi:[1,0]\n\t"
      "v_pk_add_f32 v[94:95], v[94:95], v[32:33] op_sel_hi:[1,0]\n\t"
      "v_pk_mul_f32 v[32:33], v[64:65], v[64:65]\n\t"
      "v_pk_mul_f32 v[34:35], v[66:67], v[66:67]\n\t"
      "v_pk_fma_f32 v[32:33], v[68:69], v[68:69], v[32:33]\n\t"
      "v_pk_fma_f32 v[34:35], v[70:71], v[70:71], v[34:35]\n\t"
      "v_pk_fma_f32 v[32:33], v[72:73], v[72:73], v[32:33]\n\t"
      "v_pk_fma_f32 v[34:35], v[74:75], v[74:75], v[34:35]\n\t"
      "v_pk_fma_f32 v[32:33], v[76:77], v[76:77], v[32:33]\n\t"
      "v_pk_fma_f32 v[34:35], v[78:79], v[78:79], v[34:35]\n\t"
      "v_pk_fma_f32 v[32:33], v[80:81], v[80:81], v[32:33]\n\t"
      "v_pk_fma_f32 v[34:35], v[82:83], v[82:83], v[34:35]\n\t"
      "v_pk_fma_f32 v[32:33], v[84:85], v[84:85], v[32:33]\n\t"
      "v_pk_fma_f32 v[34:35], v[86:87], v[86:87], v[34:35]\n\t"
      "v_pk_fma_f32 v[32:33], v[88:89], v[88:89], v[32:33]\n\t"
      "v_pk_fma_f32 v[34:35], v[90:91], v[90:91], v[34:35]\n\t"
      "v_pk_fma_f32 v[32:33], v[92:93], v[92:93], v[32:33]\n\t"
      "v_pk_fma_f32 v[34:35], v[94:95], v[94:95], v[34:35]\n\t"
      "v_pk_add_f32 v[32:33], v[32:33], v[34:35]\n\t"
      "v_add_f32 v32, v32, v33\n\t"
      "s_nop 1\n\t"
      "v_add_f32_dpp v32, v32, v32 quad_perm:[1,0,3,2] row_mask:0xf bank_mask:0xf\n\t"
      "s_nop 1\n\t"
      "v_add_f32_dpp v32, v32, v32 quad_perm:[2,3,0,1] row_mask:0xf bank_mask:0xf\n\t"
      "s_nop 1\n\t"
      "v_add_f32_dpp v32, v32, v32 row_half_mirror row_mask:0xf bank_mask:0xf\n\t"
      "s_nop 1\n\t"
      "v_add_f32_dpp v32, v32, v32 row_mirror row_mask:0xf bank_mask:0xf\n\t"
      "v_mov_b32 v33, v32\n\t"
      "s_nop 1\n\t"
      "v_permlane16_swap_b32 v32, v33\n\t"
      "v_add_f32 v32, v32, v33\n\t"
      "v_mov_b32 v33, 0x3727c5ac\n\t"
      "v_fmac_f32 v33, 0x3a800000, v32\n\t"
      "v_rsq_f32 v32, v33\n\t"
      "s_nop 0\n\t"
      "v_pk_mul_f32 v[64:65], v[64:65], v[32:33] op_sel_hi:[1,0]\n\t"
      "v_pk_mul_f32 v[66:67], v[66:67], v[32:33] op_sel_hi:[1,0]\n\t"
      "v_pk_mul_f32 v[68:69], v[68:69], v[32:33] op_sel_hi:[1,0]\n\t"
      "v_pk_mul_f32 v[70:71], v[70:71], v[32:33] op_sel_hi:[1,0]\n\t"
      "v_cvt_pk_bf16_f32 v48, v64, v65\n\t"
      "v_cvt_pk_bf16_f32 v49, v66, v67\n\t"
      "v_cvt_pk_bf16_f32 v50, v68, v69\n\t"
      "v_cvt_pk_bf16_f32 v51, v70, v71\n\t"
      "ds_write_b128 v41, v[48:51] offset:0\n\t"
      "v_pk_mul_f32 v[72:73], v[72:73], v[32:33] op_sel_hi:[1,0]\n\t"
      "v_pk_mul_f32 v[74:75], v[74:75], v[32:33] op_sel_hi:[1,0]\n\t"
      "v_pk_mul_f32 v[76:77], v[76:77], v[32:33] op_sel_hi:[1,0]\n\t"
      "v_pk_mul_f32 v[78:79], v[78:79], v[32:33] op_sel_hi:[1,0]\n\t"
      "v_cvt_pk_bf16_f32 v52, v72, v73\n\t"
      "v_cvt_pk_bf16_f32 v53, v74, v75\n\t"
      "v_cvt_pk_bf16_f32 v54, v76, v77\n\t"
      "v_cvt_pk_bf16_f32 v55, v78, v79\n\t"
      "ds_write_b128 v41, v[52:55] offset:4096\n\t"
      "v_pk_mul_f32 v[80:81], v[80:81], v[32:33] op_sel_hi:[1,0]\n\t"
      "v_pk_mul_f32 v[82:83], v[82:83], v[32:33] op_sel_hi:[1,0]\n\t"
      "v_pk_mul_f32 v[84:85], v[84:85], v[32:33] op_sel_hi:[1,0]\n\t"
      "v_pk_mul_f32 v[86:87], v[86:87], v[32:33] op_sel_hi:[1,0]\n\t"
      "v_cvt_pk_bf16_f32 v56, v80, v81\n\t"
      "v_cvt_pk_bf16_f32 v57, v82, v83\n\t"
      "v_cvt_pk_bf16_f32 v58, v84, v85\n\t"
      "v_cvt_pk_bf16_f32 v59, v86, v87\n\t"
      "ds_write_b128 v41, v[56:59] offset:8192\n\t"
      "v_pk_mul_f32 v[88:89], v[88:89], v[32:33] op_sel_hi:[1,0]\n\t"
      "v_pk_mul_f32 v[90:91], v[90:91], v[32:33] op_sel_hi:[1,0]\n\t"
      "v_pk_mul_f32 v[92:93], v[92:93], v[32:33] op_sel_hi:[1,0]\n\t"
      "v_pk_mul_f32 v[94:95], v[94:95], v[32:33] op_sel_hi:[1,0]\n\t"
      "v_cvt_pk_bf16_f32 v60, v88, v89\n\t"
      "v_cvt_pk_bf16_f32 v61, v90, v91\n\t"
      "v_cvt_pk_bf16_f32 v62, v92, v93\n\t"
      "v_cvt_pk_bf16_f32 v63, v94, v95\n\t"
      "ds_write_b128 v41, v[60:63] offset:12288\n\t"
      "s_waitcnt lgkmcnt(0)\n\t"
      "s_barrier\n\t"
      "ds_read_b128 v[64:67], v42 offset:0\n\t"
      "ds_read_b128 v[68:71], v42 offset:1024\n\t"
      "ds_read_b128 v[72:75], v42 offset:2048\n\t"
      "ds_read_b128 v[76:79], v42 offset:3072\n\t"
      "v_mov_b32 v32, s58\n\t"
      "v_accvgpr_write_b32 a252, v32\n\t"
      "v_accvgpr_write_b32 a254, v32\n\t"
      "v_mov_b32 v32, s59\n\t"
      "v_accvgpr_write_b32 a253, v32\n\t"
      "v_accvgpr_write_b32 a255, v32\n\t"
      "s_waitcnt lgkmcnt(3)\n\t"
      "buffer_store_dwordx4 v[64:67], v40, s[44:47], s64 offen sc1\n\t"
      "s_add_u32 s64, s64, 8192\n\t"
      "s_waitcnt lgkmcnt(2)\n\t"
      "buffer_store_dwordx4 v[68:71], v40, s[44:47], s64 offen sc1\n\t"
      "s_add_u32 s64, s64, 8192\n\t"
      "s_waitcnt lgkmcnt(1)\n\t"
      "buffer_store_dwordx4 v[72:75], v40, s[44:47], s64 offen sc1\n\t"
      "s_add_u32 s64, s64, 8192\n\t"
      "s_waitcnt lgkmcnt(0)\n\t"
      "buffer_store_dwordx4 v[76:79], v40, s[44:47], s64 offen sc1\n\t"
      "s_add_u32 s64, s64, 8192\n\t"
      "s_lshl_b32 s64, %24, 2\n\t"
      "s_add_u32 s64, s64, %14\n\t"
      "s_lshl_b32 s64, s64, 7\n\t"
      "s_add_u32 s64, s64, 52822016\n\t"
      "s_lshl_b32 s65, %17, 2\n\t"
      "s_add_u32 s65, s65, %14\n\t"
      "s_lshl_b32 s65, s65, 12\n\t"
      "s_add_u32 s65, s65, 52822016\n\t"
      "v_and_b32 v24, 31, v0\n\t"
      "v_lshlrev_b32 v24, 7, v24\n\t"
      "s_waitcnt vmcnt(0)\n\t"
      "s_mov_b64 exec, 0xff\n\t"
      "buffer_store_dwordx4 a[252:255], v1, s[44:47], s64 offen sc1\n\t"
      "s_mov_b64 exec, -1\n\t"
      "s_add_u32 m0, s62, 135168\n\t"
      "s_nop 0\n\t"
      "global_load_lds_dwordx4 v2, s[40:41]\n\t"
      "s_add_u32 m0, s62, 136192\n\t"
      "s_nop 0\n\t"
      "global_load_lds_dwordx4 v3, s[40:41]\n\t"
      "s_add_u32 s40, s40, 393216\n\t"
      "s_addc_u32 s41, s41, 0\n\t"
      "s_add_u32 m0, s62, 49152\n\t"
      "s_nop 0\n\t"
      "global_load_lds_dwordx4 v2, s[40:41]\n\t"
      "s_add_u32 m0, s62, 50176\n\t"
      "s_nop 0\n\t"
      "global_load_lds_dwordx4 v3, s[40:41]\n\t"
      "s_add_u32 m0, s63, 57344\n\t"
      "s_nop 0\n\t"
      "global_load_lds_dwordx4 v4, s[42:43]\n\t"
      "s_add_u32 m0, s62, 61440\n\t"
      "s_nop 0\n\t"
      "global_load_lds_dwordx4 v5, s[52:53]\n\t"
      "s_add_u32 m0, s62, 62464\n\t"
      "s_nop 0\n\t"
      "global_load_lds_dwordx4 v6, s[52:53]\n\t"
      "s_add_u32 m0, s63, 69632\n\t"
      "s_nop 0\n\t"
      "global_load_lds_dwordx4 v7, s[52:53]\n\t"
      "s_add_u32 m0, s63, 73728\n\t"
      "s_nop 0\n\t"
      "global_load_lds_dwordx4 v8, s[52:53]\n\t"
      "v_lshrrev_b32 v32, 4, v0\n\t"
      "v_bfe_u32 v33, v0, 2, 2\n\t"
      "v_and_b32 v34, 3, v0\n\t"
      "v_lshl_add_u32 v35, v32, 3, v33\n\t"
      "s_add_u32 s64, %16, 49152\n\t"
      "v_lshl_add_u32 v34, v34, 3, s64\n\t"
      "v_and_b32 v36, 1, v32\n\t"
      "v_lshrrev_b32 v37, 1, v33\n\t"
      "v_lshl_or_b32 v37, v36, 1, v37\n\t"
      "v_xor_b32 v9, 0, v37\n\t"
      "v_lshlrev_b32 v9, 5, v9\n\t"
      "v_lshl_add_u32 v9, v35, 7, v9\n\t"
      "v_add_u32 v10, v9, v34\n\t"
      "v_xor_b32 v9, 1, v37\n\t"
      "v_lshlrev_b32 v9, 5, v9\n\t"
      "v_lshl_add_u32 v9, v35, 7, v9\n\t"
      "v_add_u32 v11, v9, v34\n\t"
      "v_xor_b32 v9, 2, v37\n\t"
      "v_lshlrev_b32 v9, 5, v9\n\t"
      "v_lshl_add_u32 v9, v35, 7, v9\n\t"
      "v_add_u32 v12, v9, v34\n\t"
      "v_xor_b32 v9, 3, v37\n\t"
      "v_lshlrev_b32 v9, 5, v9\n\t"
      "v_lshl_add_u32 v9, v35, 7, v9\n\t"
      "v_add_u32 v13, v9, v34\n\t"
      "v_xor_b32 v9, 0, v36\n\t"
      "v_lshlrev_b32 v9, 5, v9\n\t"
      "v_lshl_add_u32 v9, v35, 6, v9\n\t"
      "v_add_u32 v14, v9, v34\n\t"
      "v_xor_b32 v9, 1, v36\n\t"
      "v_lshlrev_b32 v9, 5, v9\n\t"
      "v_lshl_add_u32 v9, v35, 6, v9\n\t"
      "v_add_u32 v15, v9, v34\n\t"
      "v_lshl_add_u32 v9, v36, 2, v33\n\t"
      "v_lshl_add_u32 v9, v9, 5, v34\n\t"
      "v_lshrrev_b32 v32, 1, v32\n\t"
      "v_lshl_add_u32 v16, v32, 10, v9\n\t"
      "v_add_u32 v17, 57344, v10\n\t"
      "v_add_u32 v18, 57344, v11\n\t"
      "v_add_u32 v19, 57344, v12\n\t"
      "v_add_u32 v20, 57344, v13\n\t"
      "v_add_u32 v21, 57344, v14\n\t"
      "v_add_u32 v22, 57344, v15\n\t"
      "v_add_u32 v23, 57344, v16\n\t"
      "s_sleep 24\n\t"
      "buffer_load_dwordx2 v[26:27], v24, s[44:47], s65 offen sc1\n\t"
      "s_waitcnt vmcnt(0)\n\t"
      "v_cmp_eq_u64 vcc, s[58:59], v[26:27]\n\t"
      "s_nop 1\n\t"
      "s_cmp_eq_u64 vcc, -1\n\t"
      "s_cbranch_scc1 .Lkm_x_ok_%=\n\t"
      "s_mov_b32 s67, 0\n\t"
      ".Lkm_x_spin_%=:\n\t"
      "s_sleep 1\n\t"
      "buffer_load_dwordx2 v[26:27], v24, s[44:47], s65 offen sc1\n\t"
      "s_add_u32 s67, s67, 1\n\t"
      "s_cmp_ge_u32 s67, 262144\n\t"
      "s_cbranch_scc1 .Lkm_x_ok_%=\n\t"
      "s_waitcnt vmcnt(0)\n\t"
      "v_cmp_eq_u64 vcc, s[58:59], v[26:27]\n\t"
      "s_nop 1\n\t"
      "s_cmp_eq_u64 vcc, -1\n\t"
      "s_cbranch_scc0 .Lkm_x_spin_%=\n\t"
      ".Lkm_x_ok_%=:\n\t"
      "global_load_dwordx4 a[32:35], v1, s[36:37] offset:-4096 sc1\n\t"
      "global_load_dwordx4 a[36:39], v1, s[36:37] offset:-3072 sc1\n\t"
      "global_load_dwordx4 a[40:43], v1, s[36:37] offset:-2048 sc1\n\t"
      "global_load_dwordx4 a[44:47], v1, s[36:37] offset:-1024 sc1\n\t"
      "global_load_dwordx4 a[48:51], v1, s[36:37] offset:0 sc1\n\t"
      "global_load_dwordx4 a[52:55], v1, s[36:37] offset:1024 sc1\n\t"
      "global_load_dwordx4 a[56:59], v1, s[36:37] offset:2048 sc1\n\t"
      "global_load_dwordx4 a[60:63], v1, s[36:37] offset:3072 sc1\n\t"
      "s_add_u32 s36, s36, 8192\n\t"
      "s_addc_u32 s37, s37, 0\n\t"
      "global_load_dwordx4 a[64:67], v1, s[36:37] offset:-4096 sc1\n\t"
      "global_load_dwordx4 a[68:71], v1, s[36:37] offset:-3072 sc1\n\t"
      "global_load_dwordx4 a[72:75], v1, s[36:37] offset:-2048 sc1\n\t"
      "global_load_dwordx4 a[76:79], v1, s[36:37] offset:-1024 sc1\n\t"
      "global_load_dwordx4 a[80:83], v1, s[36:37] offset:0 sc1\n\t"
      "global_load_dwordx4 a[84:87], v1, s[36:37] offset:1024 sc1\n\t"
      "global_load_dwordx4 a[88:91], v1, s[36:37] offset:2048 sc1\n\t"
      "global_load_dwordx4 a[92:95], v1, s[36:37] offset:3072 sc1\n\t"
      "s_waitcnt vmcnt(8)\n\t"
      "s_barrier\n\t"
      "ds_read_b64_tr_b16 a[128:129], v17 offset:28672\n\t"
      "ds_read_b64_tr_b16 a[130:131], v17 offset:29184\n\t"
      "ds_read_b64_tr_b16 a[132:133], v17 offset:32768\n\t"
      "ds_read_b64_tr_b16 a[134:135], v17 offset:33280\n\t"
      "ds_read_b64_tr_b16 a[136:137], v18 offset:28672\n\t"
      "ds_read_b64_tr_b16 a[138:139], v18 offset:29184\n\t"
      "s_add_u32 s36, s36, 8192\n\t"
      "s_addc_u32 s37, s37, 0\n\t"
      "s_add_u32 s40, s40, 393216\n\t"
      "s_addc_u32 s41, s41, 0\n\t"
      "s_add_u32 s42, s42, 393216\n\t"
      "s_addc_u32 s43, s43, 0\n\t"
      "s_add_u32 s52, s52, 458752\n\t"
      "s_addc_u32 s53, s53, 0\n\t"
      "s_waitcnt lgkmcnt(4)\n\t"
      "v_mfma_f32_16x16x32_bf16 v[48:51], a[128:131], a[32:35], 0\n\t"
      "ds_read_b64_tr_b16 a[140:141], v18 offset:32768\n\t"
      "v_mfma_f32_16x16x32_bf16 v[52:55], a[128:131], a[36:39], 0\n\t"
      "ds_read_b64_tr_b16 a[142:143], v18 offset:33280\n\t"
      "s_add_u32 m0, s62, 77824\n\t"
      "v_mfma_f32_16x16x32_bf16 v[56:59], a[128:131], a[40:43], 0\n\t"
      "global_load_lds_dwordx4 v2, s[40:41]\n\t"
      "v_mfma_f32_16x16x32_bf16 v[60:63], a[128:131], a[44:47], 0\n\t"
      "s_waitcnt lgkmcnt(4)\n\t"
      "v_mfma_f32_16x16x32_bf16 v[48:51], a[132:135], a[48:51], v[48:51]\n\t"
      "ds_read_b64_tr_b16 a[128:129], v19 offset:28672\n\t"
      "v_mfma_f32_16x16x32_bf16 v[52:55], a[132:135], a[52:55], v[52:55]\n\t"
      "ds_read_b64_tr_b16 a[130:131], v19 offset:29184\n\t"
      "v_mfma_f32_16x16x32_bf16 v[56:59], a[132:135], a[56:59], v[56:59]\n\t"
      "global_load_dwordx4 a[96:99], v1, s[36:37] offset:-4096 sc1\n\t"
      "v_mfma_f32_16x16x32_bf16 v[60:63], a[132:135], a[60:63], v[60:63]\n\t"
      "s_waitcnt lgkmcnt(4)\n\t"
      "v_mfma_f32_16x16x32_bf16 v[64:67], a[136:139], a[32:35], 0\n\t"
      "ds_read_b64_tr_b16 a[132:133], v19 offset:32768\n\t"
      "v_mfma_f32_16x16x32_bf16 v[68:71], a[136:139], a[36:39], 0\n\t"
      "ds_read_b64_tr_b16 a[134:135], v19 offset:33280\n\t"
      "v_mfma_f32_16x16x32_bf16 v[72:75], a[136:139], a[40:43], 0\n\t"
      "global_load_dwordx4 a[100:103], v1, s[36:37] offset:-3072 sc1\n\t"
      "v_mfma_f32_16x16x32_bf16 v[76:79], a[136:139], a[44:47], 0\n\t"
      "s_waitcnt lgkmcnt(4)\n\t"
      "v_mfma_f32_16x16x32_bf16 v[64:67], a[140:143], a[48:51], v[64:67]\n\t"
      "ds_read_b64_tr_b16 a[136:137], v20 offset:28672\n\t"
      "v_mfma_f32_16x16x32_bf16 v[68:71], a[140:143], a[52:55], v[68:71]\n\t"
      "ds_read_b64_tr_b16 a[138:139], v20 offset:29184\n\t"
      "v_mfma_f32_16x16x32_bf16 v[72:75], a[140:143], a[56:59], v[72:75]\n\t"
      "global_load_dwordx4 a[104:107], v1, s[36:37] offset:-2048 sc1\n\t"
      "v_mfma_f32_16x16x32_bf16 v[76:79], a[140:143], a[60:63], v[76:79]\n\t"
      "s_waitcnt lgkmcnt(4)\n\t"
      "v_mfma_f32_16x16x32_bf16 v[80:83], a[128:131], a[32:35], 0\n\t"
      "ds_read_b64_tr_b16 a[140:141], v20 offset:32768\n\t"
      "v_mfma_f32_16x16x32_bf16 v[84:87], a[128:131], a[36:39], 0\n\t"
      "ds_read_b64_tr_b16 a[142:143], v20 offset:33280\n\t"
      "v_mfma_f32_16x16x32_bf16 v[88:91], a[128:131], a[40:43], 0\n\t"
      "global_load_dwordx4 a[108:111], v1, s[36:37] offset:-1024 sc1\n\t"
      "v_mfma_f32_16x16x32_bf16 v[92:95], a[128:131], a[44:47], 0\n\t"
      "s_waitcnt lgkmcnt(4)\n\t"
      "v_mfma_f32_16x16x32_bf16 v[80:83], a[132:135], a[48:51], v[80:83]\n\t"
      "ds_read_b64_tr_b16 a[128:129], v10 offset:0\n\t"
      "v_mfma_f32_16x16x32_bf16 v[84:87], a[132:135], a[52:55], v[84:87]\n\t"
      "ds_read_b64_tr_b16 a[130:131], v10 offset:512\n\t"
      "s_add_u32 m0, s62, 78848\n\t"
      "v_mfma_f32_16x16x32_bf16 v[88:91], a[132:135], a[56:59], v[88:91]\n\t"
      "global_load_lds_dwordx4 v3, s[40:41]\n\t"
      "v_mfma_f32_16x16x32_bf16 v[92:95], a[132:135], a[60:63], v[92:95]\n\t"
      "s_waitcnt lgkmcnt(4)\n\t"
      "v_mfma_f32_16x16x32_bf16 v[96:99], a[136:139], a[32:35], 0\n\t"
      "ds_read_b64_tr_b16 a[132:133], v10 offset:4096\n\t"
      "v_mfma_f32_16x16x32_bf16 v[100:103], a[136:139], a[36:39], 0\n\t"
      "ds_read_b64_tr_b16 a[134:135], v10 offset:4608\n\t"
      "v_mfma_f32_16x16x32_bf16 v[104:107], a[136:139], a[40:43], 0\n\t"
      "global_load_dwordx4 a[112:115], v1, s[36:37] offset:0 sc1\n\t"
      "v_mfma_f32_16x16x32_bf16 v[108:111], a[136:139], a[44:47], 0\n\t"
      "s_waitcnt lgkmcnt(4)\n\t"
      "v_mfma_f32_16x16x32_bf16 v[96:99], a[140:143], a[48:51], v[96:99]\n\t"
      "ds_read_b64_tr_b16 a[136:137], v11 offset:0\n\t"
      "v_mfma_f32_16x16x32_bf16 v[100:103], a[140:143], a[52:55], v[100:103]\n\t"
      "ds_read_b64_tr_b16 a[138:139], v11 offset:512\n\t"
      "v_mfma_f32_16x16x32_bf16 v[104:107], a[140:143], a[56:59], v[104:107]\n\t"
      "global_load_dwordx4 a[116:119], v1, s[36:37] offset:1024 sc1\n\t"
      "v_mfma_f32_16x16x32_bf16 v[108:111], a[140:143], a[60:63], v[108:111]\n\t"
      "s_waitcnt vmcnt(8) lgkmcnt(4)\n\t"
      "v_mfma_f32_16x16x32_bf16 v[48:51], a[128:131], a[64:67], v[48:51]\n\t"
      "ds_read_b64_tr_b16 a[140:141], v11 offset:4096\n\t"
      "v_mfma_f32_16x16x32_bf16 v[52:55], a[128:131], a[68:71], v[52:55]\n\t"
      "ds_read_b64_tr_b16 a[142:143], v11 offset:4608\n\t"
      "v_mfma_f32_16x16x32_bf16 v[56:59], a[128:131], a[72:75], v[56:59]\n\t"
      "global_load_dwordx4 a[120:123], v1, s[36:37] offset:2048 sc1\n\t"
      "v_mfma_f32_16x16x32_bf16 v[60:63], a[128:131], a[76:79], v[60:63]\n\t"
      "s_waitcnt lgkmcnt(4)\n\t"
      "v_mfma_f32_16x16x32_bf16 v[48:51], a[132:135], a[80:83], v[48:51]\n\t"
      "ds_read_b64_tr_b16 a[128:129], v12 offset:0\n\t"
      "v_mfma_f32_16x16x32_bf16 v[52:55], a[132:135], a[84:87], v[52:55]\n\t"
      "ds_read_b64_tr_b16 a[130:131], v12 offset:512\n\t"
      "v_mfma_f32_16x16x32_bf16 v[56:59], a[132:135], a[88:91], v[56:59]\n\t"
      "global_load_dwordx4 a[124:127], v1, s[36:37] offset:3072 sc1\n\t"
      "v_mfma_f32_16x16x32_bf16 v[60:63], a[132:135], a[92:95], v[60:63]\n\t"
      "s_waitcnt lgkmcnt(4)\n\t"
      "v_mfma_f32_16x16x32_bf16 v[64:67], a[136:139], a[64:67], v[64:67]\n\t"
      "ds_read_b64_tr_b16 a[132:133], v12 offset:4096\n\t"
      "v_mfma_f32_16x16x32_bf16 v[68:71], a[136:139], a[68:71], v[68:71]\n\t"
      "ds_read_b64_tr_b16 a[134:135], v12 offset:4608\n\t"
      "s_add_u32 m0, s63, 86016\n\t"
      "v_mfma_f32_16x16x32_bf16 v[72:75], a[136:139], a[72:75], v[72:75]\n\t"
      "global_load_lds_dwordx4 v4, s[42:43]\n\t"
      "v_mfma_f32_16x16x32_bf16 v[76:79], a[136:139], a[76:79], v[76:79]\n\t"
      "s_waitcnt lgkmcnt(4)\n\t"
      "v_mfma_f32_16x16x32_bf16 v[64:67], a[140:143], a[80:83], v[64:67]\n\t"
      "ds_read_b64_tr_b16 a[136:137], v13 offset:0\n\t"
      "v_mfma_f32_16x16x32_bf16 v[68:71], a[140:143], a[84:87], v[68:71]\n\t"
      "ds_read_b64_tr_b16 a[138:139], v13 offset:512\n\t"
      "s_add_u32 m0, s62, 90112\n\t"
      "v_mfma_f32_16x16x32_bf16 v[72:75], a[140:143], a[88:91], v[72:75]\n\t"
      "global_load_lds_dwordx4 v5, s[52:53]\n\t"
      "v_mfma_f32_16x16x32_bf16 v[76:79], a[140:143], a[92:95], v[76:79]\n\t"
      "s_waitcnt lgkmcnt(4)\n\t"
      "v_mfma_f32_16x16x32_bf16 v[80:83], a[128:131], a[64:67], v[80:83]\n\t"
      "ds_read_b64_tr_b16 a[140:141], v13 offset:4096\n\t"
      "v_mfma_f32_16x16x32_bf16 v[84:87], a[128:131], a[68:71], v[84:87]\n\t"
      "ds_read_b64_tr_b16 a[142:143], v13 offset:4608\n\t"
      "s_add_u32 m0, s62, 91136\n\t"
      "v_mfma_f32_16x16x32_bf16 v[88:91], a[128:131], a[72:75], v[88:91]\n\t"
      "global_load_lds_dwordx4 v6, s[52:53]\n\t"
      "v_mfma_f32_16x16x32_bf16 v[92:95], a[128:131], a[76:79], v[92:95]\n\t"
      "s_waitcnt lgkmcnt(4)\n\t"
      "v_mfma_f32_16x16x32_bf16 v[80:83], a[132:135], a[80:83], v[80:83]\n\t"
      "ds_read_b64_tr_b16 a[128:129], v10 offset:12288\n\t"
      "v_mfma_f32_16x16x32_bf16 v[84:87], a[132:135], a[84:87], v[84:87]\n\t"
      "ds_read_b64_tr_b16 a[130:131], v10 offset:12800\n\t"
      "s_add_u32 m0, s63, 98304\n\t"
      "v_mfma_f32_16x16x32_bf16 v[88:91], a[132:135], a[88:91], v[88:91]\n\t"
      "global_load_lds_dwordx4 v7, s[52:53]\n\t"
      "v_mfma_f32_16x16x32_bf16 v[92:95], a[132:135], a[92:95], v[92:95]\n\t"
      "s_waitcnt lgkmcnt(4)\n\t"
      "v_mfma_f32_16x16x32_bf16 v[96:99], a[136:139], a[64:67], v[96:99]\n\t"
      "ds_read_b64_tr_b16 a[132:133], v10 offset:16384\n\t"
      "v_mfma_f32_16x16x32_bf16 v[100:103], a[136:139], a[68:71], v[100:103]\n\t"
      "ds_read_b64_tr_b16 a[134:135], v10 offset:16896\n\t"
      "s_add_u32 m0, s63, 102400\n\t"
      "v_mfma_f32_16x16x32_bf16 v[104:107], a[136:139], a[72:75], v[104:107]\n\t"
      "global_load_lds_dwordx4 v8, s[52:53]\n\t"
      "v_mfma_f32_16x16x32_bf16 v[108:111], a[136:139], a[76:79], v[108:111]\n\t"
      "s_add_u32 s36, s36, 8192\n\t"
      "s_addc_u32 s37, s37, 0\n\t"
      "s_waitcnt lgkmcnt(4)\n\t"
      "v_mfma_f32_16x16x32_bf16 v[96:99], a[140:143], a[80:83], v[96:99]\n\t"
      "ds_read_b64_tr_b16 a[136:137], v11 offset:12288\n\t"
      "s_add_u32 s40, s40, 393216\n\t"
      "s_addc_u32 s41, s41, 0\n\t"
      "v_mfma_f32_16x16x32_bf16 v[100:103], a[140:143], a[84:87], v[100:103]\n\t"
      "ds_read_b64_tr_b16 a[138:139], v11 offset:12800\n\t"
      "s_add_u32 s42, s42, 393216\n\t"
      "s_addc_u32 s43, s43, 0\n\t"
      "v_mfma_f32_16x16x32_bf16 v[104:107], a[140:143], a[88:91], v[104:107]\n\t"
      "s_add_u32 s52, s52, 458752\n\t"
      "s_addc_u32 s53, s53, 0\n\t"
      "v_mfma_f32_16x16x32_bf16 v[108:111], a[140:143], a[92:95], v[108:111]\n\t"
      "s_waitcnt lgkmcnt(4)\n\t"
      "v_mfma_f32_16x16x32_bf16 v[144:147], a[128:131], a[32:35], 0\n\t"
      "ds_read_b64_tr_b16 a[140:141], v11 offset:16384\n\t"
      "v_mfma_f32_16x16x32_bf16 v[148:151], a[128:131], a[36:39], 0\n\t"
      "ds_read_b64_tr_b16 a[142:143], v11 offset:16896\n\t"
      "s_add_u32 m0, s62, 106496\n\t"
      "v_mfma_f32_16x16x32_bf16 v[152:155], a[128:131], a[40:43], 0\n\t"
      "global_load_lds_dwordx4 v2, s[40:41]\n\t"
      "v_mfma_f32_16x16x32_bf16 v[156:159], a[128:131], a[44:47], 0\n\t"
      "s_waitcnt lgkmcnt(4)\n\t"
      "v_mfma_f32_16x16x32_bf16 v[144:147], a[132:135], a[48:51], v[144:147]\n\t"
      "ds_read_b64_tr_b16 a[128:129], v12 offset:12288\n\t"
      "v_mfma_f32_16x16x32_bf16 v[148:151], a[132:135], a[52:55], v[148:151]\n\t"
      "ds_read_b64_tr_b16 a[130:131], v12 offset:12800\n\t"
      "v_mfma_f32_16x16x32_bf16 v[152:155], a[132:135], a[56:59], v[152:155]\n\t"
      "global_load_dwordx4 a[0:3], v1, s[36:37] offset:-4096 sc1\n\t"
      "v_mfma_f32_16x16x32_bf16 v[156:159], a[132:135], a[60:63], v[156:159]\n\t"
      "s_waitcnt lgkmcnt(4)\n\t"
      "v_mfma_f32_16x16x32_bf16 v[160:163], a[136:139], a[32:35], 0\n\t"
      "ds_read_b64_tr_b16 a[132:133], v12 offset:16384\n\t"
      "v_mfma_f32_16x16x32_bf16 v[164:167], a[136:139], a[36:39], 0\n\t"
      "ds_read_b64_tr_b16 a[134:135], v12 offset:16896\n\t"
      "v_mfma_f32_16x16x32_bf16 v[168:171], a[136:139], a[40:43], 0\n\t"
      "global_load_dwordx4 a[4:7], v1, s[36:37] offset:-3072 sc1\n\t"
      "v_mfma_f32_16x16x32_bf16 v[172:175], a[136:139], a[44:47], 0\n\t"
      "s_waitcnt lgkmcnt(4)\n\t"
      "v_mfma_f32_16x16x32_bf16 v[160:163], a[140:143], a[48:51], v[160:163]\n\t"
      "ds_read_b64_tr_b16 a[136:137], v13 offset:12288\n\t"
      "v_mfma_f32_16x16x32_bf16 v[164:167], a[140:143], a[52:55], v[164:167]\n\t"
      "ds_read_b64_tr_b16 a[138:139], v13 offset:12800\n\t"
      "v_mfma_f32_16x16x32_bf16 v[168:171], a[140:143], a[56:59], v[168:171]\n\t"
      "global_load_dwordx4 a[8:11], v1, s[36:37] offset:-2048 sc1\n\t"
      "v_mfma_f32_16x16x32_bf16 v[172:175], a[140:143], a[60:63], v[172:175]\n\t"
      "s_waitcnt lgkmcnt(4)\n\t"
      "v_mfma_f32_16x16x32_bf16 v[176:179], a[128:131], a[32:35], 0\n\t"
      "ds_read_b64_tr_b16 a[140:141], v13 offset:16384\n\t"
      "v_mfma_f32_16x16x32_bf16 v[180:183], a[128:131], a[36:39], 0\n\t"
      "ds_read_b64_tr_b16 a[142:143], v13 offset:16896\n\t"
      "v_mfma_f32_16x16x32_bf16 v[184:187], a[128:131], a[40:43], 0\n\t"
      "global_load_dwordx4 a[12:15], v1, s[36:37] offset:-1024 sc1\n\t"
      "v_mfma_f32_16x16x32_bf16 v[188:191], a[128:131], a[44:47], 0\n\t"
      "s_waitcnt lgkmcnt(4)\n\t"
      "v_mfma_f32_16x16x32_bf16 v[176:179], a[132:135], a[48:51], v[176:179]\n\t"
      "ds_read_b64_tr_b16 a[128:129], v14 offset:20480\n\t"
      "v_mfma_f32_16x16x32_bf16 v[180:183], a[132:135], a[52:55], v[180:183]\n\t"
      "ds_read_b64_tr_b16 a[130:131], v14 offset:20736\n\t"
      "s_add_u32 m0, s62, 107520\n\t"
      "v_mfma_f32_16x16x32_bf16 v[184:187], a[132:135], a[56:59], v[184:187]\n\t"
      "global_load_lds_dwordx4 v3, s[40:41]\n\t"
      "v_mfma_f32_16x16x32_bf16 v[188:191], a[132:135], a[60:63], v[188:191]\n\t"
      "s_waitcnt lgkmcnt(4)\n\t"
      "v_mfma_f32_16x16x32_bf16 v[192:195], a[136:139], a[32:35], 0\n\t"
      "ds_read_b64_tr_b16 a[132:133], v14 offset:22528\n\t"
      "v_mfma_f32_16x16x32_bf16 v[196:199], a[136:139], a[36:39], 0\n\t"
      "ds_read_b64_tr_b16 a[134:135], v14 offset:22784\n\t"
      "v_mfma_f32_16x16x32_bf16 v[200:203], a[136:139], a[40:43], 0\n\t"
      "global_load_dwordx4 a[16:19], v1, s[36:37] offset:0 sc1\n\t"
      "v_mfma_f32_16x16x32_bf16 v[204:207], a[136:139], a[44:47], 0\n\t"
      "s_waitcnt lgkmcnt(4)\n\t"
      "v_mfma_f32_16x16x32_bf16 v[192:195], a[140:143], a[48:51], v[192:195]\n\t"
      "ds_read_b64_tr_b16 a[136:137], v15 offset:20480\n\t"
      "v_mfma_f32_16x16x32_bf16 v[196:199], a[140:143], a[52:55], v[196:199]\n\t"
      "ds_read_b64_tr_b16 a[138:139], v15 offset:20736\n\t"
      "v_mfma_f32_16x16x32_bf16 v[200:203], a[140:143], a[56:59], v[200:203]\n\t"
      "global_load_dwordx4 a[20:23], v1, s[36:37] offset:1024 sc1\n\t"
      "v_mfma_f32_16x16x32_bf16 v[204:207], a[140:143], a[60:63], v[204:207]\n\t"
      "s_waitcnt lgkmcnt(4)\n\t"
      "v_mfma_f32_16x16x32_bf16 v[208:211], a[128:131], a[32:35], 0\n\t"
      "ds_read_b64_tr_b16 a[140:141], v15 offset:22528\n\t"
      "v_mfma_f32_16x16x32_bf16 v[212:215], a[128:131], a[36:39], 0\n\t"
      "ds_read_b64_tr_b16 a[142:143], v15 offset:22784\n\t"
      "v_mfma_f32_16x16x32_bf16 v[216:219], a[128:131], a[40:43], 0\n\t"
      "global_load_dwordx4 a[24:27], v1, s[36:37] offset:2048 sc1\n\t"
      "v_mfma_f32_16x16x32_bf16 v[220:223], a[128:131], a[44:47], 0\n\t"
      "s_waitcnt lgkmcnt(4)\n\t"
      "v_mfma_f32_16x16x32_bf16 v[208:211], a[132:135], a[48:51], v[208:211]\n\t"
      "ds_read_b64_tr_b16 a[128:129], v16 offset:24576\n\t"
      "v_mfma_f32_16x16x32_bf16 v[212:215], a[132:135], a[52:55], v[212:215]\n\t"
      "ds_read_b64_tr_b16 a[130:131], v16 offset:24832\n\t"
      "v_mfma_f32_16x16x32_bf16 v[216:219], a[132:135], a[56:59], v[216:219]\n\t"
      "global_load_dwordx4 a[28:31], v1, s[36:37] offset:3072 sc1\n\t"
      "v_mfma_f32_16x16x32_bf16 v[220:223], a[132:135], a[60:63], v[220:223]\n\t"
      "s_waitcnt lgkmcnt(4)\n\t"
      "v_mfma_f32_16x16x32_bf16 v[224:227], a[136:139], a[32:35], 0\n\t"
      "ds_read_b64_tr_b16 a[132:133], v16 offset:26624\n\t"
      "v_mfma_f32_16x16x32_bf16 v[228:231], a[136:139], a[36:39], 0\n\t"
      "ds_read_b64_tr_b16 a[134:135], v16 offset:26880\n\t"
      "s_add_u32 m0, s63, 114688\n\t"
      "v_mfma_f32_16x16x32_bf16 v[232:235], a[136:139], a[40:43], 0\n\t"
      "global_load_lds_dwordx4 v4, s[42:43]\n\t"
      "v_mfma_f32_16x16x32_bf16 v[236:239], a[136:139], a[44:47], 0\n\t"
      "s_waitcnt lgkmcnt(4)\n\t"
      "v_mfma_f32_16x16x32_bf16 v[224:227], a[140:143], a[48:51], v[224:227]\n\t"
      "ds_read_b64_tr_b16 a[136:137], v14 offset:8192\n\t"
      "v_mfma_f32_16x16x32_bf16 v[228:231], a[140:143], a[52:55], v[228:231]\n\t"
      "ds_read_b64_tr_b16 a[138:139], v14 offset:8448\n\t"
      "s_add_u32 m0, s62, 118784\n\t"
      "v_mfma_f32_16x16x32_bf16 v[232:235], a[140:143], a[56:59], v[232:235]\n\t"
      "global_load_lds_dwordx4 v5, s[52:53]\n\t"
      "v_mfma_f32_16x16x32_bf16 v[236:239], a[140:143], a[60:63], v[236:239]\n\t"
      "s_waitcnt lgkmcnt(4)\n\t"
      "v_mfma_f32_16x16x32_bf16 v[240:243], a[128:131], a[32:35], 0\n\t"
      "ds_read_b64_tr_b16 a[140:141], v14 offset:10240\n\t"
      "v_mfma_f32_16x16x32_bf16 v[244:247], a[128:131], a[36:39], 0\n\t"
      "ds_read_b64_tr_b16 a[142:143], v14 offset:10496\n\t"
      "s_add_u32 m0, s62, 119808\n\t"
      "v_mfma_f32_16x16x32_bf16 v[248:251], a[128:131], a[40:43], 0\n\t"
      "global_load_lds_dwordx4 v6, s[52:53]\n\t"
      "v_mfma_f32_16x16x32_bf16 v[252:255], a[128:131], a[44:47], 0\n\t"
      "s_waitcnt lgkmcnt(4)\n\t"
      "v_mfma_f32_16x16x32_bf16 v[240:243], a[132:135], a[48:51], v[240:243]\n\t"
      "ds_read_b64_tr_b16 a[128:129], v15 offset:8192\n\t"
      "v_mfma_f32_16x16x32_bf16 v[244:247], a[132:135], a[52:55], v[244:247]\n\t"
      "ds_read_b64_tr_b16 a[130:131], v15 offset:8448\n\t"
      "s_add_u32 m0, s63, 126976\n\t"
      "v_mfma_f32_16x16x32_bf16 v[248:251], a[132:135], a[56:59], v[248:251]\n\t"
      "global_load_lds_dwordx4 v7, s[52:53]\n\t"
      "v_mfma_f32_16x16x32_bf16 v[252:255], a[132:135], a[60:63], v[252:255]\n\t"
      "s_waitcnt lgkmcnt(4)\n\t"
      "v_mfma_f32_16x16x32_bf16 v[112:115], a[136:139], a[32:35], 0\n\t"
      "ds_read_b64_tr_b16 a[132:133], v15 offset:10240\n\t"
      "v_mfma_f32_16x16x32_bf16 v[116:119], a[136:139], a[36:39], 0\n\t"
      "ds_read_b64_tr_b16 a[134:135], v15 offset:10496\n\t"
      "s_add_u32 m0, s63, 131072\n\t"
      "v_mfma_f32_16x16x32_bf16 v[120:123], a[136:139], a[40:43], 0\n\t"
      "global_load_lds_dwordx4 v8, s[52:53]\n\t"
      "v_mfma_f32_16x16x32_bf16 v[124:127], a[136:139], a[44:47], 0\n\t"
      "s_waitcnt vmcnt(15) lgkmcnt(4)\n\t"
      "s_barrier\n\t"
      "v_mfma_f32_16x16x32_bf16 v[112:115], a[140:143], a[48:51], v[112:115]\n\t"
      "ds_read_b64_tr_b16 a[136:137], v10 offset:28672\n\t"
      "s_add_u32 s36, s36, 8192\n\t"
      "s_addc_u32 s37, s37, 0\n\t"
      "v_mfma_f32_16x16x32_bf16 v[116:119], a[140:143], a[52:55], v[116:119]\n\t"
      "ds_read_b64_tr_b16 a[138:139], v10 offset:29184\n\t"
      "s_add_u32 s40, s40, 393216\n\t"
      "s_addc_u32 s41, s41, 0\n\t"
      "v_mfma_f32_16x16x32_bf16 v[120:123], a[140:143], a[56:59], v[120:123]\n\t"
      "s_add_u32 s42, s42, 393216\n\t"
      "s_addc_u32 s43, s43, 0\n\t"
      "v_mfma_f32_16x16x32_bf16 v[124:127], a[140:143], a[60:63], v[124:127]\n\t"
      "s_add_u32 s52, s52, 458752\n\t"
      "s_addc_u32 s53, s53, 0\n\t"
      "s_waitcnt lgkmcnt(4)\n\t"
      "v_mfma_f32_16x16x32_bf16 v[128:131], a[128:131], a[32:35], 0\n\t"
      "ds_read_b64_tr_b16 a[140:141], v10 offset:32768\n\t"
      "s_cmp_eq_u32 s60, 1\n\t"
      "s_cselect_b32 s56, 0, -1\n\t"
      "v_mfma_f32_16x16x32_bf16 v[132:135], a[128:131], a[36:39], 0\n\t"
      "ds_read_b64_tr_b16 a[142:143], v10 offset:33280\n\t"
      "s_or_b32 s56, s56, 0xfc000000\n\t"
      "v_mfma_f32_16x16x32_bf16 v[136:139], a[128:131], a[40:43], 0\n\t"
      "s_and_b32 s56, s56, 0xffffffff\n\t"
      "v_mfma_f32_16x16x32_bf16 v[140:143], a[128:131], a[44:47], 0\n\t"
      "s_mov_b64 exec, s[56:57]\n\t"
      "global_load_dword v31, v[28:29], off\n\t"
      "s_mov_b64 exec, -1\n\t"
      "s_waitcnt lgkmcnt(4)\n\t"
      "v_mfma_f32_16x16x32_bf16 v[128:131], a[132:135], a[48:51], v[128:131]\n\t"
      "ds_read_b64_tr_b16 a[128:129], v11 offset:28672\n\t"
      "v_add_co_u32 v28, vcc, v28, v30\n\t"
      "v_addc_co_u32 v29, vcc, 0, v29, vcc\n\t"
      "v_mfma_f32_16x16x32_bf16 v[132:135], a[132:135], a[52:55], v[132:135]\n\t"
      "ds_read_b64_tr_b16 a[130:131], v11 offset:29184\n\t"
      "v_mfma_f32_16x16x32_bf16 v[136:139], a[132:135], a[56:59], v[136:139]\n\t"
      "v_mfma_f32_16x16x32_bf16 v[140:143], a[132:135], a[60:63], v[140:143]\n\t"
      ".Lkm_loop_%=:\n\t"
      "s_waitcnt lgkmcnt(4)\n\t"
      "v_mfma_f32_16x16x32_bf16 v[48:51], a[136:139], a[96:99], v[48:51]\n\t"
      "ds_read_b64_tr_b16 a[132:133], v11 offset:32768\n\t"
      "v_and_b32 v32, 15, v0\n\t"
      "v_mfma_f32_16x16x32_bf16 v[52:55], a[136:139], a[100:103], v[52:55]\n\t"
      "ds_read_b64_tr_b16 a[134:135], v11 offset:33280\n\t"
      "s_add_u32 m0, s62, 135168\n\t"
      "v_mfma_f32_16x16x32_bf16 v[56:59], a[136:139], a[104:107], v[56:59]\n\t"
      "global_load_lds_dwordx4 v2, s[40:41]\n\t"
      "v_mfma_f32_16x16x32_bf16 v[60:63], a[136:139], a[108:111], v[60:63]\n\t"
      "s_waitcnt lgkmcnt(4)\n\t"
      "v_mfma_f32_16x16x32_bf16 v[48:51], a[140:143], a[112:115], v[48:51]\n\t"
      "ds_read_b64_tr_b16 a[136:137], v12 offset:28672\n\t"
      "v_lshrrev_b32 v33, 4, v0\n\t"
      "v_mfma_f32_16x16x32_bf16 v[52:55], a[140:143], a[116:119], v[52:55]\n\t"
      "ds_read_b64_tr_b16 a[138:139], v12 offset:29184\n\t"
      "v_lshlrev_b32 v35, 11, v32\n\t"
      "v_mfma_f32_16x16x32_bf16 v[56:59], a[140:143], a[120:123], v[56:59]\n\t"
      "global_load_dwordx4 a[32:35], v1, s[36:37] offset:-4096 sc1\n\t"
      "v_mfma_f32_16x16x32_bf16 v[60:63], a[140:143], a[124:127], v[60:63]\n\t"
      "s_waitcnt lgkmcnt(4)\n\t"
      "v_mfma_f32_16x16x32_bf16 v[64:67], a[128:131], a[96:99], v[64:67]\n\t"
      "ds_read_b64_tr_b16 a[140:141], v12 offset:32768\n\t"
      "v_and_b32 v34, 1, v33\n\t"
      "v_mfma_f32_16x16x32_bf16 v[68:71], a[128:131], a[100:103], v[68:71]\n\t"
      "ds_read_b64_tr_b16 a[142:143], v12 offset:33280\n\t"
      "v_lshl_add_u32 v35, v34, 5, v35\n\t"
      "v_mfma_f32_16x16x32_bf16 v[72:75], a[128:131], a[104:107], v[72:75]\n\t"
      "v_lshrrev_b32 v34, 1, v33\n\t"
      "v_mfma_f32_16x16x32_bf16 v[76:79], a[128:131], a[108:111], v[76:79]\n\t"
      "s_waitcnt lgkmcnt(4)\n\t"
      "v_mfma_f32_16x16x32_bf16 v[64:67], a[132:135], a[112:115], v[64:67]\n\t"
      "ds_read_b64_tr_b16 a[128:129], v13 offset:28672\n\t"
      "v_lshl_add_u32 v34, v34, 4, v35\n\t"
      "v_mfma_f32_16x16x32_bf16 v[68:71], a[132:135], a[116:119], v[68:71]\n\t"
      "ds_read_b64_tr_b16 a[130:131], v13 offset:29184\n\t"
      "v_accvgpr_write_b32 a212, v34\n\t"
      "v_mfma_f32_16x16x32_bf16 v[72:75], a[132:135], a[120:123], v[72:75]\n\t"
      "global_load_dwordx4 a[36:39], v1, s[36:37] offset:-3072 sc1\n\t"
      "v_mfma_f32_16x16x32_bf16 v[76:79], a[132:135], a[124:127], v[76:79]\n\t"
      "s_waitcnt lgkmcnt(4)\n\t"
      "v_mfma_f32_16x16x32_bf16 v[80:83], a[136:139], a[96:99], v[80:83]\n\t"
      "ds_read_b64_tr_b16 a[132:133], v13 offset:32768\n\t"
      "v_lshlrev_b32 v33, 2, v33\n\t"
      "v_mfma_f32_16x16x32_bf16 v[84:87], a[136:139], a[100:103], v[84:87]\n\t"
      "ds_read_b64_tr_b16 a[134:135], v13 offset:33280\n\t"
      "v_sub_u32 v32, v32, v33\n\t"
      "v_mfma_f32_16x16x32_bf16 v[88:91], a[136:139], a[104:107], v[88:91]\n\t"
      "global_load_dwordx4 a[40:43], v1, s[36:37] offset:-2048 sc1\n\t"
      "v_mfma_f32_16x16x32_bf16 v[92:95], a[136:139], a[108:111], v[92:95]\n\t"
      "s_waitcnt lgkmcnt(4)\n\t"
      "v_mfma_f32_16x16x32_bf16 v[80:83], a[140:143], a[112:115], v[80:83]\n\t"
      "ds_read_b64_tr_b16 a[136:137], v10 offset:40960\n\t"
      "v_accvgpr_write_b32 a217, v32\n\t"
      "v_mfma_f32_16x16x32_bf16 v[84:87], a[140:143], a[116:119], v[84:87]\n\t"
      "ds_read_b64_tr_b16 a[138:139], v10 offset:41472\n\t"
      "v_mov_b32 v33, 0xf149f2ca\n\t"
      "v_mfma_f32_16x16x32_bf16 v[88:91], a[140:143], a[120:123], v[88:91]\n\t"
      "global_load_dwordx4 a[44:47], v1, s[36:37] offset:-1024 sc1\n\t"
      "v_mfma_f32_16x16x32_bf16 v[92:95], a[140:143], a[124:127], v[92:95]\n\t"
      "s_waitcnt lgkmcnt(4)\n\t"
      "v_mfma_f32_16x16x32_bf16 v[96:99], a[128:131], a[96:99], v[96:99]\n\t"
      "ds_read_b64_tr_b16 a[140:141], v10 offset:45056\n\t"
      "v_mov_b32 v35, 0\n\t"
      "v_mfma_f32_16x16x32_bf16 v[100:103], a[128:131], a[100:103], v[100:103]\n\t"
      "ds_read_b64_tr_b16 a[142:143], v10 offset:45568\n\t"
      "s_add_u32 m0, s62, 136192\n\t"
      "v_mfma_f32_16x16x32_bf16 v[104:107], a[128:131], a[104:107], v[104:107]\n\t"
      "global_load_lds_dwordx4 v3, s[40:41]\n\t"
      "v_mfma_f32_16x16x32_bf16 v[108:111], a[128:131], a[108:111], v[108:111]\n\t"
      "s_waitcnt lgkmcnt(4)\n\t"
      "v_mfma_f32_16x16x32_bf16 v[96:99], a[132:135], a[112:115], v[96:99]\n\t"
      "ds_read_b64_tr_b16 a[128:129], v11 offset:40960\n\t"
      "v_mov_b32 v36, 0x3f803f80\n\t"
      "v_mfma_f32_16x16x32_bf16 v[100:103], a[132:135], a[116:119], v[100:103]\n\t"
      "ds_read_b64_tr_b16 a[130:131], v11 offset:41472\n\t"
      "v_cmp_gt_i32 vcc, 0, v32\n\t"
      "v_mfma_f32_16x16x32_bf16 v[104:107], a[132:135], a[120:123], v[104:107]\n\t"
      "v_accvgpr_write_b32 a192, v36\n\t"
      "v_mfma_f32_16x16x32_bf16 v[108:111], a[132:135], a[124:127], v[108:111]\n\t"
      "s_waitcnt lgkmcnt(4)\n\t"
      "v_mfma_f32_16x16x32_bf16 v[144:147], a[136:139], a[64:67], v[144:147]\n\t"
      "ds_read_b64_tr_b16 a[132:133], v11 offset:45056\n\t"
      "v_cndmask_b32 v34, v33, v35, vcc\n\t"
      "v_mfma_f32_16x16x32_bf16 v[148:151], a[136:139], a[68:71], v[148:151]\n\t"
      "ds_read_b64_tr_b16 a[134:135], v11 offset:45568\n\t"
      "v_accvgpr_write_b32 a224, v34\n\t"
      "v_mfma_f32_16x16x32_bf16 v[152:155], a[136:139], a[72:75], v[152:155]\n\t"
      "global_load_dwordx4 a[48:51], v1, s[36:37] offset:0 sc1\n\t"
      "v_mfma_f32_16x16x32_bf16 v[156:159], a[136:139], a[76:79], v[156:159]\n\t"
      "s_waitcnt lgkmcnt(4)\n\t"
      "v_mfma_f32_16x16x32_bf16 v[144:147], a[140:143], a[80:83], v[144:147]\n\t"
      "ds_read_b64_tr_b16 a[136:137], v12 offset:40960\n\t"
      "v_cndmask_b32 v34, v35, v33, vcc\n\t"
      "v_mfma_f32_16x16x32_bf16 v[148:151], a[140:143], a[84:87], v[148:151]\n\t"
      "ds_read_b64_tr_b16 a[138:139], v12 offset:41472\n\t"
      "v_accvgpr_write_b32 a228, v34\n\t"
      "v_mfma_f32_16x16x32_bf16 v[152:155], a[140:143], a[88:91], v[152:155]\n\t"
      "global_load_dwordx4 a[52:55], v1, s[36:37] offset:1024 sc1\n\t"
      "v_mfma_f32_16x16x32_bf16 v[156:159], a[140:143], a[92:95], v[156:159]\n\t"
      "s_waitcnt lgkmcnt(4)\n\t"
      "v_mfma_f32_16x16x32_bf16 v[160:163], a[128:131], a[64:67], v[160:163]\n\t"
      "ds_read_b64_tr_b16 a[140:141], v12 offset:45056\n\t"
      "v_cmp_gt_i32 vcc, 1, v32\n\t"
      "v_mfma_f32_16x16x32_bf16 v[164:167], a[128:131], a[68:71], v[164:167]\n\t"
      "ds_read_b64_tr_b16 a[142:143], v12 offset:45568\n\t"
      "v_accvgpr_write_b32 a193, v36\n\t"
      "v_mfma_f32_16x16x32_bf16 v[168:171], a[128:131], a[72:75], v[168:171]\n\t"
      "global_load_dwordx4 a[56:59], v1, s[36:37] offset:2048 sc1\n\t"
      "v_mfma_f32_16x16x32_bf16 v[172:175], a[128:131], a[76:79], v[172:175]\n\t"
      "s_waitcnt lgkmcnt(4)\n\t"
      "v_mfma_f32_16x16x32_bf16 v[160:163], a[132:135], a[80:83], v[160:163]\n\t"
      "ds_read_b64_tr_b16 a[128:129], v13 offset:40960\n\t"
      "v_cndmask_b32 v34, v33, v35, vcc\n\t"
      "v_mfma_f32_16x16x32_bf16 v[164:167], a[132:135], a[84:87], v[164:167]\n\t"
      "ds_read_b64_tr_b16 a[130:131], v13 offset:41472\n\t"
      "v_accvgpr_write_b32 a225, v34\n\t"
      "v_mfma_f32_16x16x32_bf16 v[168:171], a[132:135], a[88:91], v[168:171]\n\t"
      "global_load_dwordx4 a[60:63], v1, s[36:37] offset:3072 sc1\n\t"
      "v_mfma_f32_16x16x32_bf16 v[172:175], a[132:135], a[92:95], v[172:175]\n\t"
      "s_waitcnt lgkmcnt(4)\n\t"
      "v_mfma_f32_16x16x32_bf16 v[176:179], a[136:139], a[64:67], v[176:179]\n\t"
      "ds_read_b64_tr_b16 a[132:133], v13 offset:45056\n\t"
      "v_cndmask_b32 v34, v35, v33, vcc\n\t"
      "v_mfma_f32_16x16x32_bf16 v[180:183], a[136:139], a[68:71], v[180:183]\n\t"
      "ds_read_b64_tr_b16 a[134:135], v13 offset:45568\n\t"
      "s_add_u32 m0, s63, 143360\n\t"
      "v_mfma_f32_16x16x32_bf16 v[184:187], a[136:139], a[72:75], v[184:187]\n\t"
      "global_load_lds_dwordx4 v4, s[42:43]\n\t"
      "v_mfma_f32_16x16x32_bf16 v[188:191], a[136:139], a[76:79], v[188:191]\n\t"
      "s_waitcnt lgkmcnt(4)\n\t"
      "v_mfma_f32_16x16x32_bf16 v[176:179], a[140:143], a[80:83], v[176:179]\n\t"
      "ds_read_b64_tr_b16 a[136:137], v14 offset:49152\n\t"
      "v_accvgpr_write_b32 a229, v34\n\t"
      "v_mfma_f32_16x16x32_bf16 v[180:183], a[140:143], a[84:87], v[180:183]\n\t"
      "ds_read_b64_tr_b16 a[138:139], v14 offset:49408\n\t"
      "v_cmp_gt_i32 vcc, 2, v32\n\t"
      "v_mfma_f32_16x16x32_bf16 v[184:187], a[140:143], a[88:91], v[184:187]\n\t"
      "v_accvgpr_write_b32 a194, v36\n\t"
      "v_mfma_f32_16x16x32_bf16 v[188:191], a[140:143], a[92:95], v[188:191]\n\t"
      "s_waitcnt lgkmcnt(4)\n\t"
      "v_mfma_f32_16x16x32_bf16 v[192:195], a[128:131], a[64:67], v[192:195]\n\t"
      "ds_read_b64_tr_b16 a[140:141], v14 offset:51200\n\t"
      "v_cndmask_b32 v34, v33, v35, vcc\n\t"
      "v_mfma_f32_16x16x32_bf16 v[196:199], a[128:131], a[68:71], v[196:199]\n\t"
      "ds_read_b64_tr_b16 a[142:143], v14 offset:51456\n\t"
      "v_accvgpr_write_b32 a226, v34\n\t"
      "v_mfma_f32_16x16x32_bf16 v[200:203], a[128:131], a[72:75], v[200:203]\n\t"
      "v_cndmask_b32 v34, v35, v33, vcc\n\t"
      "v_mfma_f32_16x16x32_bf16 v[204:207], a[128:131], a[76:79], v[204:207]\n\t"
      "s_waitcnt lgkmcnt(4)\n\t"
      "v_mfma_f32_16x16x32_bf16 v[192:195], a[132:135], a[80:83], v[192:195]\n\t"
      "ds_read_b64_tr_b16 a[128:129], v15 offset:49152\n\t"
      "v_accvgpr_write_b32 a230, v34\n\t"
      "v_mfma_f32_16x16x32_bf16 v[196:199], a[132:135], a[84:87], v[196:199]\n\t"
      "ds_read_b64_tr_b16 a[130:131], v15 offset:49408\n\t"
      "s_add_u32 m0, s62, 147456\n\t"
      "v_mfma_f32_16x16x32_bf16 v[200:203], a[132:135], a[88:91], v[200:203]\n\t"
      "global_load_lds_dwordx4 v5, s[52:53]\n\t"
      "v_mfma_f32_16x16x32_bf16 v[204:207], a[132:135], a[92:95], v[204:207]\n\t"
      "s_waitcnt lgkmcnt(4)\n\t"
      "v_mfma_f32_16x16x32_bf16 v[208:211], a[136:139], a[64:67], v[208:211]\n\t"
      "ds_read_b64_tr_b16 a[132:133], v15 offset:51200\n\t"
      "v_cmp_gt_i32 vcc, 3, v32\n\t"
      "v_mfma_f32_16x16x32_bf16 v[212:215], a[136:139], a[68:71], v[212:215]\n\t"
      "ds_read_b64_tr_b16 a[134:135], v15 offset:51456\n\t"
      "s_add_u32 m0, s62, 148480\n\t"
      "v_mfma_f32_16x16x32_bf16 v[216:219], a[136:139], a[72:75], v[216:219]\n\t"
      "global_load_lds_dwordx4 v6, s[52:53]\n\t"
      "v_mfma_f32_16x16x32_bf16 v[220:223], a[136:139], a[76:79], v[220:223]\n\t"
      "s_waitcnt lgkmcnt(4)\n\t"
      "v_mfma_f32_16x16x32_bf16 v[208:211], a[140:143], a[80:83], v[208:211]\n\t"
      "ds_read_b64_tr_b16 a[136:137], v16 offset:53248\n\t"
      "v_accvgpr_write_b32 a195, v36\n\t"
      "v_mfma_f32_16x16x32_bf16 v[212:215], a[140:143], a[84:87], v[212:215]\n\t"
      "ds_read_b64_tr_b16 a[138:139], v16 offset:53504\n\t"
      "v_cndmask_b32 v34, v33, v35, vcc\n\t"
      "v_mfma_f32_16x16x32_bf16 v[216:219], a[140:143], a[88:91], v[216:219]\n\t"
      "v_accvgpr_write_b32 a227, v34\n\t"
      "v_mfma_f32_16x16x32_bf16 v[220:223], a[140:143], a[92:95], v[220:223]\n\t"
      "s_waitcnt lgkmcnt(4)\n\t"
      "v_mfma_f32_16x16x32_bf16 v[224:227], a[128:131], a[64:67], v[224:227]\n\t"
      "ds_read_b64_tr_b16 a[140:141], v16 offset:55296\n\t"
      "v_cndmask_b32 v34, v35, v33, vcc\n\t"
      "v_mfma_f32_16x16x32_bf16 v[228:231], a[128:131], a[68:71], v[228:231]\n\t"
      "ds_read_b64_tr_b16 a[142:143], v16 offset:55552\n\t"
      "s_add_u32 m0, s63, 155648\n\t"
      "v_mfma_f32_16x16x32_bf16 v[232:235], a[128:131], a[72:75], v[232:235]\n\t"
      "global_load_lds_dwordx4 v7, s[52:53]\n\t"
      "v_mfma_f32_16x16x32_bf16 v[236:239], a[128:131], a[76:79], v[236:239]\n\t"
      "s_waitcnt lgkmcnt(4)\n\t"
      "v_mfma_f32_16x16x32_bf16 v[224:227], a[132:135], a[80:83], v[224:227]\n\t"
      "ds_read_b64_tr_b16 a[128:129], v14 offset:36864\n\t"
      "v_accvgpr_write_b32 a231, v34\n\t"
      "v_mfma_f32_16x16x32_bf16 v[228:231], a[132:135], a[84:87], v[228:231]\n\t"
      "ds_read_b64_tr_b16 a[130:131], v14 offset:37120\n\t"
      "v_mfma_f32_16x16x32_bf16 v[232:235], a[132:135], a[88:91], v[232:235]\n\t"
      "v_mfma_f32_16x16x32_bf16 v[236:239], a[132:135], a[92:95], v[236:239]\n\t"
      "s_waitcnt lgkmcnt(4)\n\t"
      "v_mfma_f32_16x16x32_bf16 v[240:243], a[136:139], a[64:67], v[240:243]\n\t"
      "ds_read_b64_tr_b16 a[132:133], v14 offset:38912\n\t"
      "v_mfma_f32_16x16x32_bf16 v[244:247], a[136:139], a[68:71], v[244:247]\n\t"
      "ds_read_b64_tr_b16 a[134:135], v14 offset:39168\n\t"
      "s_add_u32 m0, s63, 159744\n\t"
      "v_mfma_f32_16x16x32_bf16 v[248:251], a[136:139], a[72:75], v[248:251]\n\t"
      "global_load_lds_dwordx4 v8, s[52:53]\n\t"
      "v_mfma_f32_16x16x32_bf16 v[252:255], a[136:139], a[76:79], v[252:255]\n\t"
      "s_waitcnt lgkmcnt(4)\n\t"
      "v_mfma_f32_16x16x32_bf16 v[240:243], a[140:143], a[80:83], v[240:243]\n\t"
      "ds_read_b64_tr_b16 a[136:137], v15 offset:36864\n\t"
      "v_mfma_f32_16x16x32_bf16 v[244:247], a[140:143], a[84:87], v[244:247]\n\t"
      "ds_read_b64_tr_b16 a[138:139], v15 offset:37120\n\t"
      "v_mfma_f32_16x16x32_bf16 v[248:251], a[140:143], a[88:91], v[248:251]\n\t"
      "v_mfma_f32_16x16x32_bf16 v[252:255], a[140:143], a[92:95], v[252:255]\n\t"
      "s_waitcnt lgkmcnt(4)\n\t"
      "v_mfma_f32_16x16x32_bf16 v[112:115], a[128:131], a[64:67], v[112:115]\n\t"
      "ds_read_b64_tr_b16 a[140:141], v15 offset:38912\n\t"
      "v_mfma_f32_16x16x32_bf16 v[116:119], a[128:131], a[68:71], v[116:119]\n\t"
      "ds_read_b64_tr_b16 a[142:143], v15 offset:39168\n\t"
      "v_mfma_f32_16x16x32_bf16 v[120:123], a[128:131], a[72:75], v[120:123]\n\t"
      "v_mfma_f32_16x16x32_bf16 v[124:127], a[128:131], a[76:79], v[124:127]\n\t"
      "s_waitcnt vmcnt(15) lgkmcnt(4)\n\t"
      "s_barrier\n\t"
      "v_mfma_f32_16x16x32_bf16 v[112:115], a[132:135], a[80:83], v[112:115]\n\t"
      "ds_read_b64_tr_b16 a[128:129], v17 offset:0\n\t"
      "s_add_u32 s36, s36, 8192\n\t"
      "s_addc_u32 s37, s37, 0\n\t"
      "v_mfma_f32_16x16x32_bf16 v[116:119], a[132:135], a[84:87], v[116:119]\n\t"
      "ds_read_b64_tr_b16 a[130:131], v17 offset:512\n\t"
      "s_add_u32 s40, s40, 393216\n\t"
      "s_addc_u32 s41, s41, 0\n\t"
      "v_mfma_f32_16x16x32_bf16 v[120:123], a[132:135], a[88:91], v[120:123]\n\t"
      "s_add_u32 s42, s42, 393216\n\t"
      "s_addc_u32 s43, s43, 0\n\t"
      "v_mfma_f32_16x16x32_bf16 v[124:127], a[132:135], a[92:95], v[124:127]\n\t"
      "s_add_u32 s52, s52, 458752\n\t"
      "s_addc_u32 s53, s53, 0\n\t"
      "s_waitcnt lgkmcnt(4)\n\t"
      "v_mfma_f32_16x16x32_bf16 v[128:131], a[136:139], a[64:67], v[128:131]\n\t"
      "ds_read_b64_tr_b16 a[132:133], v17 offset:4096\n\t"
      "v_mfma_f32_16x16x32_bf16 v[132:135], a[136:139], a[68:71], v[132:135]\n\t"
      "ds_read_b64_tr_b16 a[134:135], v17 offset:4608\n\t"
      "v_mfma_f32_16x16x32_bf16 v[136:139], a[136:139], a[72:75], v[136:139]\n\t"
      "v_mfma_f32_16x16x32_bf16 v[140:143], a[136:139], a[76:79], v[140:143]\n\t"
      "s_waitcnt lgkmcnt(4)\n\t"
      "v_mfma_f32_16x16x32_bf16 v[128:131], a[140:143], a[80:83], v[128:131]\n\t"
      "ds_read_b64_tr_b16 a[136:137], v18 offset:0\n\t"
      "v_mfma_f32_16x16x32_bf16 v[132:135], a[140:143], a[84:87], v[132:135]\n\t"
      "ds_read_b64_tr_b16 a[138:139], v18 offset:512\n\t"
      "v_mfma_f32_16x16x32_bf16 v[136:139], a[140:143], a[88:91], v[136:139]\n\t"
      "v_mfma_f32_16x16x32_bf16 v[140:143], a[140:143], a[92:95], v[140:143]\n\t"
      "s_waitcnt lgkmcnt(4)\n\t"
      "v_mfma_f32_16x16x32_bf16 v[48:51], a[128:131], a[0:3], v[48:51]\n\t"
      "ds_read_b64_tr_b16 a[140:141], v18 offset:4096\n\t"
      "s_lshl_b32 s64, %18, 3\n\t"
      "v_mfma_f32_16x16x32_bf16 v[52:55], a[128:131], a[4:7], v[52:55]\n\t"
      "ds_read_b64_tr_b16 a[142:143], v18 offset:4608\n\t"
      "s_add_u32 m0, s62, 49152\n\t"
      "v_mfma_f32_16x16x32_bf16 v[56:59], a[128:131], a[8:11], v[56:59]\n\t"
      "global_load_lds_dwordx4 v2, s[40:41]\n\t"
      "v_mfma_f32_16x16x32_bf16 v[60:63], a[128:131], a[12:15], v[60:63]\n\t"
      "s_waitcnt lgkmcnt(4)\n\t"
      "v_mfma_f32_16x16x32_bf16 v[48:51], a[132:135], a[16:19], v[48:51]\n\t"
      "ds_read_b64_tr_b16 a[128:129], v19 offset:0\n\t"
      "s_add_u32 s64, s64, %17\n\t"
      "v_mfma_f32_16x16x32_bf16 v[52:55], a[132:135], a[20:23], v[52:55]\n\t"
      "ds_read_b64_tr_b16 a[130:131], v19 offset:512\n\t"
      "s_lshl_b32 s64, s64, 2\n\t"
      "v_mfma_f32_16x16x32_bf16 v[56:59], a[132:135], a[24:27], v[56:59]\n\t"
      "global_load_dwordx4 a[64:67], v1, s[36:37] offset:-4096 sc1\n\t"
      "v_mfma_f32_16x16x32_bf16 v[60:63], a[132:135], a[28:31], v[60:63]\n\t"
      "s_waitcnt lgkmcnt(4)\n\t"
      "v_mfma_f32_16x16x32_bf16 v[64:67], a[136:139], a[0:3], v[64:67]\n\t"
      "ds_read_b64_tr_b16 a[132:133], v19 offset:4096\n\t"
      "s_add_u32 s64, s64, %14\n\t"
      "v_mfma_f32_16x16x32_bf16 v[68:71], a[136:139], a[4:7], v[68:71]\n\t"
      "ds_read_b64_tr_b16 a[134:135], v19 offset:4608\n\t"
      "s_lshl_b32 s65, s64, 12\n\t"
      "v_mfma_f32_16x16x32_bf16 v[72:75], a[136:139], a[8:11], v[72:75]\n\t"
      "s_and_b32 s66, %18, 1\n\t"
      "v_mfma_f32_16x16x32_bf16 v[76:79], a[136:139], a[12:15], v[76:79]\n\t"
      "s_waitcnt lgkmcnt(4)\n\t"
      "v_mfma_f32_16x16x32_bf16 v[64:67], a[140:143], a[16:19], v[64:67]\n\t"
      "ds_read_b64_tr_b16 a[136:137], v20 offset:0\n\t"
      "s_lshl_b32 s66, s66, 22\n\t"
      "v_mfma_f32_16x16x32_bf16 v[68:71], a[140:143], a[20:23], v[68:71]\n\t"
      "ds_read_b64_tr_b16 a[138:139], v20 offset:512\n\t"
      "s_add_u32 s65, s65, s66\n\t"
      "v_mfma_f32_16x16x32_bf16 v[72:75], a[140:143], a[24:27], v[72:75]\n\t"
      "global_load_dwordx4 a[68:71], v1, s[36:37] offset:-3072 sc1\n\t"
      "v_mfma_f32_16x16x32_bf16 v[76:79], a[140:143], a[28:31], v[76:79]\n\t"
      "s_waitcnt lgkmcnt(4)\n\t"
      "v_mfma_f32_16x16x32_bf16 v[80:83], a[128:131], a[0:3], v[80:83]\n\t"
      "ds_read_b64_tr_b16 a[140:141], v20 offset:4096\n\t"
      "s_add_u32 s68, s65, 8388608\n\t"
      "v_mfma_f32_16x16x32_bf16 v[84:87], a[128:131], a[4:7], v[84:87]\n\t"
      "ds_read_b64_tr_b16 a[142:143], v20 offset:4608\n\t"
      "s_xor_b32 s69, s68, 0x20000\n\t"
      "v_mfma_f32_16x16x32_bf16 v[88:91], a[128:131], a[8:11], v[88:91]\n\t"
      "global_load_dwordx4 a[72:75], v1, s[36:37] offset:-2048 sc1\n\t"
      "v_mfma_f32_16x16x32_bf16 v[92:95], a[128:131], a[12:15], v[92:95]\n\t"
      "s_waitcnt lgkmcnt(4)\n\t"
      "v_mfma_f32_16x16x32_bf16 v[80:83], a[132:135], a[16:19], v[80:83]\n\t"
      "ds_read_b64_tr_b16 a[128:129], v17 offset:12288\n\t"
      "s_lshl_b32 s64, s64, 7\n\t"
      "v_mfma_f32_16x16x32_bf16 v[84:87], a[132:135], a[20:23], v[84:87]\n\t"
      "ds_read_b64_tr_b16 a[130:131], v17 offset:12800\n\t"
      "s_add_u32 s76, s64, 52428800\n\t"
      "v_mfma_f32_16x16x32_bf16 v[88:91], a[132:135], a[24:27], v[88:91]\n\t"
      "global_load_dwordx4 a[76:79], v1, s[36:37] offset:-1024 sc1\n\t"
      "v_mfma_f32_16x16x32_bf16 v[92:95], a[132:135], a[28:31], v[92:95]\n\t"
      "s_waitcnt lgkmcnt(4)\n\t"
      "v_mfma_f32_16x16x32_bf16 v[96:99], a[136:139], a[0:3], v[96:99]\n\t"
      "ds_read_b64_tr_b16 a[132:133], v17 offset:16384\n\t"
      "s_xor_b32 s65, s76, 4096\n\t"
      "v_mfma_f32_16x16x32_bf16 v[100:103], a[136:139], a[4:7], v[100:103]\n\t"
      "ds_read_b64_tr_b16 a[134:135], v17 offset:16896\n\t"
      "s_add_u32 m0, s62, 50176\n\t"
      "v_mfma_f32_16x16x32_bf16 v[104:107], a[136:139], a[8:11], v[104:107]\n\t"
      "global_load_lds_dwordx4 v3, s[40:41]\n\t"
      "v_mfma_f32_16x16x32_bf16 v[108:111], a[136:139], a[12:15], v[108:111]\n\t"
      "s_waitcnt lgkmcnt(4)\n\t"
      "v_mfma_f32_16x16x32_bf16 v[96:99], a[140:143], a[16:19], v[96:99]\n\t"
      "ds_read_b64_tr_b16 a[136:137], v18 offset:12288\n\t"
      "v_mov_b32 v37, s65\n\t"
      "v_mfma_f32_16x16x32_bf16 v[100:103], a[140:143], a[20:23], v[100:103]\n\t"
      "ds_read_b64_tr_b16 a[138:139], v18 offset:12800\n\t"
      "s_sub_u32 s64, s76, 512\n\t"
      "v_mfma_f32_16x16x32_bf16 v[104:107], a[140:143], a[24:27], v[104:107]\n\t"
      "s_cmp_eq_u32 %17, 0\n\t"
      "s_cselect_b32 s64, s65, s64\n\t"
      "v_mfma_f32_16x16x32_bf16 v[108:111], a[140:143], a[28:31], v[108:111]\n\t"
      "s_waitcnt lgkmcnt(4)\n\t"
      "v_mfma_f32_16x16x32_bf16 v[144:147], a[128:131], a[96:99], v[144:147]\n\t"
      "ds_read_b64_tr_b16 a[140:141], v18 offset:16384\n\t"
      "v_writelane_b32 v37, s64, 1\n\t"
      "v_mfma_f32_16x16x32_bf16 v[148:151], a[128:131], a[100:103], v[148:151]\n\t"
      "ds_read_b64_tr_b16 a[142:143], v18 offset:16896\n\t"
      "s_sub_u32 s64, s65, 512\n\t"
      "v_mfma_f32_16x16x32_bf16 v[152:155], a[128:131], a[104:107], v[152:155]\n\t"
      "global_load_dwordx4 a[80:83], v1, s[36:37] offset:0 sc1\n\t"
      "v_mfma_f32_16x16x32_bf16 v[156:159], a[128:131], a[108:111], v[156:159]\n\t"
      "s_waitcnt lgkmcnt(4)\n\t"
      "v_mfma_f32_16x16x32_bf16 v[144:147], a[132:135], a[112:115], v[144:147]\n\t"
      "ds_read_b64_tr_b16 a[128:129], v19 offset:12288\n\t"
      "s_cmp_eq_u32 %17, 0\n\t"
      "s_cselect_b32 s64, s65, s64\n\t"
      "v_mfma_f32_16x16x32_bf16 v[148:151], a[132:135], a[116:119], v[148:151]\n\t"
      "ds_read_b64_tr_b16 a[130:131], v19 offset:12800\n\t"
      "v_writelane_b32 v37, s64, 2\n\t"
      "v_mfma_f32_16x16x32_bf16 v[152:155], a[132:135], a[120:123], v[152:155]\n\t"
      "global_load_dwordx4 a[84:87], v1, s[36:37] offset:1024 sc1\n\t"
      "v_mfma_f32_16x16x32_bf16 v[156:159], a[132:135], a[124:127], v[156:159]\n\t"
      "s_waitcnt lgkmcnt(4)\n\t"
      "v_mfma_f32_16x16x32_bf16 v[160:163], a[136:139], a[96:99], v[160:163]\n\t"
      "ds_read_b64_tr_b16 a[132:133], v19 offset:16384\n\t"
      "v_mov_b32 v24, v37\n\t"
      "v_mfma_f32_16x16x32_bf16 v[164:167], a[136:139], a[100:103], v[164:167]\n\t"
      "ds_read_b64_tr_b16 a[134:135], v19 offset:16896\n\t"
      "s_mov_b32 s77, 0\n\t"
      "v_mfma_f32_16x16x32_bf16 v[168:171], a[136:139], a[104:107], v[168:171]\n\t"
      "global_load_dwordx4 a[88:91], v1, s[36:37] offset:2048 sc1\n\t"
      "v_mfma_f32_16x16x32_bf16 v[172:175], a[136:139], a[108:111], v[172:175]\n\t"
      "s_waitcnt lgkmcnt(4)\n\t"
      "v_mfma_f32_16x16x32_bf16 v[160:163], a[140:143], a[112:115], v[160:163]\n\t"
      "ds_read_b64_tr_b16 a[136:137], v20 offset:12288\n\t"
      "s_mov_b32 s80, 0x3e8293ee\n\t"
      "v_mfma_f32_16x16x32_bf16 v[164:167], a[140:143], a[116:119], v[164:167]\n\t"
      "ds_read_b64_tr_b16 a[138:139], v20 offset:12800\n\t"
      "s_add_u32 s64, %16, 65536\n\t"
      "v_mfma_f32_16x16x32_bf16 v[168:171], a[140:143], a[120:123], v[168:171]\n\t"
      "global_load_dwordx4 a[92:95], v1, s[36:37] offset:3072 sc1\n\t"
      "v_mfma_f32_16x16x32_bf16 v[172:175], a[140:143], a[124:127], v[172:175]\n\t"
      "s_waitcnt lgkmcnt(4)\n\t"
      "v_mfma_f32_16x16x32_bf16 v[176:179], a[128:131], a[96:99], v[176:179]\n\t"
      "ds_read_b64_tr_b16 a[140:141], v20 offset:16384\n\t"
      "v_lshrrev_b32 v32, 2, v0\n\t"
      "v_mfma_f32_16x16x32_bf16 v[180:183], a[128:131], a[100:103], v[180:183]\n\t"
      "ds_read_b64_tr_b16 a[142:143], v20 offset:16896\n\t"
      "s_add_u32 m0, s63, 57344\n\t"
      "v_mfma_f32_16x16x32_bf16 v[184:187], a[128:131], a[104:107], v[184:187]\n\t"
      "global_load_lds_dwordx4 v4, s[42:43]\n\t"
      "v_mfma_f32_16x16x32_bf16 v[188:191], a[128:131], a[108:111], v[188:191]\n\t"
      "s_waitcnt lgkmcnt(4)\n\t"
      "v_mfma_f32_16x16x32_bf16 v[176:179], a[132:135], a[112:115], v[176:179]\n\t"
      "ds_read_b64_tr_b16 a[128:129], v21 offset:20480\n\t"
      "v_and_b32 v33, 3, v0\n\t"
      "v_mfma_f32_16x16x32_bf16 v[180:183], a[132:135], a[116:119], v[180:183]\n\t"
      "ds_read_b64_tr_b16 a[130:131], v21 offset:20736\n\t"
      "v_lshl_add_u32 v33, v33, 3, s64\n\t"
      "v_mfma_f32_16x16x32_bf16 v[184:187], a[132:135], a[120:123], v[184:187]\n\t"
      "v_lshl_add_u32 v33, v32, 7, v33\n\t"
      "v_mfma_f32_16x16x32_bf16 v[188:191], a[132:135], a[124:127], v[188:191]\n\t"
      "s_waitcnt lgkmcnt(4)\n\t"
      "v_mfma_f32_16x16x32_bf16 v[192:195], a[136:139], a[96:99], v[192:195]\n\t"
      "ds_read_b64_tr_b16 a[132:133], v21 offset:22528\n\t"
      "v_bfe_u32 v32, v0, 3, 2\n\t"
      "v_mfma_f32_16x16x32_bf16 v[196:199], a[136:139], a[100:103], v[196:199]\n\t"
      "ds_read_b64_tr_b16 a[134:135], v21 offset:22784\n\t"
      "v_xor_b32 v34, 0, v32\n\t"
      "v_mfma_f32_16x16x32_bf16 v[200:203], a[136:139], a[104:107], v[200:203]\n\t"
      "v_lshl_add_u32 v34, v34, 5, v33\n\t"
      "v_mfma_f32_16x16x32_bf16 v[204:207], a[136:139], a[108:111], v[204:207]\n\t"
      "s_waitcnt lgkmcnt(4)\n\t"
      "v_mfma_f32_16x16x32_bf16 v[192:195], a[140:143], a[112:115], v[192:195]\n\t"
      "ds_read_b64_tr_b16 a[136:137], v22 offset:20480\n\t"
      "v_accvgpr_write_b32 a206, v34\n\t"
      "v_mfma_f32_16x16x32_bf16 v[196:199], a[140:143], a[116:119], v[196:199]\n\t"
      "ds_read_b64_tr_b16 a[138:139], v22 offset:20736\n\t"
      "s_add_u32 m0, s62, 61440\n\t"
      "v_mfma_f32_16x16x32_bf16 v[200:203], a[140:143], a[120:123], v[200:203]\n\t"
      "global_load_lds_dwordx4 v5, s[52:53]\n\t"
      "v_mfma_f32_16x16x32_bf16 v[204:207], a[140:143], a[124:127], v[204:207]\n\t"
      "s_waitcnt lgkmcnt(4)\n\t"
      "v_mfma_f32_16x16x32_bf16 v[208:211], a[128:131], a[96:99], v[208:211]\n\t"
      "ds_read_b64_tr_b16 a[140:141], v22 offset:22528\n\t"
      "v_xor_b32 v34, 1, v32\n\t"
      "v_mfma_f32_16x16x32_bf16 v[212:215], a[128:131], a[100:103], v[212:215]\n\t"
      "ds_read_b64_tr_b16 a[142:143], v22 offset:22784\n\t"
      "s_add_u32 m0, s62, 62464\n\t"
      "v_mfma_f32_16x16x32_bf16 v[216:219], a[128:131], a[104:107], v[216:219]\n\t"
      "global_load_lds_dwordx4 v6, s[52:53]\n\t"
      "v_mfma_f32_16x16x32_bf16 v[220:223], a[128:131], a[108:111], v[220:223]\n\t"
      "s_waitcnt lgkmcnt(4)\n\t"
      "v_mfma_f32_16x16x32_bf16 v[208:211], a[132:135], a[112:115], v[208:211]\n\t"
      "ds_read_b64_tr_b16 a[128:129], v23 offset:24576\n\t"
      "v_lshl_add_u32 v34, v34, 5, v33\n\t"
      "v_mfma_f32_16x16x32_bf16 v[212:215], a[132:135], a[116:119], v[212:215]\n\t"
      "ds_read_b64_tr_b16 a[130:131], v23 offset:24832\n\t"
      "v_accvgpr_write_b32 a207, v34\n\t"
      "v_mfma_f32_16x16x32_bf16 v[216:219], a[132:135], a[120:123], v[216:219]\n\t"
      "v_xor_b32 v34, 2, v32\n\t"
      "v_mfma_f32_16x16x32_bf16 v[220:223], a[132:135], a[124:127], v[220:223]\n\t"
      "s_waitcnt lgkmcnt(4)\n\t"
      "v_mfma_f32_16x16x32_bf16 v[224:227], a[136:139], a[96:99], v[224:227]\n\t"
      "ds_read_b64_tr_b16 a[132:133], v23 offset:26624\n\t"
      "v_lshl_add_u32 v34, v34, 5, v33\n\t"
      "v_mfma_f32_16x16x32_bf16 v[228:231], a[136:139], a[100:103], v[228:231]\n\t"
      "ds_read_b64_tr_b16 a[134:135], v23 offset:26880\n\t"
      "s_add_u32 m0, s63, 69632\n\t"
      "v_mfma_f32_16x16x32_bf16 v[232:235], a[136:139], a[104:107], v[232:235]\n\t"
      "global_load_lds_dwordx4 v7, s[52:53]\n\t"
      "v_mfma_f32_16x16x32_bf16 v[236:239], a[136:139], a[108:111], v[236:239]\n\t"
      "s_waitcnt lgkmcnt(4)\n\t"
      "v_mfma_f32_16x16x32_bf16 v[224:227], a[140:143], a[112:115], v[224:227]\n\t"
      "ds_read_b64_tr_b16 a[136:137], v21 offset:8192\n\t"
      "v_accvgpr_write_b32 a208, v34\n\t"
      "v_mfma_f32_16x16x32_bf16 v[228:231], a[140:143], a[116:119], v[228:231]\n\t"
      "ds_read_b64_tr_b16 a[138:139], v21 offset:8448\n\t"
      "v_xor_b32 v34, 3, v32\n\t"
      "v_mfma_f32_16x16x32_bf16 v[232:235], a[140:143], a[120:123], v[232:235]\n\t"
      "v_lshl_add_u32 v34, v34, 5, v33\n\t"
      "v_mfma_f32_16x16x32_bf16 v[236:239], a[140:143], a[124:127], v[236:239]\n\t"
      "s_waitcnt lgkmcnt(4)\n\t"
      "v_mfma_f32_16x16x32_bf16 v[240:243], a[128:131], a[96:99], v[240:243]\n\t"
      "ds_read_b64_tr_b16 a[140:141], v21 offset:10240\n\t"
      "v_accvgpr_write_b32 a209, v34\n\t"
      "v_mfma_f32_16x16x32_bf16 v[244:247], a[128:131], a[100:103], v[244:247]\n\t"
      "ds_read_b64_tr_b16 a[142:143], v21 offset:10496\n\t"
      "s_add_u32 m0, s63, 73728\n\t"
      "v_mfma_f32_16x16x32_bf16 v[248:251], a[128:131], a[104:107], v[248:251]\n\t"
      "global_load_lds_dwordx4 v8, s[52:53]\n\t"
      "v_mfma_f32_16x16x32_bf16 v[252:255], a[128:131], a[108:111], v[252:255]\n\t"
      "s_waitcnt lgkmcnt(4)\n\t"
      "v_mfma_f32_16x16x32_bf16 v[240:243], a[132:135], a[112:115], v[240:243]\n\t"
      "ds_read_b64_tr_b16 a[128:129], v22 offset:8192\n\t"
      "v_mfma_f32_16x16x32_bf16 v[244:247], a[132:135], a[116:119], v[244:247]\n\t"
      "ds_read_b64_tr_b16 a[130:131], v22 offset:8448\n\t"
      "v_mfma_f32_16x16x32_bf16 v[248:251], a[132:135], a[120:123], v[248:251]\n\t"
      "v_mfma_f32_16x16x32_bf16 v[252:255], a[132:135], a[124:127], v[252:255]\n\t"
      "s_waitcnt lgkmcnt(4)\n\t"
      "v_mfma_f32_16x16x32_bf16 v[112:115], a[136:139], a[96:99], v[112:115]\n\t"
      "ds_read_b64_tr_b16 a[132:133], v22 offset:10240\n\t"
      "v_mfma_f32_16x16x32_bf16 v[116:119], a[136:139], a[100:103], v[116:119]\n\t"
      "ds_read_b64_tr_b16 a[134:135], v22 offset:10496\n\t"
      "v_mfma_f32_16x16x32_bf16 v[120:123], a[136:139], a[104:107], v[120:123]\n\t"
      "v_mfma_f32_16x16x32_bf16 v[124:127], a[136:139], a[108:111], v[124:127]\n\t"
      "s_lshl_b32 s66, s60, 10\n\t"
      "s_or_b32 s66, s66, %15\n\t"
      "s_or_b32 s66, s66, %14\n\t"
      "s_cmp_eq_u32 s66, 1024\n\t"
      "s_cbranch_scc0 .Lkm_retag0_%=\n\t"
      "v_lshlrev_b32 v46, 9, v0\n\t"
      "s_mov_b32 s66, 52822016\n\t"
      "buffer_load_dwordx2 v[38:39], v46, s[44:47], s66 offen sc1\n\t"
      "s_mov_b32 s66, 52854784\n\t"
      "buffer_load_dwordx2 v[40:41], v46, s[44:47], s66 offen sc1\n\t"
      "s_mov_b32 s66, 52887552\n\t"
      "buffer_load_dwordx2 v[42:43], v46, s[44:47], s66 offen sc1\n\t"
      "s_mov_b32 s66, 52920320\n\t"
      "buffer_load_dwordx2 v[44:45], v46, s[44:47], s66 offen sc1\n\t"
      ".Lkm_retag0_%=:\n\t"
      "s_waitcnt vmcnt(15) lgkmcnt(4)\n\t"
      "s_barrier\n\t"
      "v_mfma_f32_16x16x32_bf16 v[112:115], a[140:143], a[112:115], v[112:115]\n\t"
      "ds_read_b64_tr_b16 a[136:137], v17 offset:28672\n\t"
      "s_add_u32 s36, s36, 8192\n\t"
      "s_addc_u32 s37, s37, 0\n\t"
      "v_mfma_f32_16x16x32_bf16 v[116:119], a[140:143], a[116:119], v[116:119]\n\t"
      "ds_read_b64_tr_b16 a[138:139], v17 offset:29184\n\t"
      "s_add_u32 s40, s40, 393216\n\t"
      "s_addc_u32 s41, s41, 0\n\t"
      "v_mfma_f32_16x16x32_bf16 v[120:123], a[140:143], a[120:123], v[120:123]\n\t"
      "s_add_u32 s42, s42, 393216\n\t"
      "s_addc_u32 s43, s43, 0\n\t"
      "v_mfma_f32_16x16x32_bf16 v[124:127], a[140:143], a[124:127], v[124:127]\n\t"
      "s_add_u32 s52, s52, 458752\n\t"
      "s_addc_u32 s53, s53, 0\n\t"
      "s_waitcnt lgkmcnt(4)\n\t"
      "v_mfma_f32_16x16x32_bf16 v[128:131], a[128:131], a[96:99], v[128:131]\n\t"
      "ds_read_b64_tr_b16 a[140:141], v17 offset:32768\n\t"
      "s_cmp_eq_u32 s60, 1\n\t"
      "s_cselect_b32 s56, 0, -1\n\t"
      "v_mfma_f32_16x16x32_bf16 v[132:135], a[128:131], a[100:103], v[132:135]\n\t"
      "ds_read_b64_tr_b16 a[142:143], v17 offset:33280\n\t"
      "s_or_b32 s56, s56, 0xfc000000\n\t"
      "v_mfma_f32_16x16x32_bf16 v[136:139], a[128:131], a[104:107], v[136:139]\n\t"
      "s_and_b32 s56, s56, 0xffffffff\n\t"
      "v_mfma_f32_16x16x32_bf16 v[140:143], a[128:131], a[108:111], v[140:143]\n\t"
      "s_mov_b64 exec, s[56:57]\n\t"
      "global_load_dword v31, v[28:29], off\n\t"
      "s_mov_b64 exec, -1\n\t"
      "s_waitcnt lgkmcnt(4)\n\t"
      "v_mfma_f32_16x16x32_bf16 v[128:131], a[132:135], a[112:115], v[128:131]\n\t"
      "ds_read_b64_tr_b16 a[128:129], v18 offset:28672\n\t"
      "v_add_co_u32 v28, vcc, v28, v30\n\t"
      "v_addc_co_u32 v29, vcc, 0, v29, vcc\n\t"
      "v_mfma_f32_16x16x32_bf16 v[132:135], a[132:135], a[116:119], v[132:135]\n\t"
      "ds_read_b64_tr_b16 a[130:131], v18 offset:29184\n\t"
      "v_mfma_f32_16x16x32_bf16 v[136:139], a[132:135], a[120:123], v[136:139]\n\t"
      "v_mfma_f32_16x16x32_bf16 v[140:143], a[132:135], a[124:127], v[140:143]\n\t"
      "s_waitcnt lgkmcnt(4)\n\t"
      "v_mfma_f32_16x16x32_bf16 v[48:51], a[136:139], a[32:35], v[48:51]\n\t"
      "ds_read_b64_tr_b16 a[132:133], v18 offset:32768\n\t"
      "s_lshl_b32 s64, %17, 2\n\t"
      "v_mfma_f32_16x16x32_bf16 v[52:55], a[136:139], a[36:39], v[52:55]\n\t"
      "ds_read_b64_tr_b16 a[134:135], v18 offset:33280\n\t"
      "s_add_u32 s64, s64, %14\n\t"
      "v_mfma_f32_16x16x32_bf16 v[56:59], a[136:139], a[40:43], v[56:59]\n\t"
      "s_lshl_b32 s64, s64, 2\n\t"
      "v_mfma_f32_16x16x32_bf16 v[60:63], a[136:139], a[44:47], v[60:63]\n\t"
      "s_waitcnt lgkmcnt(4)\n\t"
      "v_mfma_f32_16x16x32_bf16 v[48:51], a[140:143], a[48:51], v[48:51]\n\t"
      "ds_read_b64_tr_b16 a[136:137], v19 offset:28672\n\t"
      "s_add_u32 s64, s64, %19\n\t"
      "v_mfma_f32_16x16x32_bf16 v[52:55], a[140:143], a[52:55], v[52:55]\n\t"
      "ds_read_b64_tr_b16 a[138:139], v19 offset:29184\n\t"
      "s_lshl_b32 s65, s64, 10\n\t"
      "v_mfma_f32_16x16x32_bf16 v[56:59], a[140:143], a[56:59], v[56:59]\n\t"
      "s_add_u32 s83, s65, 52559872\n\t"
      "v_mfma_f32_16x16x32_bf16 v[60:63], a[140:143], a[60:63], v[60:63]\n\t"
      "s_waitcnt lgkmcnt(4)\n\t"
      "v_mfma_f32_16x16x32_bf16 v[64:67], a[128:131], a[32:35], v[64:67]\n\t"
      "ds_read_b64_tr_b16 a[140:141], v19 offset:32768\n\t"
      "s_lshl_b32 s66, %20, 7\n\t"
      "v_mfma_f32_16x16x32_bf16 v[68:71], a[128:131], a[36:39], v[68:71]\n\t"
      "ds_read_b64_tr_b16 a[142:143], v19 offset:33280\n\t"
      "s_add_u32 s70, s83, s66\n\t"
      "v_mfma_f32_16x16x32_bf16 v[72:75], a[128:131], a[40:43], v[72:75]\n\t"
      "s_add_u32 s75, s70, 131072\n\t"
      "v_mfma_f32_16x16x32_bf16 v[76:79], a[128:131], a[44:47], v[76:79]\n\t"
      "s_waitcnt lgkmcnt(4)\n\t"
      "v_mfma_f32_16x16x32_bf16 v[64:67], a[132:135], a[48:51], v[64:67]\n\t"
      "ds_read_b64_tr_b16 a[128:129], v20 offset:28672\n\t"
      "s_mul_i32 s64, s64, 147456\n\t"
      "v_mfma_f32_16x16x32_bf16 v[68:71], a[132:135], a[52:55], v[68:71]\n\t"
      "ds_read_b64_tr_b16 a[130:131], v20 offset:29184\n\t"
      "s_add_u32 s64, s64, 16777216\n\t"
      "v_mfma_f32_16x16x32_bf16 v[72:75], a[132:135], a[56:59], v[72:75]\n\t"
      "s_lshl_b32 s65, %20, 12\n\t"
      "v_mfma_f32_16x16x32_bf16 v[76:79], a[132:135], a[60:63], v[76:79]\n\t"
      "s_waitcnt lgkmcnt(4)\n\t"
      "v_mfma_f32_16x16x32_bf16 v[80:83], a[136:139], a[32:35], v[80:83]\n\t"
      "ds_read_b64_tr_b16 a[132:133], v20 offset:32768\n\t"
      "s_add_u32 s65, s65, s64\n\t"
      "v_mfma_f32_16x16x32_bf16 v[84:87], a[136:139], a[36:39], v[84:87]\n\t"
      "ds_read_b64_tr_b16 a[134:135], v20 offset:33280\n\t"
      "s_add_u32 s82, s65, 114688\n\t"
      "v_mfma_f32_16x16x32_bf16 v[88:91], a[136:139], a[40:43], v[88:91]\n\t"
      "s_mul_i32 s65, %19, 114688\n\t"
      "v_mfma_f32_16x16x32_bf16 v[92:95], a[136:139], a[44:47], v[92:95]\n\t"
      "s_waitcnt lgkmcnt(4)\n\t"
      "v_mfma_f32_16x16x32_bf16 v[80:83], a[140:143], a[48:51], v[80:83]\n\t"
      "ds_read_b64_tr_b16 a[136:137], v17 offset:40960\n\t"
      "s_sub_u32 s64, s64, s65\n\t"
      "v_mfma_f32_16x16x32_bf16 v[84:87], a[140:143], a[52:55], v[84:87]\n\t"
      "ds_read_b64_tr_b16 a[138:139], v17 offset:41472\n\t"
      "s_lshr_b32 s65, %21, 6\n\t"
      "v_mfma_f32_16x16x32_bf16 v[88:91], a[140:143], a[56:59], v[88:91]\n\t"
      "s_lshl_b32 s65, s65, 12\n\t"
      "v_mfma_f32_16x16x32_bf16 v[92:95], a[140:143], a[60:63], v[92:95]\n\t"
      "s_waitcnt lgkmcnt(4)\n\t"
      "v_mfma_f32_16x16x32_bf16 v[96:99], a[128:131], a[32:35], v[96:99]\n\t"
      "ds_read_b64_tr_b16 a[140:141], v17 offset:45056\n\t"
      "s_add_u32 s71, s64, s65\n\t"
      "v_mfma_f32_16x16x32_bf16 v[100:103], a[128:131], a[36:39], v[100:103]\n\t"
      "ds_read_b64_tr_b16 a[142:143], v17 offset:45568\n\t"
      "s_lshr_b32 s65, %22, 6\n\t"
      "v_mfma_f32_16x16x32_bf16 v[104:107], a[128:131], a[40:43], v[104:107]\n\t"
      "s_lshl_b32 s65, s65, 12\n\t"
      "v_mfma_f32_16x16x32_bf16 v[108:111], a[128:131], a[44:47], v[108:111]\n\t"
      "s_waitcnt lgkmcnt(4)\n\t"
      "v_mfma_f32_16x16x32_bf16 v[96:99], a[132:135], a[48:51], v[96:99]\n\t"
      "ds_read_b64_tr_b16 a[128:129], v18 offset:40960\n\t"
      "s_add_u32 s73, s64, s65\n\t"
      "v_mfma_f32_16x16x32_bf16 v[100:103], a[132:135], a[52:55], v[100:103]\n\t"
      "ds_read_b64_tr_b16 a[130:131], v18 offset:41472\n\t"
      "s_lshr_b32 s65, %23, 6\n\t"
      "v_mfma_f32_16x16x32_bf16 v[104:107], a[132:135], a[56:59], v[104:107]\n\t"
      "s_lshl_b32 s65, s65, 12\n\t"
      "v_mfma_f32_16x16x32_bf16 v[108:111], a[132:135], a[60:63], v[108:111]\n\t"
      "s_waitcnt lgkmcnt(4)\n\t"
      "v_mfma_f32_16x16x32_bf16 v[144:147], a[136:139], a[0:3], v[144:147]\n\t"
      "ds_read_b64_tr_b16 a[132:133], v18 offset:45056\n\t"
      "s_add_u32 s74, s64, s65\n\t"
      "v_mfma_f32_16x16x32_bf16 v[148:151], a[136:139], a[4:7], v[148:151]\n\t"
      "ds_read_b64_tr_b16 a[134:135], v18 offset:45568\n\t"
      "s_add_u32 s72, s71, 4096\n\t"
      "v_mfma_f32_16x16x32_bf16 v[152:155], a[136:139], a[8:11], v[152:155]\n\t"
      "s_and_b32 s65, %18, 1\n\t"
      "v_mfma_f32_16x16x32_bf16 v[156:159], a[136:139], a[12:15], v[156:159]\n\t"
      "s_waitcnt lgkmcnt(4)\n\t"
      "v_mfma_f32_16x16x32_bf16 v[144:147], a[140:143], a[16:19], v[144:147]\n\t"
      "ds_read_b64_tr_b16 a[136:137], v19 offset:40960\n\t"
      "s_lshl_b32 s65, s65, 11\n\t"
      "v_mfma_f32_16x16x32_bf16 v[148:151], a[140:143], a[20:23], v[148:151]\n\t"
      "ds_read_b64_tr_b16 a[138:139], v19 offset:41472\n\t"
      "s_add_u32 s74, s74, s65\n\t"
      "v_mfma_f32_16x16x32_bf16 v[152:155], a[140:143], a[24:27], v[152:155]\n\t"
      "v_mfma_f32_16x16x32_bf16 v[156:159], a[140:143], a[28:31], v[156:159]\n\t"
      "s_waitcnt lgkmcnt(4)\n\t"
      "v_mfma_f32_16x16x32_bf16 v[160:163], a[128:131], a[0:3], v[160:163]\n\t"
      "ds_read_b64_tr_b16 a[140:141], v19 offset:45056\n\t"
      "v_mfma_f32_16x16x32_bf16 v[164:167], a[128:131], a[4:7], v[164:167]\n\t"
      "ds_read_b64_tr_b16 a[142:143], v19 offset:45568\n\t"
      "v_mfma_f32_16x16x32_bf16 v[168:171], a[128:131], a[8:11], v[168:171]\n\t"
      "v_mfma_f32_16x16x32_bf16 v[172:175], a[128:131], a[12:15], v[172:175]\n\t"
      "s_waitcnt lgkmcnt(4)\n\t"
      "v_mfma_f32_16x16x32_bf16 v[160:163], a[132:135], a[16:19], v[160:163]\n\t"
      "ds_read_b64_tr_b16 a[128:129], v20 offset:40960\n\t"
      "v_mfma_f32_16x16x32_bf16 v[164:167], a[132:135], a[20:23], v[164:167]\n\t"
      "ds_read_b64_tr_b16 a[130:131], v20 offset:41472\n\t"
      "s_add_u32 m0, s62, 77824\n\t"
      "v_mfma_f32_16x16x32_bf16 v[168:171], a[132:135], a[24:27], v[168:171]\n\t"
      "global_load_lds_dwordx4 v2, s[40:41]\n\t"
      "v_mfma_f32_16x16x32_bf16 v[172:175], a[132:135], a[28:31], v[172:175]\n\t"
      "s_waitcnt lgkmcnt(4)\n\t"
      "v_mfma_f32_16x16x32_bf16 v[176:179], a[136:139], a[0:3], v[176:179]\n\t"
      "ds_read_b64_tr_b16 a[132:133], v20 offset:45056\n\t"
      "v_mfma_f32_16x16x32_bf16 v[180:183], a[136:139], a[4:7], v[180:183]\n\t"
      "ds_read_b64_tr_b16 a[134:135], v20 offset:45568\n\t"
      "v_mfma_f32_16x16x32_bf16 v[184:187], a[136:139], a[8:11], v[184:187]\n\t"
      "global_load_dwordx4 a[96:99], v1, s[36:37] offset:-4096 sc1\n\t"
      "v_mfma_f32_16x16x32_bf16 v[188:191], a[136:139], a[12:15], v[188:191]\n\t"
      "s_waitcnt lgkmcnt(4)\n\t"
      "v_mfma_f32_16x16x32_bf16 v[176:179], a[140:143], a[16:19], v[176:179]\n\t"
      "ds_read_b64_tr_b16 a[136:137], v21 offset:49152\n\t"
      "v_mfma_f32_16x16x32_bf16 v[180:183], a[140:143], a[20:23], v[180:183]\n\t"
      "ds_read_b64_tr_b16 a[138:139], v21 offset:49408\n\t"
      "v_mfma_f32_16x16x32_bf16 v[184:187], a[140:143], a[24:27], v[184:187]\n\t"
      "global_load_dwordx4 a[100:103], v1, s[36:37] offset:-3072 sc1\n\t"
      "v_mfma_f32_16x16x32_bf16 v[188:191], a[140:143], a[28:31], v[188:191]\n\t"
      "s_waitcnt lgkmcnt(4)\n\t"
      "v_mfma_f32_16x16x32_bf16 v[192:195], a[128:131], a[0:3], v[192:195]\n\t"
      "ds_read_b64_tr_b16 a[140:141], v21 offset:51200\n\t"
      "v_mfma_f32_16x16x32_bf16 v[196:199], a[128:131], a[4:7], v[196:199]\n\t"
      "ds_read_b64_tr_b16 a[142:143], v21 offset:51456\n\t"
      "v_mfma_f32_16x16x32_bf16 v[200:203], a[128:131], a[8:11], v[200:203]\n\t"
      "global_load_dwordx4 a[104:107], v1, s[36:37] offset:-2048 sc1\n\t"
      "v_mfma_f32_16x16x32_bf16 v[204:207], a[128:131], a[12:15], v[204:207]\n\t"
      "s_waitcnt lgkmcnt(4)\n\t"
      "v_mfma_f32_16x16x32_bf16 v[192:195], a[132:135], a[16:19], v[192:195]\n\t"
      "ds_read_b64_tr_b16 a[128:129], v22 offset:49152\n\t"
      "v_mfma_f32_16x16x32_bf16 v[196:199], a[132:135], a[20:23], v[196:199]\n\t"
      "ds_read_b64_tr_b16 a[130:131], v22 offset:49408\n\t"
      "v_mfma_f32_16x16x32_bf16 v[200:203], a[132:135], a[24:27], v[200:203]\n\t"
      "global_load_dwordx4 a[108:111], v1, s[36:37] offset:-1024 sc1\n\t"
      "v_mfma_f32_16x16x32_bf16 v[204:207], a[132:135], a[28:31], v[204:207]\n\t"
      "s_waitcnt lgkmcnt(4)\n\t"
      "v_mfma_f32_16x16x32_bf16 v[208:211], a[136:139], a[0:3], v[208:211]\n\t"
      "ds_read_b64_tr_b16 a[132:133], v22 offset:51200\n\t"
      "v_mfma_f32_16x16x32_bf16 v[212:215], a[136:139], a[4:7], v[212:215]\n\t"
      "ds_read_b64_tr_b16 a[134:135], v22 offset:51456\n\t"
      "s_add_u32 m0, s62, 78848\n\t"
      "v_mfma_f32_16x16x32_bf16 v[216:219], a[136:139], a[8:11], v[216:219]\n\t"
      "global_load_lds_dwordx4 v3, s[40:41]\n\t"
      "v_mfma_f32_16x16x32_bf16 v[220:223], a[136:139], a[12:15], v[220:223]\n\t"
      "s_waitcnt lgkmcnt(4)\n\t"
      "v_mfma_f32_16x16x32_bf16 v[208:211], a[140:143], a[16:19], v[208:211]\n\t"
      "ds_read_b64_tr_b16 a[136:137], v23 offset:53248\n\t"
      "v_mfma_f32_16x16x32_bf16 v[212:215], a[140:143], a[20:23], v[212:215]\n\t"
      "ds_read_b64_tr_b16 a[138:139], v23 offset:53504\n\t"
      "v_mfma_f32_16x16x32_bf16 v[216:219], a[140:143], a[24:27], v[216:219]\n\t"
      "global_load_dwordx4 a[112:115], v1, s[36:37] offset:0 sc1\n\t"
      "v_mfma_f32_16x16x32_bf16 v[220:223], a[140:143], a[28:31], v[220:223]\n\t"
      "s_waitcnt lgkmcnt(4)\n\t"
      "v_mfma_f32_16x16x32_bf16 v[224:227], a[128:131], a[0:3], v[224:227]\n\t"
      "ds_read_b64_tr_b16 a[140:141], v23 offset:55296\n\t"
      "v_mfma_f32_16x16x32_bf16 v[228:231], a[128:131], a[4:7], v[228:231]\n\t"
      "ds_read_b64_tr_b16 a[142:143], v23 offset:55552\n\t"
      "v_mfma_f32_16x16x32_bf16 v[232:235], a[128:131], a[8:11], v[232:235]\n\t"
      "global_load_dwordx4 a[116:119], v1, s[36:37] offset:1024 sc1\n\t"
      "v_mfma_f32_16x16x32_bf16 v[236:239], a[128:131], a[12:15], v[236:239]\n\t"
      "s_waitcnt lgkmcnt(4)\n\t"
      "v_mfma_f32_16x16x32_bf16 v[224:227], a[132:135], a[16:19], v[224:227]\n\t"
      "ds_read_b64_tr_b16 a[128:129], v21 offset:36864\n\t"
      "v_mfma_f32_16x16x32_bf16 v[228:231], a[132:135], a[20:23], v[228:231]\n\t"
      "ds_read_b64_tr_b16 a[130:131], v21 offset:37120\n\t"
      "v_mfma_f32_16x16x32_bf16 v[232:235], a[132:135], a[24:27], v[232:235]\n\t"
      "global_load_dwordx4 a[120:123], v1, s[36:37] offset:2048 sc1\n\t"
      "v_mfma_f32_16x16x32_bf16 v[236:239], a[132:135], a[28:31], v[236:239]\n\t"
      "s_waitcnt lgkmcnt(4)\n\t"
      "v_mfma_f32_16x16x32_bf16 v[240:243], a[136:139], a[0:3], v[240:243]\n\t"
      "ds_read_b64_tr_b16 a[132:133], v21 offset:38912\n\t"
      "v_mfma_f32_16x16x32_bf16 v[244:247], a[136:139], a[4:7], v[244:247]\n\t"
      "ds_read_b64_tr_b16 a[134:135], v21 offset:39168\n\t"
      "v_mfma_f32_16x16x32_bf16 v[248:251], a[136:139], a[8:11], v[248:251]\n\t"
      "global_load_dwordx4 a[124:127], v1, s[36:37] offset:3072 sc1\n\t"
      "v_mfma_f32_16x16x32_bf16 v[252:255], a[136:139], a[12:15], v[252:255]\n\t"
      "s_waitcnt lgkmcnt(4)\n\t"
      "v_mfma_f32_16x16x32_bf16 v[240:243], a[140:143], a[16:19], v[240:243]\n\t"
      "ds_read_b64_tr_b16 a[136:137], v22 offset:36864\n\t"
      "v_mfma_f32_16x16x32_bf16 v[244:247], a[140:143], a[20:23], v[244:247]\n\t"
      "ds_read_b64_tr_b16 a[138:139], v22 offset:37120\n\t"
      "s_add_u32 m0, s63, 86016\n\t"
      "v_mfma_f32_16x16x32_bf16 v[248:251], a[140:143], a[24:27], v[248:251]\n\t"
      "global_load_lds_dwordx4 v4, s[42:43]\n\t"
      "v_mfma_f32_16x16x32_bf16 v[252:255], a[140:143], a[28:31], v[252:255]\n\t"
      "s_waitcnt lgkmcnt(4)\n\t"
      "v_mfma_f32_16x16x32_bf16 v[112:115], a[128:131], a[0:3], v[112:115]\n\t"
      "ds_read_b64_tr_b16 a[140:141], v22 offset:38912\n\t"
      "v_mfma_f32_16x16x32_bf16 v[116:119], a[128:131], a[4:7], v[116:119]\n\t"
      "ds_read_b64_tr_b16 a[142:143], v22 offset:39168\n\t"
      "v_mfma_f32_16x16x32_bf16 v[120:123], a[128:131], a[8:11], v[120:123]\n\t"
      "v_mfma_f32_16x16x32_bf16 v[124:127], a[128:131], a[12:15], v[124:127]\n\t"
      "s_lshl_b32 s66, s60, 10\n\t"
      "s_or_b32 s66, s66, %15\n\t"
      "s_or_b32 s66, s66, %14\n\t"
      "s_cmp_eq_u32 s66, 1024\n\t"
      "s_cbranch_scc0 .Lkm_retag1_%=\n\t"
      "s_waitcnt vmcnt(8)\n\t"
      "s_mov_b32 s66, 52822016\n\t"
      "v_cmp_eq_u64 vcc, s[58:59], v[38:39]\n\t"
      "s_nop 1\n\t"
      "s_cmp_eq_u64 vcc, -1\n\t"
      "s_cbranch_scc1 .Lkm_t0_ok_%=\n\t"
      "s_mov_b32 s67, 0\n\t"
      ".Lkm_t0_spin_%=:\n\t"
      "s_sleep 1\n\t"
      "buffer_load_dwordx2 v[38:39], v46, s[44:47], s66 offen sc1\n\t"
      "s_add_u32 s67, s67, 1\n\t"
      "s_cmp_ge_u32 s67, 262144\n\t"
      "s_cbranch_scc1 .Lkm_t0_ok_%=\n\t"
      "s_waitcnt vmcnt(0)\n\t"
      "v_cmp_eq_u64 vcc, s[58:59], v[38:39]\n\t"
      "s_nop 1\n\t"
      "s_cmp_eq_u64 vcc, -1\n\t"
      "s_cbranch_scc0 .Lkm_t0_spin_%=\n\t"
      ".Lkm_t0_ok_%=:\n\t"
      "s_mov_b32 s66, 52854784\n\t"
      "v_cmp_eq_u64 vcc, s[58:59], v[40:41]\n\t"
      "s_nop 1\n\t"
      "s_cmp_eq_u64 vcc, -1\n\t"
      "s_cbranch_scc1 .Lkm_t1_ok_%=\n\t"
      "s_mov_b32 s67, 0\n\t"
      ".Lkm_t1_spin_%=:\n\t"
      "s_sleep 1\n\t"
      "buffer_load_dwordx2 v[40:41], v46, s[44:47], s66 offen sc1\n\t"
      "s_add_u32 s67, s67, 1\n\t"
      "s_cmp_ge_u32 s67, 262144\n\t"
      "s_cbranch_scc1 .Lkm_t1_ok_%=\n\t"
      "s_waitcnt vmcnt(0)\n\t"
      "v_cmp_eq_u64 vcc, s[58:59], v[40:41]\n\t"
      "s_nop 1\n\t"
      "s_cmp_eq_u64 vcc, -1\n\t"
      "s_cbranch_scc0 .Lkm_t1_spin_%=\n\t"
      ".Lkm_t1_ok_%=:\n\t"
      "s_mov_b32 s66, 52887552\n\t"
      "v_cmp_eq_u64 vcc, s[58:59], v[42:43]\n\t"
      "s_nop 1\n\t"
      "s_cmp_eq_u64 vcc, -1\n\t"
      "s_cbranch_scc1 .Lkm_t2_ok_%=\n\t"
      "s_mov_b32 s67, 0\n\t"
      ".Lkm_t2_spin_%=:\n\t"
      "s_sleep 1\n\t"
      "buffer_load_dwordx2 v[42:43], v46, s[44:47], s66 offen sc1\n\t"
      "s_add_u32 s67, s67, 1\n\t"
      "s_cmp_ge_u32 s67, 262144\n\t"
      "s_cbranch_scc1 .Lkm_t2_ok_%=\n\t"
      "s_waitcnt vmcnt(0)\n\t"
      "v_cmp_eq_u64 vcc, s[58:59], v[42:43]\n\t"
      "s_nop 1\n\t"
      "s_cmp_eq_u64 vcc, -1\n\t"
      "s_cbranch_scc0 .Lkm_t2_spin_%=\n\t"
      ".Lkm_t2_ok_%=:\n\t"
      "s_mov_b32 s66, 52920320\n\t"
      "v_cmp_eq_u64 vcc, s[58:59], v[44:45]\n\t"
      "s_nop 1\n\t"
      "s_cmp_eq_u64 vcc, -1\n\t"
      "s_cbranch_scc1 .Lkm_t3_ok_%=\n\t"
      "s_mov_b32 s67, 0\n\t"
      ".Lkm_t3_spin_%=:\n\t"
      "s_sleep 1\n\t"
      "buffer_load_dwordx2 v[44:45], v46, s[44:47], s66 offen sc1\n\t"
      "s_add_u32 s67, s67, 1\n\t"
      "s_cmp_ge_u32 s67, 262144\n\t"
      "s_cbranch_scc1 .Lkm_t3_ok_%=\n\t"
      "s_waitcnt vmcnt(0)\n\t"
      "v_cmp_eq_u64 vcc, s[58:59], v[44:45]\n\t"
      "s_nop 1\n\t"
      "s_cmp_eq_u64 vcc, -1\n\t"
      "s_cbranch_scc0 .Lkm_t3_spin_%=\n\t"
      ".Lkm_t3_ok_%=:\n\t"
      "s_mov_b32 s66, 52428672\n\t"
      "s_mov_b64 exec, 0xff\n\t"
      "buffer_store_dwordx4 a[252:255], v1, s[44:47], s66 offen sc1\n\t"
      "s_mov_b64 exec, -1\n\t"
      ".Lkm_retag1_%=:\n\t"
      "s_waitcnt vmcnt(12) lgkmcnt(4)\n\t"
      "s_barrier\n\t"
      "v_mfma_f32_16x16x32_bf16 v[112:115], a[132:135], a[16:19], v[112:115]\n\t"
      "ds_read_b64_tr_b16 a[128:129], v10 offset:0\n\t"
      "v_mfma_f32_16x16x32_bf16 v[116:119], a[132:135], a[20:23], v[116:119]\n\t"
      "ds_read_b64_tr_b16 a[130:131], v10 offset:512\n\t"
      "s_add_u32 m0, s62, 90112\n\t"
      "v_mfma_f32_16x16x32_bf16 v[120:123], a[132:135], a[24:27], v[120:123]\n\t"
      "global_load_lds_dwordx4 v5, s[52:53]\n\t"
      "v_mfma_f32_16x16x32_bf16 v[124:127], a[132:135], a[28:31], v[124:127]\n\t"
      "s_waitcnt lgkmcnt(4)\n\t"
      "v_mfma_f32_16x16x32_bf16 v[128:131], a[136:139], a[0:3], v[128:131]\n\t"
      "ds_read_b64_tr_b16 a[132:133], v10 offset:4096\n\t"
      "v_mfma_f32_16x16x32_bf16 v[132:135], a[136:139], a[4:7], v[132:135]\n\t"
      "ds_read_b64_tr_b16 a[134:135], v10 offset:4608\n\t"
      "v_mfma_f32_16x16x32_bf16 v[136:139], a[136:139], a[8:11], v[136:139]\n\t"
      "v_mfma_f32_16x16x32_bf16 v[140:143], a[136:139], a[12:15], v[140:143]\n\t"
      "s_waitcnt lgkmcnt(4)\n\t"
      "v_mfma_f32_16x16x32_bf16 v[128:131], a[140:143], a[16:19], v[128:131]\n\t"
      "ds_read_b64_tr_b16 a[136:137], v11 offset:0\n\t"
      "v_mfma_f32_16x16x32_bf16 v[132:135], a[140:143], a[20:23], v[132:135]\n\t"
      "ds_read_b64_tr_b16 a[138:139], v11 offset:512\n\t"
      "s_add_u32 m0, s62, 91136\n\t"
      "v_mfma_f32_16x16x32_bf16 v[136:139], a[140:143], a[24:27], v[136:139]\n\t"
      "global_load_lds_dwordx4 v6, s[52:53]\n\t"
      "v_mfma_f32_16x16x32_bf16 v[140:143], a[140:143], a[28:31], v[140:143]\n\t"
      "s_waitcnt lgkmcnt(4)\n\t"
      "v_mfma_f32_16x16x32_bf16 v[48:51], a[128:131], a[64:67], v[48:51]\n\t"
      "ds_read_b64_tr_b16 a[140:141], v11 offset:4096\n\t"
      "s_lshl_b32 s64, %14, 15\n\t"
      "v_mfma_f32_16x16x32_bf16 v[52:55], a[128:131], a[68:71], v[52:55]\n\t"
      "ds_read_b64_tr_b16 a[142:143], v11 offset:4608\n\t"
      "s_add_u32 m0, s63, 98304\n\t"
      "v_mfma_f32_16x16x32_bf16 v[56:59], a[128:131], a[72:75], v[56:59]\n\t"
      "global_load_lds_dwordx4 v7, s[52:53]\n\t"
      "v_mfma_f32_16x16x32_bf16 v[60:63], a[128:131], a[76:79], v[60:63]\n\t"
      "s_waitcnt lgkmcnt(4)\n\t"
      "v_mfma_f32_16x16x32_bf16 v[48:51], a[132:135], a[80:83], v[48:51]\n\t"
      "ds_read_b64_tr_b16 a[128:129], v12 offset:0\n\t"
      "v_lshrrev_b32 v32, 3, v0\n\t"
      "v_mfma_f32_16x16x32_bf16 v[52:55], a[132:135], a[84:87], v[52:55]\n\t"
      "ds_read_b64_tr_b16 a[130:131], v12 offset:512\n\t"
      "v_lshlrev_b32 v33, 11, v32\n\t"
      "v_mfma_f32_16x16x32_bf16 v[56:59], a[132:135], a[88:91], v[56:59]\n\t"
      "v_add_u32 v33, s64, v33\n\t"
      "v_mfma_f32_16x16x32_bf16 v[60:63], a[132:135], a[92:95], v[60:63]\n\t"
      "s_waitcnt lgkmcnt(4)\n\t"
      "v_mfma_f32_16x16x32_bf16 v[64:67], a[136:139], a[64:67], v[64:67]\n\t"
      "ds_read_b64_tr_b16 a[132:133], v12 offset:4096\n\t"
      "s_xor_b32 s65, %19, 3\n\t"
      "v_mfma_f32_16x16x32_bf16 v[68:71], a[136:139], a[68:71], v[68:71]\n\t"
      "ds_read_b64_tr_b16 a[134:135], v12 offset:4608\n\t"
      "s_add_u32 m0, s63, 102400\n\t"
      "v_mfma_f32_16x16x32_bf16 v[72:75], a[136:139], a[72:75], v[72:75]\n\t"
      "global_load_lds_dwordx4 v8, s[52:53]\n\t"
      "v_mfma_f32_16x16x32_bf16 v[76:79], a[136:139], a[76:79], v[76:79]\n\t"
      "s_waitcnt lgkmcnt(4)\n\t"
      "v_mfma_f32_16x16x32_bf16 v[64:67], a[140:143], a[80:83], v[64:67]\n\t"
      "ds_read_b64_tr_b16 a[136:137], v13 offset:0\n\t"
      "s_lshl_b32 s65, s65, 2\n\t"
      "v_mfma_f32_16x16x32_bf16 v[68:71], a[140:143], a[84:87], v[68:71]\n\t"
      "ds_read_b64_tr_b16 a[138:139], v13 offset:512\n\t"
      "v_and_b32 v34, 7, v0\n\t"
      "v_mfma_f32_16x16x32_bf16 v[72:75], a[140:143], a[88:91], v[72:75]\n\t"
      "v_and_b32 v32, 6, v32\n\t"
      "v_mfma_f32_16x16x32_bf16 v[76:79], a[140:143], a[92:95], v[76:79]\n\t"
      "s_add_u32 s36, s36, 8192\n\t"
      "s_addc_u32 s37, s37, 0\n\t"
      "s_waitcnt lgkmcnt(4)\n\t"
      "v_mfma_f32_16x16x32_bf16 v[80:83], a[128:131], a[64:67], v[80:83]\n\t"
      "ds_read_b64_tr_b16 a[140:141], v13 offset:4096\n\t"
      "s_add_u32 s40, s40, 393216\n\t"
      "s_addc_u32 s41, s41, 0\n\t"
      "v_mfma_f32_16x16x32_bf16 v[84:87], a[128:131], a[68:71], v[84:87]\n\t"
      "ds_read_b64_tr_b16 a[142:143], v13 offset:4608\n\t"
      "s_add_u32 s42, s42, 393216\n\t"
      "s_addc_u32 s43, s43, 0\n\t"
      "v_mfma_f32_16x16x32_bf16 v[88:91], a[128:131], a[72:75], v[88:91]\n\t"
      "s_add_u32 s52, s52, 458752\n\t"
      "s_addc_u32 s53, s53, 0\n\t"
      "v_mfma_f32_16x16x32_bf16 v[92:95], a[128:131], a[76:79], v[92:95]\n\t"
      "s_waitcnt lgkmcnt(4)\n\t"
      "v_mfma_f32_16x16x32_bf16 v[80:83], a[132:135], a[80:83], v[80:83]\n\t"
      "ds_read_b64_tr_b16 a[128:129], v10 offset:12288\n\t"
      "v_xor_b32 v34, v34, v32\n\t"
      "v_mfma_f32_16x16x32_bf16 v[84:87], a[132:135], a[84:87], v[84:87]\n\t"
      "ds_read_b64_tr_b16 a[130:131], v10 offset:12800\n\t"
      "s_add_u32 m0, s62, 106496\n\t"
      "v_mfma_f32_16x16x32_bf16 v[88:91], a[132:135], a[88:91], v[88:91]\n\t"
      "global_load_lds_dwordx4 v2, s[40:41]\n\t"
      "v_mfma_f32_16x16x32_bf16 v[92:95], a[132:135], a[92:95], v[92:95]\n\t"
      "s_waitcnt lgkmcnt(4)\n\t"
      "v_mfma_f32_16x16x32_bf16 v[96:99], a[136:139], a[64:67], v[96:99]\n\t"
      "ds_read_b64_tr_b16 a[132:133], v10 offset:16384\n\t"
      "v_xor_b32 v34, s65, v34\n\t"
      "v_mfma_f32_16x16x32_bf16 v[100:103], a[136:139], a[68:71], v[100:103]\n\t"
      "ds_read_b64_tr_b16 a[134:135], v10 offset:16896\n\t"
      "v_xor_b32 v32, 0, v34\n\t"
      "v_mfma_f32_16x16x32_bf16 v[104:107], a[136:139], a[72:75], v[104:107]\n\t"
      "global_load_dwordx4 a[0:3], v1, s[36:37] offset:-4096 sc1\n\t"
      "v_mfma_f32_16x16x32_bf16 v[108:111], a[136:139], a[76:79], v[108:111]\n\t"
      "s_waitcnt lgkmcnt(4)\n\t"
      "v_mfma_f32_16x16x32_bf16 v[96:99], a[140:143], a[80:83], v[96:99]\n\t"
      "ds_read_b64_tr_b16 a[136:137], v11 offset:12288\n\t"
      "v_lshl_add_u32 v32, v32, 4, v33\n\t"
      "v_mfma_f32_16x16x32_bf16 v[100:103], a[140:143], a[84:87], v[100:103]\n\t"
      "ds_read_b64_tr_b16 a[138:139], v11 offset:12800\n\t"
      "v_accvgpr_write_b32 a202, v32\n\t"
      "v_mfma_f32_16x16x32_bf16 v[104:107], a[140:143], a[88:91], v[104:107]\n\t"
      "global_load_dwordx4 a[4:7], v1, s[36:37] offset:-3072 sc1\n\t"
      "v_mfma_f32_16x16x32_bf16 v[108:111], a[140:143], a[92:95], v[108:111]\n\t"
      "s_waitcnt lgkmcnt(4)\n\t"
      "v_mfma_f32_16x16x32_bf16 v[144:147], a[128:131], a[32:35], v[144:147]\n\t"
      "ds_read_b64_tr_b16 a[140:141], v11 offset:16384\n\t"
      "v_xor_b32 v32, 0, v34\n\t"
      "v_mfma_f32_16x16x32_bf16 v[148:151], a[128:131], a[36:39], v[148:151]\n\t"
      "ds_read_b64_tr_b16 a[142:143], v11 offset:16896\n\t"
      "v_lshl_add_u32 v32, v32, 4, v33\n\t"
      "v_mfma_f32_16x16x32_bf16 v[152:155], a[128:131], a[40:43], v[152:155]\n\t"
      "global_load_dwordx4 a[8:11], v1, s[36:37] offset:-2048 sc1\n\t"
      "v_mfma_f32_16x16x32_bf16 v[156:159], a[128:131], a[44:47], v[156:159]\n\t"
      "s_waitcnt lgkmcnt(4)\n\t"
      "v_mfma_f32_16x16x32_bf16 v[144:147], a[132:135], a[48:51], v[144:147]\n\t"
      "ds_read_b64_tr_b16 a[128:129], v12 offset:12288\n\t"
      "v_add_u32 v32, 16384, v32\n\t"
      "v_mfma_f32_16x16x32_bf16 v[148:151], a[132:135], a[52:55], v[148:151]\n\t"
      "ds_read_b64_tr_b16 a[130:131], v12 offset:12800\n\t"
      "v_accvgpr_write_b32 a203, v32\n\t"
      "v_mfma_f32_16x16x32_bf16 v[152:155], a[132:135], a[56:59], v[152:155]\n\t"
      "global_load_dwordx4 a[12:15], v1, s[36:37] offset:-1024 sc1\n\t"
      "v_mfma_f32_16x16x32_bf16 v[156:159], a[132:135], a[60:63], v[156:159]\n\t"
      "s_waitcnt lgkmcnt(4)\n\t"
      "v_mfma_f32_16x16x32_bf16 v[160:163], a[136:139], a[32:35], v[160:163]\n\t"
      "ds_read_b64_tr_b16 a[132:133], v12 offset:16384\n\t"
      "v_xor_b32 v32, 8, v34\n\t"
      "v_mfma_f32_16x16x32_bf16 v[164:167], a[136:139], a[36:39], v[164:167]\n\t"
      "ds_read_b64_tr_b16 a[134:135], v12 offset:16896\n\t"
      "s_add_u32 m0, s62, 107520\n\t"
      "v_mfma_f32_16x16x32_bf16 v[168:171], a[136:139], a[40:43], v[168:171]\n\t"
      "global_load_lds_dwordx4 v3, s[40:41]\n\t"
      "v_mfma_f32_16x16x32_bf16 v[172:175], a[136:139], a[44:47], v[172:175]\n\t"
      "s_waitcnt lgkmcnt(4)\n\t"
      "v_mfma_f32_16x16x32_bf16 v[160:163], a[140:143], a[48:51], v[160:163]\n\t"
      "ds_read_b64_tr_b16 a[136:137], v13 offset:12288\n\t"
      "v_lshl_add_u32 v32, v32, 4, v33\n\t"
      "v_mfma_f32_16x16x32_bf16 v[164:167], a[140:143], a[52:55], v[164:167]\n\t"
      "ds_read_b64_tr_b16 a[138:139], v13 offset:12800\n\t"
      "v_accvgpr_write_b32 a204, v32\n\t"
      "v_mfma_f32_16x16x32_bf16 v[168:171], a[140:143], a[56:59], v[168:171]\n\t"
      "global_load_dwordx4 a[16:19], v1, s[36:37] offset:0 sc1\n\t"
      "v_mfma_f32_16x16x32_bf16 v[172:175], a[140:143], a[60:63], v[172:175]\n\t"
      "s_waitcnt lgkmcnt(4)\n\t"
      "v_mfma_f32_16x16x32_bf16 v[176:179], a[128:131], a[32:35], v[176:179]\n\t"
      "ds_read_b64_tr_b16 a[140:141], v13 offset:16384\n\t"
      "v_xor_b32 v32, 8, v34\n\t"
      "v_mfma_f32_16x16x32_bf16 v[180:183], a[128:131], a[36:39], v[180:183]\n\t"
      "ds_read_b64_tr_b16 a[142:143], v13 offset:16896\n\t"
      "v_lshl_add_u32 v32, v32, 4, v33\n\t"
      "v_mfma_f32_16x16x32_bf16 v[184:187], a[128:131], a[40:43], v[184:187]\n\t"
      "global_load_dwordx4 a[20:23], v1, s[36:37] offset:1024 sc1\n\t"
      "v_mfma_f32_16x16x32_bf16 v[188:191], a[128:131], a[44:47], v[188:191]\n\t"
      "s_waitcnt lgkmcnt(4)\n\t"
      "v_mfma_f32_16x16x32_bf16 v[176:179], a[132:135], a[48:51], v[176:179]\n\t"
      "ds_read_b64_tr_b16 a[128:129], v14 offset:20480\n\t"
      "v_add_u32 v32, 16384, v32\n\t"
      "v_mfma_f32_16x16x32_bf16 v[180:183], a[132:135], a[52:55], v[180:183]\n\t"
      "ds_read_b64_tr_b16 a[130:131], v14 offset:20736\n\t"
      "v_accvgpr_write_b32 a205, v32\n\t"
      "v_mfma_f32_16x16x32_bf16 v[184:187], a[132:135], a[56:59], v[184:187]\n\t"
      "global_load_dwordx4 a[24:27], v1, s[36:37] offset:2048 sc1\n\t"
      "v_mfma_f32_16x16x32_bf16 v[188:191], a[132:135], a[60:63], v[188:191]\n\t"
      "s_waitcnt lgkmcnt(4)\n\t"
      "v_mfma_f32_16x16x32_bf16 v[192:195], a[136:139], a[32:35], v[192:195]\n\t"
      "ds_read_b64_tr_b16 a[132:133], v14 offset:22528\n\t"
      "s_lshl_b32 s64, %14, 12\n\t"
      "v_mfma_f32_16x16x32_bf16 v[196:199], a[136:139], a[36:39], v[196:199]\n\t"
      "ds_read_b64_tr_b16 a[134:135], v14 offset:22784\n\t"
      "s_add_u32 s64, s64, %16\n\t"
      "v_mfma_f32_16x16x32_bf16 v[200:203], a[136:139], a[40:43], v[200:203]\n\t"
      "global_load_dwordx4 a[28:31], v1, s[36:37] offset:3072 sc1\n\t"
      "v_mfma_f32_16x16x32_bf16 v[204:207], a[136:139], a[44:47], v[204:207]\n\t"
      "s_waitcnt lgkmcnt(4)\n\t"
      "v_mfma_f32_16x16x32_bf16 v[192:195], a[140:143], a[48:51], v[192:195]\n\t"
      "ds_read_b64_tr_b16 a[136:137], v15 offset:20480\n\t"
      "v_add_u32 v34, s64, v1\n\t"
      "v_mfma_f32_16x16x32_bf16 v[196:199], a[140:143], a[52:55], v[196:199]\n\t"
      "ds_read_b64_tr_b16 a[138:139], v15 offset:20736\n\t"
      "s_add_u32 m0, s63, 114688\n\t"
      "v_mfma_f32_16x16x32_bf16 v[200:203], a[140:143], a[56:59], v[200:203]\n\t"
      "global_load_lds_dwordx4 v4, s[42:43]\n\t"
      "v_mfma_f32_16x16x32_bf16 v[204:207], a[140:143], a[60:63], v[204:207]\n\t"
      "s_waitcnt lgkmcnt(4)\n\t"
      "v_mfma_f32_16x16x32_bf16 v[208:211], a[128:131], a[32:35], v[208:211]\n\t"
      "ds_read_b64_tr_b16 a[140:141], v15 offset:22528\n\t"
      "v_accvgpr_write_b32 a210, v34\n\t"
      "v_mfma_f32_16x16x32_bf16 v[212:215], a[128:131], a[36:39], v[212:215]\n\t"
      "ds_read_b64_tr_b16 a[142:143], v15 offset:22784\n\t"
      "s_and_b32 s65, %18, 1\n\t"
      "v_mfma_f32_16x16x32_bf16 v[216:219], a[128:131], a[40:43], v[216:219]\n\t"
      "s_lshl_b32 s65, s65, 15\n\t"
      "v_mfma_f32_16x16x32_bf16 v[220:223], a[128:131], a[44:47], v[220:223]\n\t"
      "s_waitcnt lgkmcnt(4)\n\t"
      "v_mfma_f32_16x16x32_bf16 v[208:211], a[132:135], a[48:51], v[208:211]\n\t"
      "ds_read_b64_tr_b16 a[128:129], v16 offset:24576\n\t"
      "v_add_u32 v9, s65, v34\n\t"
      "v_mfma_f32_16x16x32_bf16 v[212:215], a[132:135], a[52:55], v[212:215]\n\t"
      "ds_read_b64_tr_b16 a[130:131], v16 offset:24832\n\t"
      "s_add_u32 m0, s62, 118784\n\t"
      "v_mfma_f32_16x16x32_bf16 v[216:219], a[132:135], a[56:59], v[216:219]\n\t"
      "global_load_lds_dwordx4 v5, s[52:53]\n\t"
      "v_mfma_f32_16x16x32_bf16 v[220:223], a[132:135], a[60:63], v[220:223]\n\t"
      "s_waitcnt lgkmcnt(4)\n\t"
      "v_mfma_f32_16x16x32_bf16 v[224:227], a[136:139], a[32:35], v[224:227]\n\t"
      "ds_read_b64_tr_b16 a[132:133], v16 offset:26624\n\t"
      "v_and_b32 v32, 3, v0\n\t"
      "v_mfma_f32_16x16x32_bf16 v[228:231], a[136:139], a[36:39], v[228:231]\n\t"
      "ds_read_b64_tr_b16 a[134:135], v16 offset:26880\n\t"
      "s_add_u32 m0, s62, 119808\n\t"
      "v_mfma_f32_16x16x32_bf16 v[232:235], a[136:139], a[40:43], v[232:235]\n\t"
      "global_load_lds_dwordx4 v6, s[52:53]\n\t"
      "v_mfma_f32_16x16x32_bf16 v[236:239], a[136:139], a[44:47], v[236:239]\n\t"
      "s_waitcnt lgkmcnt(4)\n\t"
      "v_mfma_f32_16x16x32_bf16 v[224:227], a[140:143], a[48:51], v[224:227]\n\t"
      "ds_read_b64_tr_b16 a[136:137], v14 offset:8192\n\t"
      "v_lshrrev_b32 v33, 2, v0\n\t"
      "v_mfma_f32_16x16x32_bf16 v[228:231], a[140:143], a[52:55], v[228:231]\n\t"
      "ds_read_b64_tr_b16 a[138:139], v14 offset:8448\n\t"
      "s_add_u32 m0, s63, 126976\n\t"
      "v_mfma_f32_16x16x32_bf16 v[232:235], a[140:143], a[56:59], v[232:235]\n\t"
      "global_load_lds_dwordx4 v7, s[52:53]\n\t"
      "v_mfma_f32_16x16x32_bf16 v[236:239], a[140:143], a[60:63], v[236:239]\n\t"
      "s_waitcnt lgkmcnt(4)\n\t"
      "v_mfma_f32_16x16x32_bf16 v[240:243], a[128:131], a[32:35], v[240:243]\n\t"
      "ds_read_b64_tr_b16 a[140:141], v14 offset:10240\n\t"
      "v_lshlrev_b32 v33, 4, v33\n\t"
      "v_mfma_f32_16x16x32_bf16 v[244:247], a[128:131], a[36:39], v[244:247]\n\t"
      "ds_read_b64_tr_b16 a[142:143], v14 offset:10496\n\t"
      "v_lshl_add_u32 v33, v32, 8, v33\n\t"
      "v_mfma_f32_16x16x32_bf16 v[248:251], a[128:131], a[40:43], v[248:251]\n\t"
      "v_add_u32 v33, s64, v33\n\t"
      "v_mfma_f32_16x16x32_bf16 v[252:255], a[128:131], a[44:47], v[252:255]\n\t"
      "s_waitcnt lgkmcnt(4)\n\t"
      "v_mfma_f32_16x16x32_bf16 v[240:243], a[132:135], a[48:51], v[240:243]\n\t"
      "ds_read_b64_tr_b16 a[128:129], v15 offset:8192\n\t"
      "v_accvgpr_write_b32 a218, v33\n\t"
      "v_mfma_f32_16x16x32_bf16 v[244:247], a[132:135], a[52:55], v[244:247]\n\t"
      "ds_read_b64_tr_b16 a[130:131], v15 offset:8448\n\t"
      "s_add_u32 m0, s63, 131072\n\t"
      "v_mfma_f32_16x16x32_bf16 v[248:251], a[132:135], a[56:59], v[248:251]\n\t"
      "global_load_lds_dwordx4 v8, s[52:53]\n\t"
      "v_mfma_f32_16x16x32_bf16 v[252:255], a[132:135], a[60:63], v[252:255]\n\t"
      "s_waitcnt lgkmcnt(4)\n\t"
      "v_mfma_f32_16x16x32_bf16 v[112:115], a[136:139], a[32:35], v[112:115]\n\t"
      "ds_read_b64_tr_b16 a[132:133], v15 offset:10240\n\t"
      "v_mfma_f32_16x16x32_bf16 v[116:119], a[136:139], a[36:39], v[116:119]\n\t"
      "ds_read_b64_tr_b16 a[134:135], v15 offset:10496\n\t"
      "v_mfma_f32_16x16x32_bf16 v[120:123], a[136:139], a[40:43], v[120:123]\n\t"
      "v_mfma_f32_16x16x32_bf16 v[124:127], a[136:139], a[44:47], v[124:127]\n\t"
      "s_waitcnt vmcnt(15) lgkmcnt(4)\n\t"
      "s_barrier\n\t"
      "v_mfma_f32_16x16x32_bf16 v[112:115], a[140:143], a[48:51], v[112:115]\n\t"
      "ds_read_b64_tr_b16 a[136:137], v10 offset:28672\n\t"
      "s_add_u32 s36, s36, 8192\n\t"
      "s_addc_u32 s37, s37, 0\n\t"
      "v_mfma_f32_16x16x32_bf16 v[116:119], a[140:143], a[52:55], v[116:119]\n\t"
      "ds_read_b64_tr_b16 a[138:139], v10 offset:29184\n\t"
      "s_add_u32 s40, s40, 393216\n\t"
      "s_addc_u32 s41, s41, 0\n\t"
      "v_mfma_f32_16x16x32_bf16 v[120:123], a[140:143], a[56:59], v[120:123]\n\t"
      "s_add_u32 s42, s42, 393216\n\t"
      "s_addc_u32 s43, s43, 0\n\t"
      "v_mfma_f32_16x16x32_bf16 v[124:127], a[140:143], a[60:63], v[124:127]\n\t"
      "s_add_u32 s52, s52, 458752\n\t"
      "s_addc_u32 s53, s53, 0\n\t"
      "s_waitcnt lgkmcnt(4)\n\t"
      "v_mfma_f32_16x16x32_bf16 v[128:131], a[128:131], a[32:35], v[128:131]\n\t"
      "ds_read_b64_tr_b16 a[140:141], v10 offset:32768\n\t"
      "v_mfma_f32_16x16x32_bf16 v[132:135], a[128:131], a[36:39], v[132:135]\n\t"
      "ds_read_b64_tr_b16 a[142:143], v10 offset:33280\n\t"
      "v_mfma_f32_16x16x32_bf16 v[136:139], a[128:131], a[40:43], v[136:139]\n\t"
      "v_mfma_f32_16x16x32_bf16 v[140:143], a[128:131], a[44:47], v[140:143]\n\t"
      "s_waitcnt lgkmcnt(4)\n\t"
      "v_mfma_f32_16x16x32_bf16 v[128:131], a[132:135], a[48:51], v[128:131]\n\t"
      "ds_read_b64_tr_b16 a[128:129], v11 offset:28672\n\t"
      "v_mfma_f32_16x16x32_bf16 v[132:135], a[132:135], a[52:55], v[132:135]\n\t"
      "ds_read_b64_tr_b16 a[130:131], v11 offset:29184\n\t"
      "v_mfma_f32_16x16x32_bf16 v[136:139], a[132:135], a[56:59], v[136:139]\n\t"
      "v_mfma_f32_16x16x32_bf16 v[140:143], a[132:135], a[60:63], v[140:143]\n\t"
      "s_sub_u32 s60, s60, 1\n\t"
      "s_cmp_lg_u32 s60, 0\n\t"
      "s_cbranch_scc1 .Lkm_loop_%=\n\t"
      "s_waitcnt lgkmcnt(4)\n\t"
      "v_mfma_f32_16x16x32_bf16 v[48:51], a[136:139], a[96:99], v[48:51]\n\t"
      "ds_read_b64_tr_b16 a[132:133], v11 offset:32768\n\t"
      "v_mfma_f32_16x16x32_bf16 v[52:55], a[136:139], a[100:103], v[52:55]\n\t"
      "ds_read_b64_tr_b16 a[134:135], v11 offset:33280\n\t"
      "v_mfma_f32_16x16x32_bf16 v[56:59], a[136:139], a[104:107], v[56:59]\n\t"
      "v_mfma_f32_16x16x32_bf16 v[60:63], a[136:139], a[108:111], v[60:63]\n\t"
      "s_waitcnt lgkmcnt(4)\n\t"
      "v_mfma_f32_16x16x32_bf16 v[48:51], a[140:143], a[112:115], v[48:51]\n\t"
      "ds_read_b64_tr_b16 a[136:137], v12 offset:28672\n\t"
      "v_mfma_f32_16x16x32_bf16 v[52:55], a[140:143], a[116:119], v[52:55]\n\t"
      "ds_read_b64_tr_b16 a[138:139], v12 offset:29184\n\t"
      "v_mfma_f32_16x16x32_bf16 v[56:59], a[140:143], a[120:123], v[56:59]\n\t"
      "v_mfma_f32_16x16x32_bf16 v[60:63], a[140:143], a[124:127], v[60:63]\n\t"
      "s_waitcnt lgkmcnt(4)\n\t"
      "v_mfma_f32_16x16x32_bf16 v[64:67], a[128:131], a[96:99], v[64:67]\n\t"
      "ds_read_b64_tr_b16 a[140:141], v12 offset:32768\n\t"
      "v_mfma_f32_16x16x32_bf16 v[68:71], a[128:131], a[100:103], v[68:71]\n\t"
      "ds_read_b64_tr_b16 a[142:143], v12 offset:33280\n\t"
      "v_mfma_f32_16x16x32_bf16 v[72:75], a[128:131], a[104:107], v[72:75]\n\t"
      "v_mfma_f32_16x16x32_bf16 v[76:79], a[128:131], a[108:111], v[76:79]\n\t"
      "s_waitcnt lgkmcnt(4)\n\t"
      "v_mfma_f32_16x16x32_bf16 v[64:67], a[132:135], a[112:115], v[64:67]\n\t"
      "ds_read_b64_tr_b16 a[128:129], v13 offset:28672\n\t"
      "v_mfma_f32_16x16x32_bf16 v[68:71], a[132:135], a[116:119], v[68:71]\n\t"
      "ds_read_b64_tr_b16 a[130:131], v13 offset:29184\n\t"
      "v_mfma_f32_16x16x32_bf16 v[72:75], a[132:135], a[120:123], v[72:75]\n\t"
      "v_mfma_f32_16x16x32_bf16 v[76:79], a[132:135], a[124:127], v[76:79]\n\t"
      "s_waitcnt lgkmcnt(4)\n\t"
      "v_mfma_f32_16x16x32_bf16 v[80:83], a[136:139], a[96:99], v[80:83]\n\t"
      "ds_read_b64_tr_b16 a[132:133], v13 offset:32768\n\t"
      "v_mfma_f32_16x16x32_bf16 v[84:87], a[136:139], a[100:103], v[84:87]\n\t"
      "ds_read_b64_tr_b16 a[134:135], v13 offset:33280\n\t"
      "v_mfma_f32_16x16x32_bf16 v[88:91], a[136:139], a[104:107], v[88:91]\n\t"
      "v_mfma_f32_16x16x32_bf16 v[92:95], a[136:139], a[108:111], v[92:95]\n\t"
      "s_waitcnt vmcnt(5) lgkmcnt(4)\n\t"
      "s_barrier\n\t"
      "v_mfma_f32_16x16x32_bf16 v[80:83], a[140:143], a[112:115], v[80:83]\n\t"
      "ds_read_b64_tr_b16 a[136:137], v17 offset:0\n\t"
      "v_mfma_f32_16x16x32_bf16 v[84:87], a[140:143], a[116:119], v[84:87]\n\t"
      "ds_read_b64_tr_b16 a[138:139], v17 offset:512\n\t"
      "v_mfma_f32_16x16x32_bf16 v[88:91], a[140:143], a[120:123], v[88:91]\n\t"
      "v_mfma_f32_16x16x32_bf16 v[92:95], a[140:143], a[124:127], v[92:95]\n\t"
      "s_waitcnt lgkmcnt(4)\n\t"
      "v_mfma_f32_16x16x32_bf16 v[96:99], a[128:131], a[96:99], v[96:99]\n\t"
      "ds_read_b64_tr_b16 a[140:141], v17 offset:4096\n\t"
      "v_mfma_f32_16x16x32_bf16 v[100:103], a[128:131], a[100:103], v[100:103]\n\t"
      "ds_read_b64_tr_b16 a[142:143], v17 offset:4608\n\t"
      "s_add_u32 m0, s63, 143360\n\t"
      "v_mfma_f32_16x16x32_bf16 v[104:107], a[128:131], a[104:107], v[104:107]\n\t"
      "global_load_lds_dwordx4 v4, s[42:43]\n\t"
      "v_mfma_f32_16x16x32_bf16 v[108:111], a[128:131], a[108:111], v[108:111]\n\t"
      "s_waitcnt lgkmcnt(4)\n\t"
      "v_mfma_f32_16x16x32_bf16 v[96:99], a[132:135], a[112:115], v[96:99]\n\t"
      "ds_read_b64_tr_b16 a[128:129], v18 offset:0\n\t"
      "v_mfma_f32_16x16x32_bf16 v[100:103], a[132:135], a[116:119], v[100:103]\n\t"
      "ds_read_b64_tr_b16 a[130:131], v18 offset:512\n\t"
      "v_mfma_f32_16x16x32_bf16 v[104:107], a[132:135], a[120:123], v[104:107]\n\t"
      "v_mfma_f32_16x16x32_bf16 v[108:111], a[132:135], a[124:127], v[108:111]\n\t"
      "s_waitcnt lgkmcnt(4)\n\t"
      "v_mfma_f32_16x16x32_bf16 v[48:51], a[136:139], a[0:3], v[48:51]\n\t"
      "ds_read_b64_tr_b16 a[132:133], v18 offset:4096\n\t"
      "v_mfma_f32_16x16x32_bf16 v[52:55], a[136:139], a[4:7], v[52:55]\n\t"
      "ds_read_b64_tr_b16 a[134:135], v18 offset:4608\n\t"
      "s_add_u32 m0, s62, 147456\n\t"
      "v_mfma_f32_16x16x32_bf16 v[56:59], a[136:139], a[8:11], v[56:59]\n\t"
      "global_load_lds_dwordx4 v5, s[52:53]\n\t"
      "v_mfma_f32_16x16x32_bf16 v[60:63], a[136:139], a[12:15], v[60:63]\n\t"
      "s_waitcnt lgkmcnt(4)\n\t"
      "v_mfma_f32_16x16x32_bf16 v[48:51], a[140:143], a[16:19], v[48:51]\n\t"
      "ds_read_b64_tr_b16 a[136:137], v19 offset:0\n\t"
      "v_mfma_f32_16x16x32_bf16 v[52:55], a[140:143], a[20:23], v[52:55]\n\t"
      "ds_read_b64_tr_b16 a[138:139], v19 offset:512\n\t"
      "v_mfma_f32_16x16x32_bf16 v[56:59], a[140:143], a[24:27], v[56:59]\n\t"
      "v_mfma_f32_16x16x32_bf16 v[60:63], a[140:143], a[28:31], v[60:63]\n\t"
      "s_waitcnt lgkmcnt(4)\n\t"
      "v_mfma_f32_16x16x32_bf16 v[64:67], a[128:131], a[0:3], v[64:67]\n\t"
      "ds_read_b64_tr_b16 a[140:141], v19 offset:4096\n\t"
      "v_mfma_f32_16x16x32_bf16 v[68:71], a[128:131], a[4:7], v[68:71]\n\t"
      "ds_read_b64_tr_b16 a[142:143], v19 offset:4608\n\t"
      "s_add_u32 m0, s62, 148480\n\t"
      "v_mfma_f32_16x16x32_bf16 v[72:75], a[128:131], a[8:11], v[72:75]\n\t"
      "global_load_lds_dwordx4 v6, s[52:53]\n\t"
      "v_mfma_f32_16x16x32_bf16 v[76:79], a[128:131], a[12:15], v[76:79]\n\t"
      "s_waitcnt lgkmcnt(4)\n\t"
      "v_mfma_f32_16x16x32_bf16 v[64:67], a[132:135], a[16:19], v[64:67]\n\t"
      "ds_read_b64_tr_b16 a[128:129], v20 offset:0\n\t"
      "v_mfma_f32_16x16x32_bf16 v[68:71], a[132:135], a[20:23], v[68:71]\n\t"
      "ds_read_b64_tr_b16 a[130:131], v20 offset:512\n\t"
      "s_add_u32 m0, s63, 155648\n\t"
      "v_mfma_f32_16x16x32_bf16 v[72:75], a[132:135], a[24:27], v[72:75]\n\t"
      "global_load_lds_dwordx4 v7, s[52:53]\n\t"
      "v_mfma_f32_16x16x32_bf16 v[76:79], a[132:135], a[28:31], v[76:79]\n\t"
      "s_waitcnt lgkmcnt(4)\n\t"
      "v_mfma_f32_16x16x32_bf16 v[80:83], a[136:139], a[0:3], v[80:83]\n\t"
      "ds_read_b64_tr_b16 a[132:133], v20 offset:4096\n\t"
      "v_mfma_f32_16x16x32_bf16 v[84:87], a[136:139], a[4:7], v[84:87]\n\t"
      "ds_read_b64_tr_b16 a[134:135], v20 offset:4608\n\t"
      "v_mfma_f32_16x16x32_bf16 v[88:91], a[136:139], a[8:11], v[88:91]\n\t"
      "v_mfma_f32_16x16x32_bf16 v[92:95], a[136:139], a[12:15], v[92:95]\n\t"
      "s_waitcnt lgkmcnt(4)\n\t"
      "v_mfma_f32_16x16x32_bf16 v[80:83], a[140:143], a[16:19], v[80:83]\n\t"
      "ds_read_b64_tr_b16 a[136:137], v10 offset:40960\n\t"
      "v_cvt_pk_bf16_f32 v48, v48, v49\n\t"
      "v_mfma_f32_16x16x32_bf16 v[84:87], a[140:143], a[20:23], v[84:87]\n\t"
      "ds_read_b64_tr_b16 a[138:139], v10 offset:41472\n\t"
      "v_cvt_pk_bf16_f32 v49, v50, v51\n\t"
      "v_mfma_f32_16x16x32_bf16 v[88:91], a[140:143], a[24:27], v[88:91]\n\t"
      "v_cvt_pk_bf16_f32 v50, v64, v65\n\t"
      "v_cvt_pk_bf16_f32 v51, v66, v67\n\t"
      "v_mfma_f32_16x16x32_bf16 v[92:95], a[140:143], a[28:31], v[92:95]\n\t"
      "buffer_store_dwordx4 v[48:51], v1, s[44:47], s68 offen offset:0 sc1\n\t"
      "s_waitcnt lgkmcnt(4)\n\t"
      "v_mfma_f32_16x16x32_bf16 v[96:99], a[128:131], a[0:3], v[96:99]\n\t"
      "ds_read_b64_tr_b16 a[140:141], v10 offset:45056\n\t"
      "v_cvt_pk_bf16_f32 v52, v52, v53\n\t"
      "v_mfma_f32_16x16x32_bf16 v[100:103], a[128:131], a[4:7], v[100:103]\n\t"
      "ds_read_b64_tr_b16 a[142:143], v10 offset:45568\n\t"
      "s_add_u32 m0, s63, 159744\n\t"
      "v_mfma_f32_16x16x32_bf16 v[104:107], a[128:131], a[8:11], v[104:107]\n\t"
      "global_load_lds_dwordx4 v8, s[52:53]\n\t"
      "v_cvt_pk_bf16_f32 v53, v54, v55\n\t"
      "v_mfma_f32_16x16x32_bf16 v[108:111], a[128:131], a[12:15], v[108:111]\n\t"
      "v_cvt_pk_bf16_f32 v54, v68, v69\n\t"
      "s_waitcnt lgkmcnt(4)\n\t"
      "v_mfma_f32_16x16x32_bf16 v[96:99], a[132:135], a[16:19], v[96:99]\n\t"
      "ds_read_b64_tr_b16 a[128:129], v11 offset:40960\n\t"
      "v_cvt_pk_bf16_f32 v55, v70, v71\n\t"
      "v_mfma_f32_16x16x32_bf16 v[100:103], a[132:135], a[20:23], v[100:103]\n\t"
      "ds_read_b64_tr_b16 a[130:131], v11 offset:41472\n\t"
      "buffer_store_dwordx4 v[52:55], v1, s[44:47], s68 offen offset:1024 sc1\n\t"
      "v_mfma_f32_16x16x32_bf16 v[104:107], a[132:135], a[24:27], v[104:107]\n\t"
      "v_cvt_pk_bf16_f32 v56, v56, v57\n\t"
      "v_cvt_pk_bf16_f32 v57, v58, v59\n\t"
      "v_mfma_f32_16x16x32_bf16 v[108:111], a[132:135], a[28:31], v[108:111]\n\t"
      "v_cvt_pk_bf16_f32 v58, v72, v73\n\t"
      "s_waitcnt lgkmcnt(4)\n\t"
      "v_mfma_f32_16x16x32_bf16 v[144:147], a[136:139], a[64:67], v[144:147]\n\t"
      "ds_read_b64_tr_b16 a[132:133], v11 offset:45056\n\t"
      "v_cvt_pk_bf16_f32 v59, v74, v75\n\t"
      "v_mfma_f32_16x16x32_bf16 v[148:151], a[136:139], a[68:71], v[148:151]\n\t"
      "ds_read_b64_tr_b16 a[134:135], v11 offset:45568\n\t"
      "buffer_store_dwordx4 v[56:59], v1, s[44:47], s68 offen offset:2048 sc1\n\t"
      "v_mfma_f32_16x16x32_bf16 v[152:155], a[136:139], a[72:75], v[152:155]\n\t"
      "v_cvt_pk_bf16_f32 v60, v60, v61\n\t"
      "v_cvt_pk_bf16_f32 v61, v62, v63\n\t"
      "v_mfma_f32_16x16x32_bf16 v[156:159], a[136:139], a[76:79], v[156:159]\n\t"
      "v_cvt_pk_bf16_f32 v62, v76, v77\n\t"
      "s_waitcnt lgkmcnt(4)\n\t"
      "v_mfma_f32_16x16x32_bf16 v[144:147], a[140:143], a[80:83], v[144:147]\n\t"
      "ds_read_b64_tr_b16 a[136:137], v12 offset:40960\n\t"
      "v_cvt_pk_bf16_f32 v63, v78, v79\n\t"
      "v_mfma_f32_16x16x32_bf16 v[148:151], a[140:143], a[84:87], v[148:151]\n\t"
      "ds_read_b64_tr_b16 a[138:139], v12 offset:41472\n\t"
      "buffer_store_dwordx4 v[60:63], v1, s[44:47], s68 offen offset:3072 sc1\n\t"
      "v_mfma_f32_16x16x32_bf16 v[152:155], a[140:143], a[88:91], v[152:155]\n\t"
      "v_cvt_pk_bf16_f32 v80, v80, v81\n\t"
      "v_cvt_pk_bf16_f32 v81, v82, v83\n\t"
      "v_mfma_f32_16x16x32_bf16 v[156:159], a[140:143], a[92:95], v[156:159]\n\t"
      "v_cvt_pk_bf16_f32 v82, v96, v97\n\t"
      "s_waitcnt lgkmcnt(4)\n\t"
      "v_mfma_f32_16x16x32_bf16 v[160:163], a[128:131], a[64:67], v[160:163]\n\t"
      "ds_read_b64_tr_b16 a[140:141], v12 offset:45056\n\t"
      "v_cvt_pk_bf16_f32 v83, v98, v99\n\t"
      "v_mfma_f32_16x16x32_bf16 v[164:167], a[128:131], a[68:71], v[164:167]\n\t"
      "ds_read_b64_tr_b16 a[142:143], v12 offset:45568\n\t"
      "buffer_store_dwordx4 v[80:83], v1, s[44:47], s69 offen offset:0 sc1\n\t"
      "v_mfma_f32_16x16x32_bf16 v[168:171], a[128:131], a[72:75], v[168:171]\n\t"
      "v_cvt_pk_bf16_f32 v84, v84, v85\n\t"
      "v_cvt_pk_bf16_f32 v85, v86, v87\n\t"
      "v_mfma_f32_16x16x32_bf16 v[172:175], a[128:131], a[76:79], v[172:175]\n\t"
      "v_cvt_pk_bf16_f32 v86, v100, v101\n\t"
      "s_waitcnt lgkmcnt(4)\n\t"
      "v_mfma_f32_16x16x32_bf16 v[160:163], a[132:135], a[80:83], v[160:163]\n\t"
      "ds_read_b64_tr_b16 a[128:129], v13 offset:40960\n\t"
      "v_cvt_pk_bf16_f32 v87, v102, v103\n\t"
      "v_mfma_f32_16x16x32_bf16 v[164:167], a[132:135], a[84:87], v[164:167]\n\t"
      "ds_read_b64_tr_b16 a[130:131], v13 offset:41472\n\t"
      "buffer_store_dwordx4 v[84:87], v1, s[44:47], s69 offen offset:1024 sc1\n\t"
      "v_mfma_f32_16x16x32_bf16 v[168:171], a[132:135], a[88:91], v[168:171]\n\t"
      "v_cvt_pk_bf16_f32 v88, v88, v89\n\t"
      "v_cvt_pk_bf16_f32 v89, v90, v91\n\t"
      "v_mfma_f32_16x16x32_bf16 v[172:175], a[132:135], a[92:95], v[172:175]\n\t"
      "v_cvt_pk_bf16_f32 v90, v104, v105\n\t"
      "s_waitcnt lgkmcnt(4)\n\t"
      "v_mfma_f32_16x16x32_bf16 v[176:179], a[136:139], a[64:67], v[176:179]\n\t"
      "ds_read_b64_tr_b16 a[132:133], v13 offset:45056\n\t"
      "v_cvt_pk_bf16_f32 v91, v106, v107\n\t"
      "v_mfma_f32_16x16x32_bf16 v[180:183], a[136:139], a[68:71], v[180:183]\n\t"
      "ds_read_b64_tr_b16 a[134:135], v13 offset:45568\n\t"
      "buffer_store_dwordx4 v[88:91], v1, s[44:47], s69 offen offset:2048 sc1\n\t"
      "v_mfma_f32_16x16x32_bf16 v[184:187], a[136:139], a[72:75], v[184:187]\n\t"
      "v_cvt_pk_bf16_f32 v92, v92, v93\n\t"
      "v_cvt_pk_bf16_f32 v93, v94, v95\n\t"
      "v_mfma_f32_16x16x32_bf16 v[188:191], a[136:139], a[76:79], v[188:191]\n\t"
      "v_cvt_pk_bf16_f32 v94, v108, v109\n\t"
      "s_waitcnt lgkmcnt(4)\n\t"
      "v_mfma_f32_16x16x32_bf16 v[176:179], a[140:143], a[80:83], v[176:179]\n\t"
      "ds_read_b64_tr_b16 a[136:137], v14 offset:49152\n\t"
      "v_cvt_pk_bf16_f32 v95, v110, v111\n\t"
      "v_mfma_f32_16x16x32_bf16 v[180:183], a[140:143], a[84:87], v[180:183]\n\t"
      "ds_read_b64_tr_b16 a[138:139], v14 offset:49408\n\t"
      "buffer_store_dwordx4 v[92:95], v1, s[44:47], s69 offen offset:3072 sc1\n\t"
      "v_mfma_f32_16x16x32_bf16 v[184:187], a[140:143], a[88:91], v[184:187]\n\t"
      "v_mfma_f32_16x16x32_bf16 v[188:191], a[140:143], a[92:95], v[188:191]\n\t"
      "s_waitcnt lgkmcnt(4)\n\t"
      "v_mfma_f32_16x16x32_bf16 v[192:195], a[128:131], a[64:67], v[192:195]\n\t"
      "ds_read_b64_tr_b16 a[140:141], v14 offset:51200\n\t"
      "v_mfma_f32_16x16x32_bf16 v[196:199], a[128:131], a[68:71], v[196:199]\n\t"
      "ds_read_b64_tr_b16 a[142:143], v14 offset:51456\n\t"
      "v_mfma_f32_16x16x32_bf16 v[200:203], a[128:131], a[72:75], v[200:203]\n\t"
      "v_mfma_f32_16x16x32_bf16 v[204:207], a[128:131], a[76:79], v[204:207]\n\t"
      "s_waitcnt lgkmcnt(4)\n\t"
      "v_mfma_f32_16x16x32_bf16 v[192:195], a[132:135], a[80:83], v[192:195]\n\t"
      "ds_read_b64_tr_b16 a[128:129], v15 offset:49152\n\t"
      "v_mfma_f32_16x16x32_bf16 v[196:199], a[132:135], a[84:87], v[196:199]\n\t"
      "ds_read_b64_tr_b16 a[130:131], v15 offset:49408\n\t"
      "v_mfma_f32_16x16x32_bf16 v[200:203], a[132:135], a[88:91], v[200:203]\n\t"
      "v_mfma_f32_16x16x32_bf16 v[204:207], a[132:135], a[92:95], v[204:207]\n\t"
      "s_waitcnt lgkmcnt(4)\n\t"
      "v_mfma_f32_16x16x32_bf16 v[208:211], a[136:139], a[64:67], v[208:211]\n\t"
      "ds_read_b64_tr_b16 a[132:133], v15 offset:51200\n\t"
      "v_mfma_f32_16x16x32_bf16 v[212:215], a[136:139], a[68:71], v[212:215]\n\t"
      "ds_read_b64_tr_b16 a[134:135], v15 offset:51456\n\t"
      "v_mfma_f32_16x16x32_bf16 v[216:219], a[136:139], a[72:75], v[216:219]\n\t"
      "v_mfma_f32_16x16x32_bf16 v[220:223], a[136:139], a[76:79], v[220:223]\n\t"
      "s_waitcnt lgkmcnt(4)\n\t"
      "v_mfma_f32_16x16x32_bf16 v[208:211], a[140:143], a[80:83], v[208:211]\n\t"
      "ds_read_b64_tr_b16 a[136:137], v16 offset:53248\n\t"
      "v_mfma_f32_16x16x32_bf16 v[212:215], a[140:143], a[84:87], v[212:215]\n\t"
      "ds_read_b64_tr_b16 a[138:139], v16 offset:53504\n\t"
      "v_mfma_f32_16x16x32_bf16 v[216:219], a[140:143], a[88:91], v[216:219]\n\t"
      "v_mfma_f32_16x16x32_bf16 v[220:223], a[140:143], a[92:95], v[220:223]\n\t"
      "s_waitcnt lgkmcnt(4)\n\t"
      "v_mfma_f32_16x16x32_bf16 v[224:227], a[128:131], a[64:67], v[224:227]\n\t"
      "ds_read_b64_tr_b16 a[140:141], v16 offset:55296\n\t"
      "v_mfma_f32_16x16x32_bf16 v[228:231], a[128:131], a[68:71], v[228:231]\n\t"
      "ds_read_b64_tr_b16 a[142:143], v16 offset:55552\n\t"
      "v_mfma_f32_16x16x32_bf16 v[232:235], a[128:131], a[72:75], v[232:235]\n\t"
      "v_mfma_f32_16x16x32_bf16 v[236:239], a[128:131], a[76:79], v[236:239]\n\t"
      "s_waitcnt lgkmcnt(4)\n\t"
      "v_mfma_f32_16x16x32_bf16 v[224:227], a[132:135], a[80:83], v[224:227]\n\t"
      "ds_read_b64_tr_b16 a[128:129], v14 offset:36864\n\t"
      "v_mfma_f32_16x16x32_bf16 v[228:231], a[132:135], a[84:87], v[228:231]\n\t"
      "ds_read_b64_tr_b16 a[130:131], v14 offset:37120\n\t"
      "v_mfma_f32_16x16x32_bf16 v[232:235], a[132:135], a[88:91], v[232:235]\n\t"
      "v_mfma_f32_16x16x32_bf16 v[236:239], a[132:135], a[92:95], v[236:239]\n\t"
      "s_waitcnt lgkmcnt(4)\n\t"
      "v_mfma_f32_16x16x32_bf16 v[240:243], a[136:139], a[64:67], v[240:243]\n\t"
      "ds_read_b64_tr_b16 a[132:133], v14 offset:38912\n\t"
      "s_mov_b32 s48, %0\n\t"
      "v_mfma_f32_16x16x32_bf16 v[244:247], a[136:139], a[68:71], v[244:247]\n\t"
      "ds_read_b64_tr_b16 a[134:135], v14 offset:39168\n\t"
      "s_mov_b32 s49, %1\n\t"
      "v_mfma_f32_16x16x32_bf16 v[248:251], a[136:139], a[72:75], v[248:251]\n\t"
      "s_mov_b32 s50, 53477376\n\t"
      "v_mfma_f32_16x16x32_bf16 v[252:255], a[136:139], a[76:79], v[252:255]\n\t"
      "s_waitcnt lgkmcnt(4)\n\t"
      "v_mfma_f32_16x16x32_bf16 v[240:243], a[140:143], a[80:83], v[240:243]\n\t"
      "ds_read_b64_tr_b16 a[136:137], v15 offset:36864\n\t"
      "s_mov_b32 s51, 0x20000\n\t"
      "v_mfma_f32_16x16x32_bf16 v[244:247], a[140:143], a[84:87], v[244:247]\n\t"
      "ds_read_b64_tr_b16 a[138:139], v15 offset:37120\n\t"
      "s_cmp_eq_u32 %17, 0\n\t"
      "s_cselect_b32 s50, 0, s50\n\t"
      "v_mfma_f32_16x16x32_bf16 v[248:251], a[140:143], a[88:91], v[248:251]\n\t"
      "s_lshl_b32 s64, %14, 12\n\t"
      "v_mfma_f32_16x16x32_bf16 v[252:255], a[140:143], a[92:95], v[252:255]\n\t"
      "s_waitcnt lgkmcnt(4)\n\t"
      "v_mfma_f32_16x16x32_bf16 v[112:115], a[128:131], a[64:67], v[112:115]\n\t"
      "ds_read_b64_tr_b16 a[140:141], v15 offset:38912\n\t"
      "s_add_u32 s64, s64, %16\n\t"
      "v_mfma_f32_16x16x32_bf16 v[116:119], a[128:131], a[68:71], v[116:119]\n\t"
      "ds_read_b64_tr_b16 a[142:143], v15 offset:39168\n\t"
      "s_and_b32 s66, %18, 1\n\t"
      "v_mfma_f32_16x16x32_bf16 v[120:123], a[128:131], a[72:75], v[120:123]\n\t"
      "s_lshl_b32 s66, s66, 15\n\t"
      "v_mfma_f32_16x16x32_bf16 v[124:127], a[128:131], a[76:79], v[124:127]\n\t"
      "s_waitcnt vmcnt(7) lgkmcnt(4)\n\t"
      "s_barrier\n\t"
      "v_mfma_f32_16x16x32_bf16 v[112:115], a[132:135], a[80:83], v[112:115]\n\t"
      "ds_read_b64_tr_b16 a[128:129], v17 offset:12288\n\t"
      "s_xor_b32 s66, s66, 32768\n\t"
      "v_mfma_f32_16x16x32_bf16 v[116:119], a[132:135], a[84:87], v[116:119]\n\t"
      "ds_read_b64_tr_b16 a[130:131], v17 offset:12800\n\t"
      "s_add_u32 s62, s64, s66\n\t"
      "v_mfma_f32_16x16x32_bf16 v[120:123], a[132:135], a[88:91], v[120:123]\n\t"
      "s_xor_b32 s66, s68, 4194304\n\t"
      "v_mfma_f32_16x16x32_bf16 v[124:127], a[132:135], a[92:95], v[124:127]\n\t"
      "s_waitcnt lgkmcnt(4)\n\t"
      "v_mfma_f32_16x16x32_bf16 v[128:131], a[136:139], a[64:67], v[128:131]\n\t"
      "ds_read_b64_tr_b16 a[132:133], v17 offset:16384\n\t"
      "s_and_b32 s65, s68, 4290772991\n\t"
      "v_mfma_f32_16x16x32_bf16 v[132:135], a[136:139], a[68:71], v[132:135]\n\t"
      "ds_read_b64_tr_b16 a[134:135], v17 offset:16896\n\t"
      "s_sub_u32 s79, s65, 16384\n\t"
      "v_mfma_f32_16x16x32_bf16 v[136:139], a[136:139], a[72:75], v[136:139]\n\t"
      "s_add_u32 s61, s79, 4194304\n\t"
      "v_mfma_f32_16x16x32_bf16 v[140:143], a[136:139], a[76:79], v[140:143]\n\t"
      "s_waitcnt lgkmcnt(4)\n\t"
      "v_mfma_f32_16x16x32_bf16 v[128:131], a[140:143], a[80:83], v[128:131]\n\t"
      "ds_read_b64_tr_b16 a[136:137], v17 offset:40960\n\t"
      "v_mfma_f32_16x16x32_bf16 v[132:135], a[140:143], a[84:87], v[132:135]\n\t"
      "ds_read_b64_tr_b16 a[138:139], v17 offset:41472\n\t"
      "v_mfma_f32_16x16x32_bf16 v[136:139], a[140:143], a[88:91], v[136:139]\n\t"
      "v_mfma_f32_16x16x32_bf16 v[140:143], a[140:143], a[92:95], v[140:143]\n\t"
      "s_waitcnt lgkmcnt(4)\n\t"
      "v_mfma_f32_16x16x32_bf16 v[144:147], a[128:131], a[96:99], v[144:147]\n\t"
      "ds_read_b64_tr_b16 a[140:141], v17 offset:45056\n\t"
      "v_mfma_f32_16x16x32_bf16 v[148:151], a[128:131], a[100:103], v[148:151]\n\t"
      "ds_read_b64_tr_b16 a[142:143], v17 offset:45568\n\t"
      "v_mfma_f32_16x16x32_bf16 v[152:155], a[128:131], a[104:107], v[152:155]\n\t"
      "v_mfma_f32_16x16x32_bf16 v[156:159], a[128:131], a[108:111], v[156:159]\n\t"
      "s_waitcnt lgkmcnt(4)\n\t"
      "v_mfma_f32_16x16x32_bf16 v[144:147], a[132:135], a[112:115], v[144:147]\n\t"
      "ds_read_b64_tr_b16 a[128:129], v18 offset:12288\n\t"
      "v_mfma_f32_16x16x32_bf16 v[148:151], a[132:135], a[116:119], v[148:151]\n\t"
      "ds_read_b64_tr_b16 a[130:131], v18 offset:12800\n\t"
      "v_mfma_f32_16x16x32_bf16 v[152:155], a[132:135], a[120:123], v[152:155]\n\t"
      "v_mfma_f32_16x16x32_bf16 v[156:159], a[132:135], a[124:127], v[156:159]\n\t"
      "s_waitcnt lgkmcnt(4)\n\t"
      "v_mfma_f32_16x16x32_bf16 v[144:147], a[136:139], a[0:3], v[144:147]\n\t"
      "ds_read_b64_tr_b16 a[132:133], v18 offset:16384\n\t"
      "v_mfma_f32_16x16x32_bf16 v[148:151], a[136:139], a[4:7], v[148:151]\n\t"
      "ds_read_b64_tr_b16 a[134:135], v18 offset:16896\n\t"
      "v_mfma_f32_16x16x32_bf16 v[152:155], a[136:139], a[8:11], v[152:155]\n\t"
      "s_lshl_b32 s56, %20, 8\n\t"
      "v_mfma_f32_16x16x32_bf16 v[156:159], a[136:139], a[12:15], v[156:159]\n\t"
      "s_waitcnt lgkmcnt(4)\n\t"
      "v_mfma_f32_16x16x32_bf16 v[144:147], a[140:143], a[16:19], v[144:147]\n\t"
      "ds_read_b64_tr_b16 a[136:137], v18 offset:40960\n\t"
      "v_mfma_f32_16x16x32_bf16 v[148:151], a[140:143], a[20:23], v[148:151]\n\t"
      "ds_read_b64_tr_b16 a[138:139], v18 offset:41472\n\t"
      "v_mfma_f32_16x16x32_bf16 v[152:155], a[140:143], a[24:27], v[152:155]\n\t"
      "s_mul_i32 s65, %19, 1835008\n\t"
      "v_mfma_f32_16x16x32_bf16 v[156:159], a[140:143], a[28:31], v[156:159]\n\t"
      "s_waitcnt lgkmcnt(4)\n\t"
      "v_mfma_f32_16x16x32_bf16 v[160:163], a[128:131], a[96:99], v[160:163]\n\t"
      "ds_read_b64_tr_b16 a[140:141], v18 offset:45056\n\t"
      "v_mfma_f32_16x16x32_bf16 v[164:167], a[128:131], a[100:103], v[164:167]\n\t"
      "ds_read_b64_tr_b16 a[142:143], v18 offset:45568\n\t"
      "v_mfma_f32_16x16x32_bf16 v[168:171], a[128:131], a[104:107], v[168:171]\n\t"
      "s_add_u32 s56, s56, s65\n\t"
      "v_mfma_f32_16x16x32_bf16 v[172:175], a[128:131], a[108:111], v[172:175]\n\t"
      "s_waitcnt lgkmcnt(4)\n\t"
      "v_mfma_f32_16x16x32_bf16 v[160:163], a[132:135], a[112:115], v[160:163]\n\t"
      "ds_read_b64_tr_b16 a[128:129], v19 offset:12288\n\t"
      "v_mfma_f32_16x16x32_bf16 v[164:167], a[132:135], a[116:119], v[164:167]\n\t"
      "ds_read_b64_tr_b16 a[130:131], v19 offset:12800\n\t"
      "v_mfma_f32_16x16x32_bf16 v[168:171], a[132:135], a[120:123], v[168:171]\n\t"
      "s_add_u32 s40, %6, s56\n\t"
      "s_addc_u32 s41, %7, 0\n\t"
      "v_mfma_f32_16x16x32_bf16 v[172:175], a[132:135], a[124:127], v[172:175]\n\t"
      "s_waitcnt lgkmcnt(4)\n\t"
      "v_mfma_f32_16x16x32_bf16 v[160:163], a[136:139], a[0:3], v[160:163]\n\t"
      "ds_read_b64_tr_b16 a[132:133], v19 offset:16384\n\t"
      "v_mfma_f32_16x16x32_bf16 v[164:167], a[136:139], a[4:7], v[164:167]\n\t"
      "ds_read_b64_tr_b16 a[134:135], v19 offset:16896\n\t"
      "v_mfma_f32_16x16x32_bf16 v[168:171], a[136:139], a[8:11], v[168:171]\n\t"
      "s_mov_b64 s[42:43], s[40:41]\n\t"
      "v_mfma_f32_16x16x32_bf16 v[172:175], a[136:139], a[12:15], v[172:175]\n\t"
      "s_waitcnt lgkmcnt(4)\n\t"
      "v_mfma_f32_16x16x32_bf16 v[160:163], a[140:143], a[16:19], v[160:163]\n\t"
      "ds_read_b64_tr_b16 a[136:137], v19 offset:40960\n\t"
      "v_mfma_f32_16x16x32_bf16 v[164:167], a[140:143], a[20:23], v[164:167]\n\t"
      "ds_read_b64_tr_b16 a[138:139], v19 offset:41472\n\t"
      "v_mfma_f32_16x16x32_bf16 v[168:171], a[140:143], a[24:27], v[168:171]\n\t"
      "s_lshl_b32 s60, %14, 1\n\t"
      "v_mfma_f32_16x16x32_bf16 v[172:175], a[140:143], a[28:31], v[172:175]\n\t"
      "s_waitcnt lgkmcnt(4)\n\t"
      "v_mfma_f32_16x16x32_bf16 v[176:179], a[128:131], a[96:99], v[176:179]\n\t"
      "ds_read_b64_tr_b16 a[140:141], v19 offset:45056\n\t"
      "v_mfma_f32_16x16x32_bf16 v[180:183], a[128:131], a[100:103], v[180:183]\n\t"
      "ds_read_b64_tr_b16 a[142:143], v19 offset:45568\n\t"
      "v_mfma_f32_16x16x32_bf16 v[184:187], a[128:131], a[104:107], v[184:187]\n\t"
      "s_waitcnt vmcnt(0)\n\t"
      "s_mov_b64 exec, 0xff\n\t"
      "buffer_store_dwordx4 a[252:255], v1, s[44:47], s76 offen sc1\n\t"
      "s_mov_b64 exec, -1\n\t"
      "s_sub_u32 s56, s82, 114688\n\t"
      "v_mfma_f32_16x16x32_bf16 v[188:191], a[128:131], a[108:111], v[188:191]\n\t"
      "v_cvt_pk_bf16_f32 v144, v144, v145\n\t"
      "s_waitcnt lgkmcnt(4)\n\t"
      "v_mfma_f32_16x16x32_bf16 v[176:179], a[132:135], a[112:115], v[176:179]\n\t"
      "ds_read_b64_tr_b16 a[128:129], v20 offset:12288\n\t"
      "v_cvt_pk_bf16_f32 v145, v146, v147\n\t"
      "v_mfma_f32_16x16x32_bf16 v[180:183], a[132:135], a[116:119], v[180:183]\n\t"
      "ds_read_b64_tr_b16 a[130:131], v20 offset:12800\n\t"
      "v_cvt_pk_bf16_f32 v146, v160, v161\n\t"
      "v_mfma_f32_16x16x32_bf16 v[184:187], a[132:135], a[120:123], v[184:187]\n\t"
      "s_lshl_b32 s65, %20, 12\n\t"
      "v_cvt_pk_bf16_f32 v147, v162, v163\n\t"
      "v_mfma_f32_16x16x32_bf16 v[188:191], a[132:135], a[124:127], v[188:191]\n\t"
      "v_pk_max_i16 v144, v144, 0\n\t"
      "s_waitcnt lgkmcnt(4)\n\t"
      "v_mfma_f32_16x16x32_bf16 v[176:179], a[136:139], a[0:3], v[176:179]\n\t"
      "ds_read_b64_tr_b16 a[132:133], v20 offset:16384\n\t"
      "v_pk_max_i16 v145, v145, 0\n\t"
      "v_mfma_f32_16x16x32_bf16 v[180:183], a[136:139], a[4:7], v[180:183]\n\t"
      "ds_read_b64_tr_b16 a[134:135], v20 offset:16896\n\t"
      "v_pk_max_i16 v146, v146, 0\n\t"
      "v_mfma_f32_16x16x32_bf16 v[184:187], a[136:139], a[8:11], v[184:187]\n\t"
      "s_sub_u32 s56, s56, s65\n\t"
      "v_pk_max_i16 v147, v147, 0\n\t"
      "v_mfma_f32_16x16x32_bf16 v[188:191], a[136:139], a[12:15], v[188:191]\n\t"
      "v_cvt_pk_bf16_f32 v148, v148, v149\n\t"
      "s_waitcnt lgkmcnt(4)\n\t"
      "v_mfma_f32_16x16x32_bf16 v[176:179], a[140:143], a[16:19], v[176:179]\n\t"
      "ds_read_b64_tr_b16 a[136:137], v20 offset:40960\n\t"
      "v_cvt_pk_bf16_f32 v149, v150, v151\n\t"
      "v_mfma_f32_16x16x32_bf16 v[180:183], a[140:143], a[20:23], v[180:183]\n\t"
      "ds_read_b64_tr_b16 a[138:139], v20 offset:41472\n\t"
      "v_cvt_pk_bf16_f32 v150, v164, v165\n\t"
      "v_mfma_f32_16x16x32_bf16 v[184:187], a[140:143], a[24:27], v[184:187]\n\t"
      "s_add_u32 s36, %0, s56\n\t"
      "s_addc_u32 s37, %1, 0\n\t"
      "v_mfma_f32_16x16x32_bf16 v[188:191], a[140:143], a[28:31], v[188:191]\n\t"
      "v_cvt_pk_bf16_f32 v151, v166, v167\n\t"
      "s_waitcnt lgkmcnt(4)\n\t"
      "v_mfma_f32_16x16x32_bf16 v[192:195], a[128:131], a[96:99], v[192:195]\n\t"
      "ds_read_b64_tr_b16 a[140:141], v20 offset:45056\n\t"
      "v_pk_max_i16 v148, v148, 0\n\t"
      "v_mfma_f32_16x16x32_bf16 v[196:199], a[128:131], a[100:103], v[196:199]\n\t"
      "ds_read_b64_tr_b16 a[142:143], v20 offset:45568\n\t"
      "v_pk_max_i16 v149, v149, 0\n\t"
      "v_mfma_f32_16x16x32_bf16 v[200:203], a[128:131], a[104:107], v[200:203]\n\t"
      "v_accvgpr_read_b32 v2, a202\n\t"
      "v_pk_max_i16 v150, v150, 0\n\t"
      "v_mfma_f32_16x16x32_bf16 v[204:207], a[128:131], a[108:111], v[204:207]\n\t"
      "v_pk_max_i16 v151, v151, 0\n\t"
      "s_waitcnt lgkmcnt(4)\n\t"
      "v_mfma_f32_16x16x32_bf16 v[192:195], a[132:135], a[112:115], v[192:195]\n\t"
      "ds_read_b64_tr_b16 a[128:129], v21 offset:20480\n\t"
      "v_cvt_pk_bf16_f32 v152, v152, v153\n\t"
      "v_mfma_f32_16x16x32_bf16 v[196:199], a[132:135], a[116:119], v[196:199]\n\t"
      "ds_read_b64_tr_b16 a[130:131], v21 offset:20736\n\t"
      "v_cvt_pk_bf16_f32 v153, v154, v155\n\t"
      "v_mfma_f32_16x16x32_bf16 v[200:203], a[132:135], a[120:123], v[200:203]\n\t"
      "v_accvgpr_read_b32 v3, a203\n\t"
      "v_cvt_pk_bf16_f32 v154, v168, v169\n\t"
      "v_mfma_f32_16x16x32_bf16 v[204:207], a[132:135], a[124:127], v[204:207]\n\t"
      "v_cvt_pk_bf16_f32 v155, v170, v171\n\t"
      "s_waitcnt lgkmcnt(4)\n\t"
      "v_mfma_f32_16x16x32_bf16 v[192:195], a[136:139], a[0:3], v[192:195]\n\t"
      "ds_read_b64_tr_b16 a[132:133], v21 offset:22528\n\t"
      "v_pk_max_i16 v152, v152, 0\n\t"
      "v_mfma_f32_16x16x32_bf16 v[196:199], a[136:139], a[4:7], v[196:199]\n\t"
      "ds_read_b64_tr_b16 a[134:135], v21 offset:22784\n\t"
      "v_pk_max_i16 v153, v153, 0\n\t"
      "v_mfma_f32_16x16x32_bf16 v[200:203], a[136:139], a[8:11], v[200:203]\n\t"
      "v_accvgpr_read_b32 v4, a204\n\t"
      "v_pk_max_i16 v154, v154, 0\n\t"
      "v_mfma_f32_16x16x32_bf16 v[204:207], a[136:139], a[12:15], v[204:207]\n\t"
      "v_pk_max_i16 v155, v155, 0\n\t"
      "s_waitcnt lgkmcnt(4)\n\t"
      "v_mfma_f32_16x16x32_bf16 v[192:195], a[140:143], a[16:19], v[192:195]\n\t"
      "ds_read_b64_tr_b16 a[136:137], v21 offset:49152\n\t"
      "v_cvt_pk_bf16_f32 v156, v156, v157\n\t"
      "v_mfma_f32_16x16x32_bf16 v[196:199], a[140:143], a[20:23], v[196:199]\n\t"
      "ds_read_b64_tr_b16 a[138:139], v21 offset:49408\n\t"
      "v_cvt_pk_bf16_f32 v157, v158, v159\n\t"
      "v_mfma_f32_16x16x32_bf16 v[200:203], a[140:143], a[24:27], v[200:203]\n\t"
      "v_accvgpr_read_b32 v5, a205\n\t"
      "v_cvt_pk_bf16_f32 v158, v172, v173\n\t"
      "v_mfma_f32_16x16x32_bf16 v[204:207], a[140:143], a[28:31], v[204:207]\n\t"
      "v_cvt_pk_bf16_f32 v159, v174, v175\n\t"
      "s_waitcnt lgkmcnt(4)\n\t"
      "v_mfma_f32_16x16x32_bf16 v[208:211], a[128:131], a[96:99], v[208:211]\n\t"
      "ds_read_b64_tr_b16 a[140:141], v21 offset:51200\n\t"
      "v_pk_max_i16 v156, v156, 0\n\t"
      "v_mfma_f32_16x16x32_bf16 v[212:215], a[128:131], a[100:103], v[212:215]\n\t"
      "ds_read_b64_tr_b16 a[142:143], v21 offset:51456\n\t"
      "v_pk_max_i16 v157, v157, 0\n\t"
      "v_mfma_f32_16x16x32_bf16 v[216:219], a[128:131], a[104:107], v[216:219]\n\t"
      "v_pk_max_i16 v158, v158, 0\n\t"
      "v_pk_max_i16 v159, v159, 0\n\t"
      "v_mfma_f32_16x16x32_bf16 v[220:223], a[128:131], a[108:111], v[220:223]\n\t"
      "v_cvt_pk_bf16_f32 v176, v176, v177\n\t"
      "s_waitcnt lgkmcnt(4)\n\t"
      "v_mfma_f32_16x16x32_bf16 v[208:211], a[132:135], a[112:115], v[208:211]\n\t"
      "ds_read_b64_tr_b16 a[128:129], v22 offset:20480\n\t"
      "v_cvt_pk_bf16_f32 v177, v178, v179\n\t"
      "v_mfma_f32_16x16x32_bf16 v[212:215], a[132:135], a[116:119], v[212:215]\n\t"
      "ds_read_b64_tr_b16 a[130:131], v22 offset:20736\n\t"
      "v_cvt_pk_bf16_f32 v178, v192, v193\n\t"
      "v_mfma_f32_16x16x32_bf16 v[216:219], a[132:135], a[120:123], v[216:219]\n\t"
      "v_cvt_pk_bf16_f32 v179, v194, v195\n\t"
      "v_pk_max_i16 v176, v176, 0\n\t"
      "v_mfma_f32_16x16x32_bf16 v[220:223], a[132:135], a[124:127], v[220:223]\n\t"
      "v_pk_max_i16 v177, v177, 0\n\t"
      "s_waitcnt lgkmcnt(4)\n\t"
      "v_mfma_f32_16x16x32_bf16 v[208:211], a[136:139], a[0:3], v[208:211]\n\t"
      "ds_read_b64_tr_b16 a[132:133], v22 offset:22528\n\t"
      "v_pk_max_i16 v178, v178, 0\n\t"
      "v_mfma_f32_16x16x32_bf16 v[212:215], a[136:139], a[4:7], v[212:215]\n\t"
      "ds_read_b64_tr_b16 a[134:135], v22 offset:22784\n\t"
      "v_pk_max_i16 v179, v179, 0\n\t"
      "v_mfma_f32_16x16x32_bf16 v[216:219], a[136:139], a[8:11], v[216:219]\n\t"
      "v_cvt_pk_bf16_f32 v180, v180, v181\n\t"
      "v_cvt_pk_bf16_f32 v181, v182, v183\n\t"
      "v_mfma_f32_16x16x32_bf16 v[220:223], a[136:139], a[12:15], v[220:223]\n\t"
      "v_cvt_pk_bf16_f32 v182, v196, v197\n\t"
      "s_waitcnt lgkmcnt(4)\n\t"
      "v_mfma_f32_16x16x32_bf16 v[208:211], a[140:143], a[16:19], v[208:211]\n\t"
      "ds_read_b64_tr_b16 a[136:137], v22 offset:49152\n\t"
      "v_cvt_pk_bf16_f32 v183, v198, v199\n\t"
      "v_mfma_f32_16x16x32_bf16 v[212:215], a[140:143], a[20:23], v[212:215]\n\t"
      "ds_read_b64_tr_b16 a[138:139], v22 offset:49408\n\t"
      "v_pk_max_i16 v180, v180, 0\n\t"
      "v_mfma_f32_16x16x32_bf16 v[216:219], a[140:143], a[24:27], v[216:219]\n\t"
      "v_pk_max_i16 v181, v181, 0\n\t"
      "v_pk_max_i16 v182, v182, 0\n\t"
      "v_mfma_f32_16x16x32_bf16 v[220:223], a[140:143], a[28:31], v[220:223]\n\t"
      "v_pk_max_i16 v183, v183, 0\n\t"
      "s_waitcnt lgkmcnt(4)\n\t"
      "v_mfma_f32_16x16x32_bf16 v[224:227], a[128:131], a[96:99], v[224:227]\n\t"
      "ds_read_b64_tr_b16 a[140:141], v22 offset:51200\n\t"
      "v_cvt_pk_bf16_f32 v184, v184, v185\n\t"
      "v_mfma_f32_16x16x32_bf16 v[228:231], a[128:131], a[100:103], v[228:231]\n\t"
      "ds_read_b64_tr_b16 a[142:143], v22 offset:51456\n\t"
      "v_cvt_pk_bf16_f32 v185, v186, v187\n\t"
      "v_mfma_f32_16x16x32_bf16 v[232:235], a[128:131], a[104:107], v[232:235]\n\t"
      "v_cvt_pk_bf16_f32 v186, v200, v201\n\t"
      "v_cvt_pk_bf16_f32 v187, v202, v203\n\t"
      "v_mfma_f32_16x16x32_bf16 v[236:239], a[128:131], a[108:111], v[236:239]\n\t"
      "v_pk_max_i16 v184, v184, 0\n\t"
      "s_waitcnt lgkmcnt(4)\n\t"
      "v_mfma_f32_16x16x32_bf16 v[224:227], a[132:135], a[112:115], v[224:227]\n\t"
      "ds_read_b64_tr_b16 a[128:129], v23 offset:24576\n\t"
      "v_pk_max_i16 v185, v185, 0\n\t"
      "v_mfma_f32_16x16x32_bf16 v[228:231], a[132:135], a[116:119], v[228:231]\n\t"
      "ds_read_b64_tr_b16 a[130:131], v23 offset:24832\n\t"
      "v_pk_max_i16 v186, v186, 0\n\t"
      "v_mfma_f32_16x16x32_bf16 v[232:235], a[132:135], a[120:123], v[232:235]\n\t"
      "v_pk_max_i16 v187, v187, 0\n\t"
      "v_cvt_pk_bf16_f32 v188, v188, v189\n\t"
      "v_mfma_f32_16x16x32_bf16 v[236:239], a[132:135], a[124:127], v[236:239]\n\t"
      "v_cvt_pk_bf16_f32 v189, v190, v191\n\t"
      "s_waitcnt lgkmcnt(4)\n\t"
      "v_mfma_f32_16x16x32_bf16 v[224:227], a[136:139], a[0:3], v[224:227]\n\t"
      "ds_read_b64_tr_b16 a[132:133], v23 offset:26624\n\t"
      "v_cvt_pk_bf16_f32 v190, v204, v205\n\t"
      "v_mfma_f32_16x16x32_bf16 v[228:231], a[136:139], a[4:7], v[228:231]\n\t"
      "ds_read_b64_tr_b16 a[134:135], v23 offset:26880\n\t"
      "v_cvt_pk_bf16_f32 v191, v206, v207\n\t"
      "v_mfma_f32_16x16x32_bf16 v[232:235], a[136:139], a[8:11], v[232:235]\n\t"
      "buffer_load_dwordx2 v[26:27], v24, s[44:47], s77 offen sc1\n\t"
      "v_pk_max_i16 v188, v188, 0\n\t"
      "v_mfma_f32_16x16x32_bf16 v[236:239], a[136:139], a[12:15], v[236:239]\n\t"
      "v_pk_max_i16 v189, v189, 0\n\t"
      "s_waitcnt lgkmcnt(4)\n\t"
      "v_mfma_f32_16x16x32_bf16 v[224:227], a[140:143], a[16:19], v[224:227]\n\t"
      "ds_read_b64_tr_b16 a[136:137], v23 offset:53248\n\t"
      "v_pk_max_i16 v190, v190, 0\n\t"
      "v_mfma_f32_16x16x32_bf16 v[228:231], a[140:143], a[20:23], v[228:231]\n\t"
      "ds_read_b64_tr_b16 a[138:139], v23 offset:53504\n\t"
      "v_pk_max_i16 v191, v191, 0\n\t"
      "v_mfma_f32_16x16x32_bf16 v[232:235], a[140:143], a[24:27], v[232:235]\n\t"
      "v_mfma_f32_16x16x32_bf16 v[236:239], a[140:143], a[28:31], v[236:239]\n\t"
      "s_waitcnt lgkmcnt(4)\n\t"
      "v_mfma_f32_16x16x32_bf16 v[240:243], a[128:131], a[96:99], v[240:243]\n\t"
      "ds_read_b64_tr_b16 a[140:141], v23 offset:55296\n\t"
      "v_mfma_f32_16x16x32_bf16 v[244:247], a[128:131], a[100:103], v[244:247]\n\t"
      "ds_read_b64_tr_b16 a[142:143], v23 offset:55552\n\t"
      "v_mfma_f32_16x16x32_bf16 v[248:251], a[128:131], a[104:107], v[248:251]\n\t"
      "v_mfma_f32_16x16x32_bf16 v[252:255], a[128:131], a[108:111], v[252:255]\n\t"
      "s_waitcnt lgkmcnt(4)\n\t"
      "v_mfma_f32_16x16x32_bf16 v[240:243], a[132:135], a[112:115], v[240:243]\n\t"
      "ds_read_b64_tr_b16 a[128:129], v21 offset:8192\n\t"
      "v_cvt_pk_bf16_f32 v208, v208, v209\n\t"
      "v_mfma_f32_16x16x32_bf16 v[244:247], a[132:135], a[116:119], v[244:247]\n\t"
      "ds_read_b64_tr_b16 a[130:131], v21 offset:8448\n\t"
      "v_cvt_pk_bf16_f32 v209, v210, v211\n\t"
      "v_mfma_f32_16x16x32_bf16 v[248:251], a[132:135], a[120:123], v[248:251]\n\t"
      "v_cvt_pk_bf16_f32 v210, v224, v225\n\t"
      "v_cvt_pk_bf16_f32 v211, v226, v227\n\t"
      "v_mfma_f32_16x16x32_bf16 v[252:255], a[132:135], a[124:127], v[252:255]\n\t"
      "v_pk_max_i16 v208, v208, 0\n\t"
      "s_waitcnt lgkmcnt(4)\n\t"
      "v_mfma_f32_16x16x32_bf16 v[240:243], a[136:139], a[0:3], v[240:243]\n\t"
      "ds_read_b64_tr_b16 a[132:133], v21 offset:10240\n\t"
      "v_pk_max_i16 v209, v209, 0\n\t"
      "v_mfma_f32_16x16x32_bf16 v[244:247], a[136:139], a[4:7], v[244:247]\n\t"
      "ds_read_b64_tr_b16 a[134:135], v21 offset:10496\n\t"
      "v_pk_max_i16 v210, v210, 0\n\t"
      "v_mfma_f32_16x16x32_bf16 v[248:251], a[136:139], a[8:11], v[248:251]\n\t"
      "v_pk_max_i16 v211, v211, 0\n\t"
      "v_cvt_pk_bf16_f32 v212, v212, v213\n\t"
      "v_mfma_f32_16x16x32_bf16 v[252:255], a[136:139], a[12:15], v[252:255]\n\t"
      "v_cvt_pk_bf16_f32 v213, v214, v215\n\t"
      "s_waitcnt lgkmcnt(4)\n\t"
      "v_mfma_f32_16x16x32_bf16 v[240:243], a[140:143], a[16:19], v[240:243]\n\t"
      "ds_read_b64_tr_b16 a[136:137], v21 offset:36864\n\t"
      "v_cvt_pk_bf16_f32 v214, v228, v229\n\t"
      "v_mfma_f32_16x16x32_bf16 v[244:247], a[140:143], a[20:23], v[244:247]\n\t"
      "ds_read_b64_tr_b16 a[138:139], v21 offset:37120\n\t"
      "v_cvt_pk_bf16_f32 v215, v230, v231\n\t"
      "v_mfma_f32_16x16x32_bf16 v[248:251], a[140:143], a[24:27], v[248:251]\n\t"
      "v_pk_max_i16 v212, v212, 0\n\t"
      "v_pk_max_i16 v213, v213, 0\n\t"
      "v_mfma_f32_16x16x32_bf16 v[252:255], a[140:143], a[28:31], v[252:255]\n\t"
      "v_pk_max_i16 v214, v214, 0\n\t"
      "s_waitcnt lgkmcnt(4)\n\t"
      "v_mfma_f32_16x16x32_bf16 v[112:115], a[128:131], a[96:99], v[112:115]\n\t"
      "ds_read_b64_tr_b16 a[140:141], v21 offset:38912\n\t"
      "v_pk_max_i16 v215, v215, 0\n\t"
      "v_mfma_f32_16x16x32_bf16 v[116:119], a[128:131], a[100:103], v[116:119]\n\t"
      "ds_read_b64_tr_b16 a[142:143], v21 offset:39168\n\t"
      "v_cvt_pk_bf16_f32 v216, v216, v217\n\t"
      "v_mfma_f32_16x16x32_bf16 v[120:123], a[128:131], a[104:107], v[120:123]\n\t"
      "v_cvt_pk_bf16_f32 v217, v218, v219\n\t"
      "v_cvt_pk_bf16_f32 v218, v232, v233\n\t"
      "v_mfma_f32_16x16x32_bf16 v[124:127], a[128:131], a[108:111], v[124:127]\n\t"
      "v_cvt_pk_bf16_f32 v219, v234, v235\n\t"
      "s_waitcnt lgkmcnt(4)\n\t"
      "v_mfma_f32_16x16x32_bf16 v[112:115], a[132:135], a[112:115], v[112:115]\n\t"
      "ds_read_b64_tr_b16 a[128:129], v22 offset:8192\n\t"
      "v_pk_max_i16 v216, v216, 0\n\t"
      "v_mfma_f32_16x16x32_bf16 v[116:119], a[132:135], a[116:119], v[116:119]\n\t"
      "ds_read_b64_tr_b16 a[130:131], v22 offset:8448\n\t"
      "v_pk_max_i16 v217, v217, 0\n\t"
      "v_mfma_f32_16x16x32_bf16 v[120:123], a[132:135], a[120:123], v[120:123]\n\t"
      "v_pk_max_i16 v218, v218, 0\n\t"
      "v_pk_max_i16 v219, v219, 0\n\t"
      "v_mfma_f32_16x16x32_bf16 v[124:127], a[132:135], a[124:127], v[124:127]\n\t"
      "v_cvt_pk_bf16_f32 v220, v220, v221\n\t"
      "s_waitcnt lgkmcnt(4)\n\t"
      "v_mfma_f32_16x16x32_bf16 v[112:115], a[136:139], a[0:3], v[112:115]\n\t"
      "ds_read_b64_tr_b16 a[132:133], v22 offset:10240\n\t"
      "v_cvt_pk_bf16_f32 v221, v222, v223\n\t"
      "v_mfma_f32_16x16x32_bf16 v[116:119], a[136:139], a[4:7], v[116:119]\n\t"
      "ds_read_b64_tr_b16 a[134:135], v22 offset:10496\n\t"
      "v_cvt_pk_bf16_f32 v222, v236, v237\n\t"
      "v_mfma_f32_16x16x32_bf16 v[120:123], a[136:139], a[8:11], v[120:123]\n\t"
      "v_cvt_pk_bf16_f32 v223, v238, v239\n\t"
      "v_pk_max_i16 v220, v220, 0\n\t"
      "v_mfma_f32_16x16x32_bf16 v[124:127], a[136:139], a[12:15], v[124:127]\n\t"
      "v_pk_max_i16 v221, v221, 0\n\t"
      "s_waitcnt lgkmcnt(4)\n\t"
      "v_mfma_f32_16x16x32_bf16 v[112:115], a[140:143], a[16:19], v[112:115]\n\t"
      "ds_read_b64_tr_b16 a[136:137], v22 offset:36864\n\t"
      "v_pk_max_i16 v222, v222, 0\n\t"
      "v_mfma_f32_16x16x32_bf16 v[116:119], a[140:143], a[20:23], v[116:119]\n\t"
      "ds_read_b64_tr_b16 a[138:139], v22 offset:37120\n\t"
      "v_pk_max_i16 v223, v223, 0\n\t"
      "v_mfma_f32_16x16x32_bf16 v[120:123], a[140:143], a[24:27], v[120:123]\n\t"
      "v_cvt_pk_bf16_f32 v240, v240, v241\n\t"
      "v_cvt_pk_bf16_f32 v241, v242, v243\n\t"
      "v_mfma_f32_16x16x32_bf16 v[124:127], a[140:143], a[28:31], v[124:127]\n\t"
      "v_pk_max_i16 v240, v240, 0\n\t"
      "s_waitcnt lgkmcnt(4)\n\t"
      "v_mfma_f32_16x16x32_bf16 v[128:131], a[128:131], a[96:99], v[128:131]\n\t"
      "ds_read_b64_tr_b16 a[140:141], v22 offset:38912\n\t"
      "v_pk_max_i16 v241, v241, 0\n\t"
      "v_mfma_f32_16x16x32_bf16 v[132:135], a[128:131], a[100:103], v[132:135]\n\t"
      "ds_read_b64_tr_b16 a[142:143], v22 offset:39168\n\t"
      "v_cvt_pk_bf16_f32 v244, v244, v245\n\t"
      "v_mfma_f32_16x16x32_bf16 v[136:139], a[128:131], a[104:107], v[136:139]\n\t"
      "s_waitcnt vmcnt(0)\n\t"
      "v_cmp_eq_u64 vcc, s[58:59], v[26:27]\n\t"
      "s_nop 1\n\t"
      "s_cmp_eq_u64 vcc, -1\n\t"
      "s_cbranch_scc1 .Lkm_kv_ok_%=\n\t"
      "s_mov_b32 s67, 0\n\t"
      ".Lkm_kv_spin_%=:\n\t"
      "s_sleep 1\n\t"
      "buffer_load_dwordx2 v[26:27], v24, s[44:47], s77 offen sc1\n\t"
      "s_add_u32 s67, s67, 1\n\t"
      "s_cmp_ge_u32 s67, 262144\n\t"
      "s_cbranch_scc1 .Lkm_kv_ok_%=\n\t"
      "s_waitcnt vmcnt(0)\n\t"
      "v_cmp_eq_u64 vcc, s[58:59], v[26:27]\n\t"
      "s_nop 1\n\t"
      "s_cmp_eq_u64 vcc, -1\n\t"
      "s_cbranch_scc0 .Lkm_kv_spin_%=\n\t"
      ".Lkm_kv_ok_%=:\n\t"
      "v_mfma_f32_16x16x32_bf16 v[140:143], a[128:131], a[108:111], v[140:143]\n\t"
      "s_nop 0\n\t"
      "s_waitcnt lgkmcnt(4)\n\t"
      "v_mfma_f32_16x16x32_bf16 v[128:131], a[132:135], a[112:115], v[128:131]\n\t"
      "s_add_u32 m0, s64, 0\n\t"
      "v_cvt_pk_bf16_f32 v245, v246, v247\n\t"
      "v_mfma_f32_16x16x32_bf16 v[132:135], a[132:135], a[116:119], v[132:135]\n\t"
      "buffer_load_dwordx4 v1, s[48:51], s79 offen offset:0 sc1 lds\n\t"
      "v_pk_max_i16 v244, v244, 0\n\t"
      "v_mfma_f32_16x16x32_bf16 v[136:139], a[132:135], a[120:123], v[136:139]\n\t"
      "buffer_load_dwordx4 v1, s[48:51], s79 offen offset:1024 sc1 lds\n\t"
      "v_pk_max_i16 v245, v245, 0\n\t"
      "v_mfma_f32_16x16x32_bf16 v[140:143], a[132:135], a[124:127], v[140:143]\n\t"
      "buffer_load_dwordx4 v1, s[48:51], s79 offen offset:2048 sc1 lds\n\t"
      "s_waitcnt lgkmcnt(2)\n\t"
      "v_mfma_f32_16x16x32_bf16 v[128:131], a[136:139], a[0:3], v[128:131]\n\t"
      "buffer_load_dwordx4 v1, s[48:51], s79 offen offset:3072 sc1 lds\n\t"
      "v_cvt_pk_bf16_f32 v248, v248, v249\n\t"
      "v_mfma_f32_16x16x32_bf16 v[132:135], a[136:139], a[4:7], v[132:135]\n\t"
      "s_nop 0\n\t"
      "v_cvt_pk_bf16_f32 v249, v250, v251\n\t"
      "v_mfma_f32_16x16x32_bf16 v[136:139], a[136:139], a[8:11], v[136:139]\n\t"
      "s_add_u32 m0, s62, 16384\n\t"
      "v_pk_max_i16 v248, v248, 0\n\t"
      "v_mfma_f32_16x16x32_bf16 v[140:143], a[136:139], a[12:15], v[140:143]\n\t"
      "buffer_load_dwordx4 v1, s[44:47], s66 offen offset:0 sc1 lds\n\t"
      "s_waitcnt lgkmcnt(0)\n\t"
      "v_mfma_f32_16x16x32_bf16 v[128:131], a[140:143], a[16:19], v[128:131]\n\t"
      "buffer_load_dwordx4 v1, s[44:47], s66 offen offset:1024 sc1 lds\n\t"
      "v_pk_max_i16 v249, v249, 0\n\t"
      "v_mfma_f32_16x16x32_bf16 v[132:135], a[140:143], a[20:23], v[132:135]\n\t"
      "buffer_load_dwordx4 v1, s[44:47], s66 offen offset:2048 sc1 lds\n\t"
      "v_cvt_pk_bf16_f32 v252, v252, v253\n\t"
      "v_mfma_f32_16x16x32_bf16 v[136:139], a[140:143], a[24:27], v[136:139]\n\t"
      "buffer_load_dwordx4 v1, s[44:47], s66 offen offset:3072 sc1 lds\n\t"
      "v_cvt_pk_bf16_f32 v253, v254, v255\n\t"
      "v_mfma_f32_16x16x32_bf16 v[140:143], a[140:143], a[28:31], v[140:143]\n\t"
      "s_nop 0\n\t"
      "s_add_u32 m0, s64, 32768\n\t"
      "s_nop 0\n\t"
      "buffer_load_dwordx4 v1, s[48:51], s61 offen offset:0 sc1 lds\n\t"
      "buffer_load_dwordx4 v1, s[48:51], s61 offen offset:1024 sc1 lds\n\t"
      "buffer_load_dwordx4 v1, s[48:51], s61 offen offset:2048 sc1 lds\n\t"
      "buffer_load_dwordx4 v1, s[48:51], s61 offen offset:3072 sc1 lds\n\t"
      "ds_write_b128 v9, v[48:51] offset:16384\n\t"
      "ds_write_b128 v9, v[52:55] offset:17408\n\t"
      "ds_write_b128 v9, v[56:59] offset:18432\n\t"
      "ds_write_b128 v9, v[60:63] offset:19456\n\t"
      "v_pk_max_i16 v252, v252, 0\n\t"
      "v_pk_max_i16 v253, v253, 0\n\t"
      "v_cvt_pk_bf16_f32 v32, v112, v113\n\t"
      "v_cvt_pk_bf16_f32 v33, v114, v115\n\t"
      "v_cvt_pk_bf16_f32 v34, v128, v129\n\t"
      "v_cvt_pk_bf16_f32 v35, v130, v131\n\t"
      "v_cvt_pk_bf16_f32 v36, v116, v117\n\t"
      "v_cvt_pk_bf16_f32 v37, v118, v119\n\t"
      "v_cvt_pk_bf16_f32 v38, v132, v133\n\t"
      "v_cvt_pk_bf16_f32 v39, v134, v135\n\t"
      "v_cvt_pk_bf16_f32 v40, v120, v121\n\t"
      "v_cvt_pk_bf16_f32 v41, v122, v123\n\t"
      "v_cvt_pk_bf16_f32 v42, v136, v137\n\t"
      "v_cvt_pk_bf16_f32 v43, v138, v139\n\t"
      "v_cvt_pk_bf16_f32 v44, v124, v125\n\t"
      "v_cvt_pk_bf16_f32 v45, v126, v127\n\t"
      "v_cvt_pk_bf16_f32 v46, v140, v141\n\t"
      "v_cvt_pk_bf16_f32 v47, v142, v143\n\t"
      "v_mov_b32 v13, 0\n\t"
      "v_accvgpr_read_b32 v6, a206\n\t"
      "v_accvgpr_read_b32 v7, a207\n\t"
      "v_accvgpr_read_b32 v8, a208\n\t"
      "v_accvgpr_read_b32 v9, a209\n\t"
      "v_accvgpr_read_b32 v10, a210\n\t"
      "v_accvgpr_read_b32 v18, a218\n\t"
      "v_accvgpr_read_b32 v12, a212\n\t"
      "v_accvgpr_read_b32 v17, a217\n\t"
      "v_lshlrev_b32 v16, 3, v0\n\t"
      "v_and_b32 v20, 7, v0\n\t"
      "v_lshlrev_b32 v20, 7, v20\n\t"
      "s_lshl_b32 s62, %14, 11\n\t"
      "s_add_u32 s62, s62, %16\n\t"
      "s_add_u32 s62, s62, 65536\n\t"
      "s_waitcnt lgkmcnt(0)\n\t"
      "v_accvgpr_read_b32 v116, a224\n\t"
      "v_accvgpr_read_b32 v120, a228\n\t"
      "v_mov_b32 v124, 0xf149f2ca\n\t"
      "v_accvgpr_read_b32 v117, a225\n\t"
      "v_accvgpr_read_b32 v121, a229\n\t"
      "v_mov_b32 v125, 0xf149f2ca\n\t"
      "v_accvgpr_read_b32 v118, a226\n\t"
      "v_accvgpr_read_b32 v122, a230\n\t"
      "v_mov_b32 v126, 0xf149f2ca\n\t"
      "v_accvgpr_read_b32 v119, a227\n\t"
      "v_accvgpr_read_b32 v123, a231\n\t"
      "v_mov_b32 v127, 0xf149f2ca\n\t"
      "s_waitcnt vmcnt(4)\n\t"
      "s_barrier\n\t"
      "ds_read_b128 a[48:51], v10 offset:0\n\t"
      "ds_read_b128 a[52:55], v10 offset:1024\n\t"
      "ds_read_b128 a[56:59], v10 offset:2048\n\t"
      "ds_read_b128 a[60:63], v10 offset:3072\n\t"
      "ds_read_b128 a[64:67], v10 offset:4096\n\t"
      "ds_read_b128 a[68:71], v10 offset:5120\n\t"
      "ds_read_b128 a[72:75], v10 offset:6144\n\t"
      "ds_read_b128 a[76:79], v10 offset:7168\n\t"
      "s_cmp_eq_u32 %17, 0\n\t"
      "s_cbranch_scc0 .Lkm_n0_0_%=\n\t"
      "s_waitcnt lgkmcnt(6)\n\t"
      "v_mfma_f32_16x16x32_bf16 v[48:51], a[48:51], v[32:35], v[124:127]\n\t"
      "ds_read_b128 a[80:83], v10 offset:8192\n\t"
      "v_mfma_f32_16x16x32_bf16 v[52:55], a[52:55], v[32:35], v[124:127]\n\t"
      "ds_read_b128 a[84:87], v10 offset:9216\n\t"
      "s_waitcnt lgkmcnt(6)\n\t"
      "v_mfma_f32_16x16x32_bf16 v[56:59], a[56:59], v[32:35], v[124:127]\n\t"
      "ds_read_b128 a[88:91], v10 offset:10240\n\t"
      "v_mfma_f32_16x16x32_bf16 v[60:63], a[60:63], v[32:35], v[124:127]\n\t"
      "ds_read_b128 a[92:95], v10 offset:11264\n\t"
      "v_max_f32 v140, v48, v49\n\t"
      "v_max_f32 v141, v50, v51\n\t"
      "s_cmp_eq_u32 s60, 6\n\t"
      "s_cbranch_scc1 .Lkm_n2_0_%=\n\t"
      "s_waitcnt lgkmcnt(6)\n\t"
      "v_mfma_f32_16x16x32_bf16 v[64:67], a[64:67], v[32:35], v[124:127]\n\t"
      "ds_read_b128 a[96:99], v10 offset:12288\n\t"
      "v_max3_f32 v140, v52, v53, v140\n\t"
      "v_max3_f32 v141, v54, v55, v141\n\t"
      "v_mfma_f32_16x16x32_bf16 v[68:71], a[68:71], v[32:35], v[124:127]\n\t"
      "ds_read_b128 a[100:103], v10 offset:13312\n\t"
      "v_max3_f32 v140, v56, v57, v140\n\t"
      "v_max3_f32 v141, v58, v59, v141\n\t"
      "s_waitcnt lgkmcnt(6)\n\t"
      "v_mfma_f32_16x16x32_bf16 v[72:75], a[72:75], v[32:35], v[124:127]\n\t"
      "ds_read_b128 a[104:107], v10 offset:14336\n\t"
      "v_max3_f32 v140, v60, v61, v140\n\t"
      "v_max3_f32 v141, v62, v63, v141\n\t"
      "v_mfma_f32_16x16x32_bf16 v[76:79], a[76:79], v[32:35], v[124:127]\n\t"
      "ds_read_b128 a[108:111], v10 offset:15360\n\t"
      "v_max3_f32 v140, v64, v65, v140\n\t"
      "v_max3_f32 v141, v66, v67, v141\n\t"
      "s_cmp_eq_u32 s60, 4\n\t"
      "s_cbranch_scc1 .Lkm_n4_0_%=\n\t"
      "s_waitcnt lgkmcnt(6)\n\t"
      "v_mfma_f32_16x16x32_bf16 v[80:83], a[80:83], v[32:35], v[124:127]\n\t"
      "ds_read_b128 a[112:115], v10 offset:16384\n\t"
      "v_max3_f32 v140, v68, v69, v140\n\t"
      "v_max3_f32 v141, v70, v71, v141\n\t"
      "v_mfma_f32_16x16x32_bf16 v[84:87], a[84:87], v[32:35], v[124:127]\n\t"
      "ds_read_b128 a[116:119], v10 offset:17408\n\t"
      "v_max3_f32 v140, v72, v73, v140\n\t"
      "v_max3_f32 v141, v74, v75, v141\n\t"
      "s_waitcnt lgkmcnt(6)\n\t"
      "v_mfma_f32_16x16x32_bf16 v[88:91], a[88:91], v[32:35], v[124:127]\n\t"
      "v_max3_f32 v140, v76, v77, v140\n\t"
      "v_max3_f32 v141, v78, v79, v141\n\t"
      "v_mfma_f32_16x16x32_bf16 v[92:95], a[92:95], v[32:35], v[124:127]\n\t"
      "v_max3_f32 v140, v80, v81, v140\n\t"
      "v_max3_f32 v141, v82, v83, v141\n\t"
      "s_cmp_eq_u32 s60, 2\n\t"
      "s_cbranch_scc1 .Lkm_n6_0_%=\n\t"
      "s_waitcnt lgkmcnt(4)\n\t"
      "v_mfma_f32_16x16x32_bf16 v[96:99], a[96:99], v[32:35], v[124:127]\n\t"
      "v_max3_f32 v140, v84, v85, v140\n\t"
      "v_max3_f32 v141, v86, v87, v141\n\t"
      "v_mfma_f32_16x16x32_bf16 v[100:103], a[100:103], v[32:35], v[124:127]\n\t"
      "v_max3_f32 v140, v88, v89, v140\n\t"
      "v_max3_f32 v141, v90, v91, v141\n\t"
      "s_waitcnt lgkmcnt(2)\n\t"
      "v_mfma_f32_16x16x32_bf16 v[104:107], a[104:107], v[32:35], v[124:127]\n\t"
      "v_max3_f32 v140, v92, v93, v140\n\t"
      "v_max3_f32 v141, v94, v95, v141\n\t"
      "v_mfma_f32_16x16x32_bf16 v[108:111], a[108:111], v[32:35], v[124:127]\n\t"
      "v_max3_f32 v140, v96, v97, v140\n\t"
      "v_max3_f32 v141, v98, v99, v141\n\t"
      "s_branch .Lkm_n8_0_%=\n\t"
      ".Lkm_n0_0_%=:\n\t"
      "s_waitcnt lgkmcnt(6)\n\t"
      "v_mfma_f32_16x16x32_bf16 v[48:51], a[48:51], v[32:35], v[116:119]\n\t"
      "ds_read_b128 a[80:83], v10 offset:8192\n\t"
      "v_mfma_f32_16x16x32_bf16 v[52:55], a[52:55], v[32:35], 0\n\t"
      "ds_read_b128 a[84:87], v10 offset:9216\n\t"
      ".Lkm_n1_0_%=:\n\t"
      "s_waitcnt lgkmcnt(6)\n\t"
      "v_mfma_f32_16x16x32_bf16 v[56:59], a[56:59], v[32:35], 0\n\t"
      "ds_read_b128 a[88:91], v10 offset:10240\n\t"
      "v_mfma_f32_16x16x32_bf16 v[60:63], a[60:63], v[32:35], 0\n\t"
      "ds_read_b128 a[92:95], v10 offset:11264\n\t"
      "v_max_f32 v140, v48, v49\n\t"
      "v_max_f32 v141, v50, v51\n\t"
      ".Lkm_n2_0_%=:\n\t"
      "s_waitcnt lgkmcnt(6)\n\t"
      "v_mfma_f32_16x16x32_bf16 v[64:67], a[64:67], v[32:35], 0\n\t"
      "ds_read_b128 a[96:99], v10 offset:12288\n\t"
      "v_max3_f32 v140, v52, v53, v140\n\t"
      "v_max3_f32 v141, v54, v55, v141\n\t"
      "v_mfma_f32_16x16x32_bf16 v[68:71], a[68:71], v[32:35], 0\n\t"
      "ds_read_b128 a[100:103], v10 offset:13312\n\t"
      "v_max3_f32 v140, v56, v57, v140\n\t"
      "v_max3_f32 v141, v58, v59, v141\n\t"
      ".Lkm_n3_0_%=:\n\t"
      "s_waitcnt lgkmcnt(6)\n\t"
      "v_mfma_f32_16x16x32_bf16 v[72:75], a[72:75], v[32:35], 0\n\t"
      "ds_read_b128 a[104:107], v10 offset:14336\n\t"
      "v_max3_f32 v140, v60, v61, v140\n\t"
      "v_max3_f32 v141, v62, v63, v141\n\t"
      "v_mfma_f32_16x16x32_bf16 v[76:79], a[76:79], v[32:35], 0\n\t"
      "ds_read_b128 a[108:111], v10 offset:15360\n\t"
      "v_max3_f32 v140, v64, v65, v140\n\t"
      "v_max3_f32 v141, v66, v67, v141\n\t"
      ".Lkm_n4_0_%=:\n\t"
      "s_waitcnt lgkmcnt(6)\n\t"
      "v_mfma_f32_16x16x32_bf16 v[80:83], a[80:83], v[32:35], 0\n\t"
      "ds_read_b128 a[112:115], v10 offset:16384\n\t"
      "v_max3_f32 v140, v68, v69, v140\n\t"
      "v_max3_f32 v141, v70, v71, v141\n\t"
      "v_mfma_f32_16x16x32_bf16 v[84:87], a[84:87], v[32:35], 0\n\t"
      "ds_read_b128 a[116:119], v10 offset:17408\n\t"
      "v_max3_f32 v140, v72, v73, v140\n\t"
      "v_max3_f32 v141, v74, v75, v141\n\t"
      ".Lkm_n5_0_%=:\n\t"
      "s_waitcnt lgkmcnt(6)\n\t"
      "v_mfma_f32_16x16x32_bf16 v[88:91], a[88:91], v[32:35], 0\n\t"
      "v_max3_f32 v140, v76, v77, v140\n\t"
      "v_max3_f32 v141, v78, v79, v141\n\t"
      "v_mfma_f32_16x16x32_bf16 v[92:95], a[92:95], v[32:35], 0\n\t"
      "v_max3_f32 v140, v80, v81, v140\n\t"
      "v_max3_f32 v141, v82, v83, v141\n\t"
      ".Lkm_n6_0_%=:\n\t"
      "s_waitcnt lgkmcnt(4)\n\t"
      "v_mfma_f32_16x16x32_bf16 v[96:99], a[96:99], v[32:35], 0\n\t"
      "v_max3_f32 v140, v84, v85, v140\n\t"
      "v_max3_f32 v141, v86, v87, v141\n\t"
      "v_mfma_f32_16x16x32_bf16 v[100:103], a[100:103], v[32:35], 0\n\t"
      "v_max3_f32 v140, v88, v89, v140\n\t"
      "v_max3_f32 v141, v90, v91, v141\n\t"
      ".Lkm_n7_0_%=:\n\t"
      "s_waitcnt lgkmcnt(2)\n\t"
      "v_mfma_f32_16x16x32_bf16 v[104:107], a[104:107], v[32:35], 0\n\t"
      "v_max3_f32 v140, v92, v93, v140\n\t"
      "v_max3_f32 v141, v94, v95, v141\n\t"
      "v_mfma_f32_16x16x32_bf16 v[108:111], a[108:111], v[32:35], 0\n\t"
      "v_max3_f32 v140, v96, v97, v140\n\t"
      "v_max3_f32 v141, v98, v99, v141\n\t"
      ".Lkm_n8_0_%=:\n\t"
      "s_waitcnt lgkmcnt(0)\n\t"
      "v_mfma_f32_16x16x32_bf16 v[112:115], a[112:115], v[32:35], v[120:123]\n\t"
      "v_max3_f32 v140, v100, v101, v140\n\t"
      "v_max3_f32 v141, v102, v103, v141\n\t"
      "s_waitcnt vmcnt(0)\n\t"
      "s_barrier\n\t"
      "ds_read_b64_tr_b16 a[120:121], v18 offset:32768\n\t"
      "ds_read_b64_tr_b16 a[122:123], v18 offset:33792\n\t"
      "ds_read_b64_tr_b16 a[124:125], v18 offset:32776\n\t"
      "ds_read_b64_tr_b16 a[126:127], v18 offset:33800\n\t"
      "ds_read_b64_tr_b16 a[128:129], v18 offset:34816\n\t"
      "ds_read_b64_tr_b16 a[130:131], v18 offset:35840\n\t"
      "ds_read_b64_tr_b16 a[132:133], v18 offset:34824\n\t"
      "ds_read_b64_tr_b16 a[134:135], v18 offset:35848\n\t"
      "v_max3_f32 v140, v104, v105, v140\n\t"
      "v_max3_f32 v141, v106, v107, v141\n\t"
      "v_max3_f32 v140, v108, v109, v140\n\t"
      "v_max3_f32 v141, v110, v111, v141\n\t"
      "v_max3_f32 v140, v112, v113, v140\n\t"
      "v_max3_f32 v141, v114, v115, v141\n\t"
      "v_max_f32 v140, v140, v141\n\t"
      "v_mov_b32 v141, v140\n\t"
      "s_nop 1\n\t"
      "v_permlane32_swap_b32 v140, v141\n\t"
      "v_max_f32 v140, v140, v141\n\t"
      "v_mov_b32 v141, v140\n\t"
      "s_nop 1\n\t"
      "v_permlane16_swap_b32 v140, v141\n\t"
      "v_max_f32 v140, v140, v141\n\t"
      "v_mul_f32 v140, s80, v140\n\t"
      "v_pk_fma_f32 v[48:49], v[48:49], s[80:81], v[140:141] op_sel_hi:[1,0,0] neg_lo:[0,0,1] neg_hi:[0,0,1]\n\t"
      "v_pk_fma_f32 v[50:51], v[50:51], s[80:81], v[140:141] op_sel_hi:[1,0,0] neg_lo:[0,0,1] neg_hi:[0,0,1]\n\t"
      "v_exp_f32 v48, v48\n\t"
      "v_pk_fma_f32 v[52:53], v[52:53], s[80:81], v[140:141] op_sel_hi:[1,0,0] neg_lo:[0,0,1] neg_hi:[0,0,1]\n\t"
      "v_exp_f32 v49, v49\n\t"
      "v_exp_f32 v50, v50\n\t"
      "v_pk_fma_f32 v[54:55], v[54:55], s[80:81], v[140:141] op_sel_hi:[1,0,0] neg_lo:[0,0,1] neg_hi:[0,0,1]\n\t"
      "v_exp_f32 v51, v51\n\t"
      "v_exp_f32 v52, v52\n\t"
      "v_exp_f32 v53, v53\n\t"
      "v_exp_f32 v54, v54\n\t"
      "v_exp_f32 v55, v55\n\t"
      "v_cvt_pk_bf16_f32 v48, v48, v49\n\t"
      "v_cvt_pk_bf16_f32 v49, v50, v51\n\t"
      "v_cvt_pk_bf16_f32 v50, v52, v53\n\t"
      "v_cvt_pk_bf16_f32 v51, v54, v55\n\t"
      "buffer_store_dwordx4 v[144:147], v1, s[44:47], s71 offen offset:0 sc1\n\t"
      "buffer_store_dwordx4 v[148:151], v1, s[44:47], s71 offen offset:1024 sc1\n\t"
      "v_pk_fma_f32 v[56:57], v[56:57], s[80:81], v[140:141] op_sel_hi:[1,0,0] neg_lo:[0,0,1] neg_hi:[0,0,1]\n\t"
      "v_pk_fma_f32 v[58:59], v[58:59], s[80:81], v[140:141] op_sel_hi:[1,0,0] neg_lo:[0,0,1] neg_hi:[0,0,1]\n\t"
      "v_exp_f32 v56, v56\n\t"
      "v_pk_fma_f32 v[60:61], v[60:61], s[80:81], v[140:141] op_sel_hi:[1,0,0] neg_lo:[0,0,1] neg_hi:[0,0,1]\n\t"
      "s_waitcnt lgkmcnt(4)\n\t"
      "v_mfma_f32_16x16x32_bf16 v[128:131], a[120:123], v[48:51], 0\n\t"
      "v_exp_f32 v57, v57\n\t"
      "v_exp_f32 v58, v58\n\t"
      "v_pk_fma_f32 v[62:63], v[62:63], s[80:81], v[140:141] op_sel_hi:[1,0,0] neg_lo:[0,0,1] neg_hi:[0,0,1]\n\t"
      "v_exp_f32 v59, v59\n\t"
      "v_mfma_f32_16x16x32_bf16 v[132:135], a[124:127], v[48:51], 0\n\t"
      "ds_read_b64_tr_b16 a[136:137], v18 offset:36864\n\t"
      "ds_read_b64_tr_b16 a[138:139], v18 offset:37888\n\t"
      "ds_read_b64_tr_b16 a[140:141], v18 offset:36872\n\t"
      "ds_read_b64_tr_b16 a[142:143], v18 offset:37896\n\t"
      "v_exp_f32 v60, v60\n\t"
      "v_exp_f32 v61, v61\n\t"
      "v_exp_f32 v62, v62\n\t"
      "v_exp_f32 v63, v63\n\t"
      "v_mfma_f32_16x16x32_bf16 v[136:139], a[192:195], v[48:51], 0\n\t"
      "v_cvt_pk_bf16_f32 v56, v56, v57\n\t"
      "v_cvt_pk_bf16_f32 v57, v58, v59\n\t"
      "v_cvt_pk_bf16_f32 v58, v60, v61\n\t"
      "v_cvt_pk_bf16_f32 v59, v62, v63\n\t"
      "buffer_store_dwordx4 v[152:155], v1, s[44:47], s71 offen offset:2048 sc1\n\t"
      "v_pk_fma_f32 v[64:65], v[64:65], s[80:81], v[140:141] op_sel_hi:[1,0,0] neg_lo:[0,0,1] neg_hi:[0,0,1]\n\t"
      "v_pk_fma_f32 v[66:67], v[66:67], s[80:81], v[140:141] op_sel_hi:[1,0,0] neg_lo:[0,0,1] neg_hi:[0,0,1]\n\t"
      "v_exp_f32 v64, v64\n\t"
      "v_pk_fma_f32 v[68:69], v[68:69], s[80:81], v[140:141] op_sel_hi:[1,0,0] neg_lo:[0,0,1] neg_hi:[0,0,1]\n\t"
      "s_waitcnt lgkmcnt(4)\n\t"
      "v_mfma_f32_16x16x32_bf16 v[128:131], a[128:131], v[56:59], v[128:131]\n\t"
      "v_exp_f32 v65, v65\n\t"
      "v_exp_f32 v66, v66\n\t"
      "v_pk_fma_f32 v[70:71], v[70:71], s[80:81], v[140:141] op_sel_hi:[1,0,0] neg_lo:[0,0,1] neg_hi:[0,0,1]\n\t"
      "v_exp_f32 v67, v67\n\t"
      "v_mfma_f32_16x16x32_bf16 v[132:135], a[132:135], v[56:59], v[132:135]\n\t"
      "ds_read_b64_tr_b16 a[144:145], v18 offset:38912\n\t"
      "ds_read_b64_tr_b16 a[146:147], v18 offset:39936\n\t"
      "ds_read_b64_tr_b16 a[148:149], v18 offset:38920\n\t"
      "ds_read_b64_tr_b16 a[150:151], v18 offset:39944\n\t"
      "v_exp_f32 v68, v68\n\t"
      "v_exp_f32 v69, v69\n\t"
      "v_exp_f32 v70, v70\n\t"
      "v_exp_f32 v71, v71\n\t"
      "v_mfma_f32_16x16x32_bf16 v[136:139], a[192:195], v[56:59], v[136:139]\n\t"
      "v_cvt_pk_bf16_f32 v64, v64, v65\n\t"
      "v_cvt_pk_bf16_f32 v65, v66, v67\n\t"
      "v_cvt_pk_bf16_f32 v66, v68, v69\n\t"
      "v_cvt_pk_bf16_f32 v67, v70, v71\n\t"
      "buffer_store_dwordx4 v[156:159], v1, s[44:47], s71 offen offset:3072 sc1\n\t"
      "v_pk_fma_f32 v[72:73], v[72:73], s[80:81], v[140:141] op_sel_hi:[1,0,0] neg_lo:[0,0,1] neg_hi:[0,0,1]\n\t"
      "v_pk_fma_f32 v[74:75], v[74:75], s[80:81], v[140:141] op_sel_hi:[1,0,0] neg_lo:[0,0,1] neg_hi:[0,0,1]\n\t"
      "v_exp_f32 v72, v72\n\t"
      "v_pk_fma_f32 v[76:77], v[76:77], s[80:81], v[140:141] op_sel_hi:[1,0,0] neg_lo:[0,0,1] neg_hi:[0,0,1]\n\t"
      "s_waitcnt lgkmcnt(4)\n\t"
      "v_mfma_f32_16x16x32_bf16 v[128:131], a[136:139], v[64:67], v[128:131]\n\t"
      "v_exp_f32 v73, v73\n\t"
      "v_exp_f32 v74, v74\n\t"
      "v_pk_fma_f32 v[78:79], v[78:79], s[80:81], v[140:141] op_sel_hi:[1,0,0] neg_lo:[0,0,1] neg_hi:[0,0,1]\n\t"
      "v_exp_f32 v75, v75\n\t"
      "v_mfma_f32_16x16x32_bf16 v[132:135], a[140:143], v[64:67], v[132:135]\n\t"
      "ds_read_b64_tr_b16 a[152:153], v18 offset:40960\n\t"
      "ds_read_b64_tr_b16 a[154:155], v18 offset:41984\n\t"
      "ds_read_b64_tr_b16 a[156:157], v18 offset:40968\n\t"
      "ds_read_b64_tr_b16 a[158:159], v18 offset:41992\n\t"
      "v_exp_f32 v76, v76\n\t"
      "v_exp_f32 v77, v77\n\t"
      "v_exp_f32 v78, v78\n\t"
      "v_exp_f32 v79, v79\n\t"
      "v_mfma_f32_16x16x32_bf16 v[136:139], a[192:195], v[64:67], v[136:139]\n\t"
      "v_cvt_pk_bf16_f32 v72, v72, v73\n\t"
      "v_cvt_pk_bf16_f32 v73, v74, v75\n\t"
      "v_cvt_pk_bf16_f32 v74, v76, v77\n\t"
      "v_cvt_pk_bf16_f32 v75, v78, v79\n\t"
      "buffer_store_dwordx4 v[176:179], v1, s[44:47], s72 offen offset:0 sc1\n\t"
      "buffer_store_dwordx4 v[180:183], v1, s[44:47], s72 offen offset:1024 sc1\n\t"
      "v_pk_fma_f32 v[80:81], v[80:81], s[80:81], v[140:141] op_sel_hi:[1,0,0] neg_lo:[0,0,1] neg_hi:[0,0,1]\n\t"
      "v_pk_fma_f32 v[82:83], v[82:83], s[80:81], v[140:141] op_sel_hi:[1,0,0] neg_lo:[0,0,1] neg_hi:[0,0,1]\n\t"
      "v_exp_f32 v80, v80\n\t"
      "v_pk_fma_f32 v[84:85], v[84:85], s[80:81], v[140:141] op_sel_hi:[1,0,0] neg_lo:[0,0,1] neg_hi:[0,0,1]\n\t"
      "s_waitcnt lgkmcnt(4)\n\t"
      "v_mfma_f32_16x16x32_bf16 v[128:131], a[144:147], v[72:75], v[128:131]\n\t"
      "v_exp_f32 v81, v81\n\t"
      "v_exp_f32 v82, v82\n\t"
      "v_pk_fma_f32 v[86:87], v[86:87], s[80:81], v[140:141] op_sel_hi:[1,0,0] neg_lo:[0,0,1] neg_hi:[0,0,1]\n\t"
      "v_exp_f32 v83, v83\n\t"
      "v_mfma_f32_16x16x32_bf16 v[132:135], a[148:151], v[72:75], v[132:135]\n\t"
      "ds_read_b64_tr_b16 a[160:161], v18 offset:43008\n\t"
      "ds_read_b64_tr_b16 a[162:163], v18 offset:44032\n\t"
      "ds_read_b64_tr_b16 a[164:165], v18 offset:43016\n\t"
      "ds_read_b64_tr_b16 a[166:167], v18 offset:44040\n\t"
      "v_exp_f32 v84, v84\n\t"
      "v_exp_f32 v85, v85\n\t"
      "v_exp_f32 v86, v86\n\t"
      "v_exp_f32 v87, v87\n\t"
      "v_mfma_f32_16x16x32_bf16 v[136:139], a[192:195], v[72:75], v[136:139]\n\t"
      "v_cvt_pk_bf16_f32 v80, v80, v81\n\t"
      "v_cvt_pk_bf16_f32 v81, v82, v83\n\t"
      "v_cvt_pk_bf16_f32 v82, v84, v85\n\t"
      "v_cvt_pk_bf16_f32 v83, v86, v87\n\t"
      "buffer_store_dwordx4 v[184:187], v1, s[44:47], s72 offen offset:2048 sc1\n\t"
      "v_pk_fma_f32 v[88:89], v[88:89], s[80:81], v[140:141] op_sel_hi:[1,0,0] neg_lo:[0,0,1] neg_hi:[0,0,1]\n\t"
      "v_pk_fma_f32 v[90:91], v[90:91], s[80:81], v[140:141] op_sel_hi:[1,0,0] neg_lo:[0,0,1] neg_hi:[0,0,1]\n\t"
      "v_exp_f32 v88, v88\n\t"
      "v_pk_fma_f32 v[92:93], v[92:93], s[80:81], v[140:141] op_sel_hi:[1,0,0] neg_lo:[0,0,1] neg_hi:[0,0,1]\n\t"
      "s_waitcnt lgkmcnt(4)\n\t"
      "v_mfma_f32_16x16x32_bf16 v[128:131], a[152:155], v[80:83], v[128:131]\n\t"
      "v_exp_f32 v89, v89\n\t"
      "v_exp_f32 v90, v90\n\t"
      "v_pk_fma_f32 v[94:95], v[94:95], s[80:81], v[140:141] op_sel_hi:[1,0,0] neg_lo:[0,0,1] neg_hi:[0,0,1]\n\t"
      "v_exp_f32 v91, v91\n\t"
      "v_mfma_f32_16x16x32_bf16 v[132:135], a[156:159], v[80:83], v[132:135]\n\t"
      "ds_read_b64_tr_b16 a[168:169], v18 offset:45056\n\t"
      "ds_read_b64_tr_b16 a[170:171], v18 offset:46080\n\t"
      "ds_read_b64_tr_b16 a[172:173], v18 offset:45064\n\t"
      "ds_read_b64_tr_b16 a[174:175], v18 offset:46088\n\t"
      "v_exp_f32 v92, v92\n\t"
      "v_exp_f32 v93, v93\n\t"
      "v_exp_f32 v94, v94\n\t"
      "v_exp_f32 v95, v95\n\t"
      "v_mfma_f32_16x16x32_bf16 v[136:139], a[192:195], v[80:83], v[136:139]\n\t"
      "v_cvt_pk_bf16_f32 v88, v88, v89\n\t"
      "v_cvt_pk_bf16_f32 v89, v90, v91\n\t"
      "v_cvt_pk_bf16_f32 v90, v92, v93\n\t"
      "v_cvt_pk_bf16_f32 v91, v94, v95\n\t"
      "buffer_store_dwordx4 v[188:191], v1, s[44:47], s72 offen offset:3072 sc1\n\t"
      "v_pk_fma_f32 v[96:97], v[96:97], s[80:81], v[140:141] op_sel_hi:[1,0,0] neg_lo:[0,0,1] neg_hi:[0,0,1]\n\t"
      "v_pk_fma_f32 v[98:99], v[98:99], s[80:81], v[140:141] op_sel_hi:[1,0,0] neg_lo:[0,0,1] neg_hi:[0,0,1]\n\t"
      "v_exp_f32 v96, v96\n\t"
      "v_pk_fma_f32 v[100:101], v[100:101], s[80:81], v[140:141] op_sel_hi:[1,0,0] neg_lo:[0,0,1] neg_hi:[0,0,1]\n\t"
      "s_waitcnt lgkmcnt(4)\n\t"
      "v_mfma_f32_16x16x32_bf16 v[128:131], a[160:163], v[88:91], v[128:131]\n\t"
      "v_exp_f32 v97, v97\n\t"
      "v_exp_f32 v98, v98\n\t"
      "v_pk_fma_f32 v[102:103], v[102:103], s[80:81], v[140:141] op_sel_hi:[1,0,0] neg_lo:[0,0,1] neg_hi:[0,0,1]\n\t"
      "v_exp_f32 v99, v99\n\t"
      "v_mfma_f32_16x16x32_bf16 v[132:135], a[164:167], v[88:91], v[132:135]\n\t"
      "ds_read_b64_tr_b16 a[176:177], v18 offset:47104\n\t"
      "ds_read_b64_tr_b16 a[178:179], v18 offset:48128\n\t"
      "ds_read_b64_tr_b16 a[180:181], v18 offset:47112\n\t"
      "ds_read_b64_tr_b16 a[182:183], v18 offset:48136\n\t"
      "v_exp_f32 v100, v100\n\t"
      "v_exp_f32 v101, v101\n\t"
      "v_exp_f32 v102, v102\n\t"
      "v_exp_f32 v103, v103\n\t"
      "v_mfma_f32_16x16x32_bf16 v[136:139], a[192:195], v[88:91], v[136:139]\n\t"
      "v_cvt_pk_bf16_f32 v96, v96, v97\n\t"
      "v_cvt_pk_bf16_f32 v97, v98, v99\n\t"
      "v_cvt_pk_bf16_f32 v98, v100, v101\n\t"
      "v_cvt_pk_bf16_f32 v99, v102, v103\n\t"
      "buffer_store_dwordx4 v[208:211], v1, s[44:47], s73 offen offset:0 sc1\n\t"
      "buffer_store_dwordx4 v[212:215], v1, s[44:47], s73 offen offset:1024 sc1\n\t"
      "v_pk_fma_f32 v[104:105], v[104:105], s[80:81], v[140:141] op_sel_hi:[1,0,0] neg_lo:[0,0,1] neg_hi:[0,0,1]\n\t"
      "v_pk_fma_f32 v[106:107], v[106:107], s[80:81], v[140:141] op_sel_hi:[1,0,0] neg_lo:[0,0,1] neg_hi:[0,0,1]\n\t"
      "v_exp_f32 v104, v104\n\t"
      "v_pk_fma_f32 v[108:109], v[108:109], s[80:81], v[140:141] op_sel_hi:[1,0,0] neg_lo:[0,0,1] neg_hi:[0,0,1]\n\t"
      "s_waitcnt lgkmcnt(4)\n\t"
      "v_mfma_f32_16x16x32_bf16 v[128:131], a[168:171], v[96:99], v[128:131]\n\t"
      "v_exp_f32 v105, v105\n\t"
      "v_exp_f32 v106, v106\n\t"
      "v_pk_fma_f32 v[110:111], v[110:111], s[80:81], v[140:141] op_sel_hi:[1,0,0] neg_lo:[0,0,1] neg_hi:[0,0,1]\n\t"
      "v_exp_f32 v107, v107\n\t"
      "v_mfma_f32_16x16x32_bf16 v[132:135], a[172:175], v[96:99], v[132:135]\n\t"
      "ds_read_b64_tr_b16 a[184:185], v18 offset:49152\n\t"
      "ds_read_b64_tr_b16 a[186:187], v18 offset:50176\n\t"
      "ds_read_b64_tr_b16 a[188:189], v18 offset:49160\n\t"
      "ds_read_b64_tr_b16 a[190:191], v18 offset:50184\n\t"
      "v_exp_f32 v108, v108\n\t"
      "v_exp_f32 v109, v109\n\t"
      "v_exp_f32 v110, v110\n\t"
      "v_exp_f32 v111, v111\n\t"
      "v_mfma_f32_16x16x32_bf16 v[136:139], a[192:195], v[96:99], v[136:139]\n\t"
      "v_cvt_pk_bf16_f32 v104, v104, v105\n\t"
      "v_cvt_pk_bf16_f32 v105, v106, v107\n\t"
      "v_cvt_pk_bf16_f32 v106, v108, v109\n\t"
      "v_cvt_pk_bf16_f32 v107, v110, v111\n\t"
      "buffer_store_dwordx4 v[216:219], v1, s[44:47], s73 offen offset:2048 sc1\n\t"
      "v_pk_fma_f32 v[112:113], v[112:113], s[80:81], v[140:141] op_sel_hi:[1,0,0] neg_lo:[0,0,1] neg_hi:[0,0,1]\n\t"
      "v_pk_fma_f32 v[114:115], v[114:115], s[80:81], v[140:141] op_sel_hi:[1,0,0] neg_lo:[0,0,1] neg_hi:[0,0,1]\n\t"
      "s_waitcnt lgkmcnt(4)\n\t"
      "v_mfma_f32_16x16x32_bf16 v[128:131], a[176:179], v[104:107], v[128:131]\n\t"
      "v_exp_f32 v112, v112\n\t"
      "v_exp_f32 v113, v113\n\t"
      "v_mfma_f32_16x16x32_bf16 v[132:135], a[180:183], v[104:107], v[132:135]\n\t"
      "v_exp_f32 v114, v114\n\t"
      "v_exp_f32 v115, v115\n\t"
      "v_mfma_f32_16x16x32_bf16 v[136:139], a[192:195], v[104:107], v[136:139]\n\t"
      "v_cvt_pk_bf16_f32 v112, v112, v113\n\t"
      "v_cvt_pk_bf16_f32 v113, v114, v115\n\t"
      "v_mov_b64 v[114:115], 0\n\t"
      "buffer_store_dwordx4 v[220:223], v1, s[44:47], s73 offen offset:3072 sc1\n\t"
      "s_waitcnt lgkmcnt(0)\n\t"
      "v_mfma_f32_16x16x32_bf16 v[128:131], a[184:187], v[112:115], v[128:131]\n\t"
      "v_mfma_f32_16x16x32_bf16 v[132:135], a[188:191], v[112:115], v[132:135]\n\t"
      "v_mfma_f32_16x16x32_bf16 v[136:139], a[192:195], v[112:115], v[136:139]\n\t"
      "s_cmp_eq_u32 %17, 0\n\t"
      "s_cbranch_scc0 .Lkm_n0_1_%=\n\t"
      "v_mfma_f32_16x16x32_bf16 v[48:51], a[52:55], v[36:39], v[124:127]\n\t"
      "v_mfma_f32_16x16x32_bf16 v[52:55], a[56:59], v[36:39], v[124:127]\n\t"
      "v_mfma_f32_16x16x32_bf16 v[56:59], a[60:63], v[36:39], v[124:127]\n\t"
      "s_cmp_eq_u32 s60, 6\n\t"
      "s_cbranch_scc1 .Lkm_n2_1_%=\n\t"
      "v_mfma_f32_16x16x32_bf16 v[60:63], a[64:67], v[36:39], v[124:127]\n\t"
      "s_nop 2\n\t"
      "v_max_f32 v140, v48, v49\n\t"
      "v_max_f32 v141, v50, v51\n\t"
      "v_mfma_f32_16x16x32_bf16 v[64:67], a[68:71], v[36:39], v[124:127]\n\t"
      "v_max3_f32 v140, v52, v53, v140\n\t"
      "v_max3_f32 v141, v54, v55, v141\n\t"
      "v_mfma_f32_16x16x32_bf16 v[68:71], a[72:75], v[36:39], v[124:127]\n\t"
      "v_max3_f32 v140, v56, v57, v140\n\t"
      "v_max3_f32 v141, v58, v59, v141\n\t"
      "v_mfma_f32_16x16x32_bf16 v[72:75], a[76:79], v[36:39], v[124:127]\n\t"
      "v_max3_f32 v140, v60, v61, v140\n\t"
      "v_max3_f32 v141, v62, v63, v141\n\t"
      "s_cmp_eq_u32 s60, 4\n\t"
      "s_cbranch_scc1 .Lkm_n4_1_%=\n\t"
      "v_mfma_f32_16x16x32_bf16 v[76:79], a[80:83], v[36:39], v[124:127]\n\t"
      "v_max3_f32 v140, v64, v65, v140\n\t"
      "v_max3_f32 v141, v66, v67, v141\n\t"
      "v_mfma_f32_16x16x32_bf16 v[80:83], a[84:87], v[36:39], v[124:127]\n\t"
      "v_max3_f32 v140, v68, v69, v140\n\t"
      "v_max3_f32 v141, v70, v71, v141\n\t"
      "v_mfma_f32_16x16x32_bf16 v[84:87], a[88:91], v[36:39], v[124:127]\n\t"
      "v_max3_f32 v140, v72, v73, v140\n\t"
      "v_max3_f32 v141, v74, v75, v141\n\t"
      "v_mfma_f32_16x16x32_bf16 v[88:91], a[92:95], v[36:39], v[124:127]\n\t"
      "v_max3_f32 v140, v76, v77, v140\n\t"
      "v_max3_f32 v141, v78, v79, v141\n\t"
      "s_cmp_eq_u32 s60, 2\n\t"
      "s_cbranch_scc1 .Lkm_n6_1_%=\n\t"
      "v_mfma_f32_16x16x32_bf16 v[92:95], a[96:99], v[36:39], v[124:127]\n\t"
      "v_max3_f32 v140, v80, v81, v140\n\t"
      "v_max3_f32 v141, v82, v83, v141\n\t"
      "v_mfma_f32_16x16x32_bf16 v[96:99], a[100:103], v[36:39], v[124:127]\n\t"
      "v_max3_f32 v140, v84, v85, v140\n\t"
      "v_max3_f32 v141, v86, v87, v141\n\t"
      "v_mfma_f32_16x16x32_bf16 v[100:103], a[104:107], v[36:39], v[124:127]\n\t"
      "v_max3_f32 v140, v88, v89, v140\n\t"
      "v_max3_f32 v141, v90, v91, v141\n\t"
      "v_mfma_f32_16x16x32_bf16 v[104:107], a[108:111], v[36:39], v[124:127]\n\t"
      "v_max3_f32 v140, v92, v93, v140\n\t"
      "v_max3_f32 v141, v94, v95, v141\n\t"
      "s_branch .Lkm_n8_1_%=\n\t"
      ".Lkm_n0_1_%=:\n\t"
      "v_mfma_f32_16x16x32_bf16 v[48:51], a[52:55], v[36:39], v[116:119]\n\t"
      ".Lkm_n1_1_%=:\n\t"
      "v_mfma_f32_16x16x32_bf16 v[52:55], a[56:59], v[36:39], 0\n\t"
      "v_mfma_f32_16x16x32_bf16 v[56:59], a[60:63], v[36:39], 0\n\t"
      ".Lkm_n2_1_%=:\n\t"
      "v_mfma_f32_16x16x32_bf16 v[60:63], a[64:67], v[36:39], 0\n\t"
      "s_nop 4\n\t"
      "v_max_f32 v140, v48, v49\n\t"
      "v_max_f32 v141, v50, v51\n\t"
      "v_mfma_f32_16x16x32_bf16 v[64:67], a[68:71], v[36:39], 0\n\t"
      "v_max3_f32 v140, v52, v53, v140\n\t"
      "v_max3_f32 v141, v54, v55, v141\n\t"
      ".Lkm_n3_1_%=:\n\t"
      "v_mfma_f32_16x16x32_bf16 v[68:71], a[72:75], v[36:39], 0\n\t"
      "v_max3_f32 v140, v56, v57, v140\n\t"
      "v_max3_f32 v141, v58, v59, v141\n\t"
      "v_mfma_f32_16x16x32_bf16 v[72:75], a[76:79], v[36:39], 0\n\t"
      "v_max3_f32 v140, v60, v61, v140\n\t"
      "v_max3_f32 v141, v62, v63, v141\n\t"
      ".Lkm_n4_1_%=:\n\t"
      "v_mfma_f32_16x16x32_bf16 v[76:79], a[80:83], v[36:39], 0\n\t"
      "v_max3_f32 v140, v64, v65, v140\n\t"
      "v_max3_f32 v141, v66, v67, v141\n\t"
      "v_mfma_f32_16x16x32_bf16 v[80:83], a[84:87], v[36:39], 0\n\t"
      "v_max3_f32 v140, v68, v69, v140\n\t"
      "v_max3_f32 v141, v70, v71, v141\n\t"
      ".Lkm_n5_1_%=:\n\t"
      "v_mfma_f32_16x16x32_bf16 v[84:87], a[88:91], v[36:39], 0\n\t"
      "v_max3_f32 v140, v72, v73, v140\n\t"
      "v_max3_f32 v141, v74, v75, v141\n\t"
      "v_mfma_f32_16x16x32_bf16 v[88:91], a[92:95], v[36:39], 0\n\t"
      "v_max3_f32 v140, v76, v77, v140\n\t"
      "v_max3_f32 v141, v78, v79, v141\n\t"
      ".Lkm_n6_1_%=:\n\t"
      "v_mfma_f32_16x16x32_bf16 v[92:95], a[96:99], v[36:39], 0\n\t"
      "v_max3_f32 v140, v80, v81, v140\n\t"
      "v_max3_f32 v141, v82, v83, v141\n\t"
      "v_mfma_f32_16x16x32_bf16 v[96:99], a[100:103], v[36:39], 0\n\t"
      "v_max3_f32 v140, v84, v85, v140\n\t"
      "v_max3_f32 v141, v86, v87, v141\n\t"
      ".Lkm_n7_1_%=:\n\t"
      "v_mfma_f32_16x16x32_bf16 v[100:103], a[104:107], v[36:39], 0\n\t"
      "v_max3_f32 v140, v88, v89, v140\n\t"
      "v_max3_f32 v141, v90, v91, v141\n\t"
      "v_mfma_f32_16x16x32_bf16 v[104:107], a[108:111], v[36:39], 0\n\t"
      "v_max3_f32 v140, v92, v93, v140\n\t"
      "v_max3_f32 v141, v94, v95, v141\n\t"
      ".Lkm_n8_1_%=:\n\t"
      "v_mfma_f32_16x16x32_bf16 v[108:111], a[112:115], v[36:39], 0\n\t"
      "v_max3_f32 v140, v96, v97, v140\n\t"
      "v_max3_f32 v141, v98, v99, v141\n\t"
      "v_mfma_f32_16x16x32_bf16 v[112:115], a[116:119], v[36:39], v[120:123]\n\t"
      "v_max3_f32 v140, v100, v101, v140\n\t"
      "v_max3_f32 v141, v102, v103, v141\n\t"
      "v_max3_f32 v140, v104, v105, v140\n\t"
      "v_rcp_f32 v142, v136\n\t"
      "v_max3_f32 v141, v106, v107, v141\n\t"
      "v_pk_mul_f32 v[128:129], v[128:129], v[142:143] op_sel_hi:[1,0]\n\t"
      "v_max3_f32 v140, v108, v109, v140\n\t"
      "v_pk_mul_f32 v[130:131], v[130:131], v[142:143] op_sel_hi:[1,0]\n\t"
      "v_max3_f32 v141, v110, v111, v141\n\t"
      "v_pk_mul_f32 v[132:133], v[132:133], v[142:143] op_sel_hi:[1,0]\n\t"
      "v_max3_f32 v140, v112, v113, v140\n\t"
      "v_pk_mul_f32 v[134:135], v[134:135], v[142:143] op_sel_hi:[1,0]\n\t"
      "v_max3_f32 v141, v114, v115, v141\n\t"
      "v_cvt_pk_bf16_f32 v128, v128, v129\n\t"
      "v_max_f32 v140, v140, v141\n\t"
      "v_mov_b32 v141, v140\n\t"
      "v_cvt_pk_bf16_f32 v129, v130, v131\n\t"
      "v_cvt_pk_bf16_f32 v130, v132, v133\n\t"
      "v_permlane32_swap_b32 v140, v141\n\t"
      "v_cvt_pk_bf16_f32 v131, v134, v135\n\t"
      "v_max_f32 v140, v140, v141\n\t"
      "v_mov_b32 v141, v140\n\t"
      "buffer_store_dwordx4 v[128:131], v1, s[44:47], s82 offen offset:0 sc1\n\t"
      "s_nop 0\n\t"
      "v_permlane16_swap_b32 v140, v141\n\t"
      "v_max_f32 v140, v140, v141\n\t"
      "v_mul_f32 v140, s80, v140\n\t"
      "v_pk_fma_f32 v[48:49], v[48:49], s[80:81], v[140:141] op_sel_hi:[1,0,0] neg_lo:[0,0,1] neg_hi:[0,0,1]\n\t"
      "v_pk_fma_f32 v[50:51], v[50:51], s[80:81], v[140:141] op_sel_hi:[1,0,0] neg_lo:[0,0,1] neg_hi:[0,0,1]\n\t"
      "v_exp_f32 v48, v48\n\t"
      "v_exp_f32 v49, v49\n\t"
      "v_exp_f32 v50, v50\n\t"
      "v_exp_f32 v51, v51\n\t"
      "s_nop 0\n\t"
      "v_cvt_pk_bf16_f32 v51, v50, v51\n\t"
      "v_cvt_pk_bf16_f32 v50, v48, v49\n\t"
      "v_mov_b64 v[48:49], 0\n\t"
      "buffer_store_dwordx2 v[240:241], v16, s[44:47], s74 offen offset:0 sc1\n\t"
      "buffer_store_dwordx2 v[244:245], v16, s[44:47], s74 offen offset:512 sc1\n\t"
      "v_pk_fma_f32 v[52:53], v[52:53], s[80:81], v[140:141] op_sel_hi:[1,0,0] neg_lo:[0,0,1] neg_hi:[0,0,1]\n\t"
      "v_pk_fma_f32 v[54:55], v[54:55], s[80:81], v[140:141] op_sel_hi:[1,0,0] neg_lo:[0,0,1] neg_hi:[0,0,1]\n\t"
      "v_exp_f32 v52, v52\n\t"
      "v_pk_fma_f32 v[56:57], v[56:57], s[80:81], v[140:141] op_sel_hi:[1,0,0] neg_lo:[0,0,1] neg_hi:[0,0,1]\n\t"
      "v_mfma_f32_16x16x32_bf16 v[128:131], a[120:123], v[48:51], 0\n\t"
      "ds_read_b128 a[48:51], v10 offset:2048\n\t"
      "ds_read_b128 a[52:55], v10 offset:3072\n\t"
      "v_exp_f32 v53, v53\n\t"
      "v_exp_f32 v54, v54\n\t"
      "v_pk_fma_f32 v[58:59], v[58:59], s[80:81], v[140:141] op_sel_hi:[1,0,0] neg_lo:[0,0,1] neg_hi:[0,0,1]\n\t"
      "v_exp_f32 v55, v55\n\t"
      "v_mfma_f32_16x16x32_bf16 v[132:135], a[124:127], v[48:51], 0\n\t"
      "v_exp_f32 v56, v56\n\t"
      "v_exp_f32 v57, v57\n\t"
      "v_exp_f32 v58, v58\n\t"
      "v_exp_f32 v59, v59\n\t"
      "v_mfma_f32_16x16x32_bf16 v[136:139], a[192:195], v[48:51], 0\n\t"
      "ds_read_b64_tr_b16 a[120:121], v18 offset:34816\n\t"
      "ds_read_b64_tr_b16 a[122:123], v18 offset:35840\n\t"
      "ds_read_b64_tr_b16 a[124:125], v18 offset:34824\n\t"
      "ds_read_b64_tr_b16 a[126:127], v18 offset:35848\n\t"
      "v_cvt_pk_bf16_f32 v52, v52, v53\n\t"
      "v_cvt_pk_bf16_f32 v53, v54, v55\n\t"
      "v_cvt_pk_bf16_f32 v54, v56, v57\n\t"
      "v_cvt_pk_bf16_f32 v55, v58, v59\n\t"
      "buffer_store_dwordx2 v[248:249], v16, s[44:47], s74 offen offset:1024 sc1\n\t"
      "v_pk_fma_f32 v[60:61], v[60:61], s[80:81], v[140:141] op_sel_hi:[1,0,0] neg_lo:[0,0,1] neg_hi:[0,0,1]\n\t"
      "v_pk_fma_f32 v[62:63], v[62:63], s[80:81], v[140:141] op_sel_hi:[1,0,0] neg_lo:[0,0,1] neg_hi:[0,0,1]\n\t"
      "v_exp_f32 v60, v60\n\t"
      "v_pk_fma_f32 v[64:65], v[64:65], s[80:81], v[140:141] op_sel_hi:[1,0,0] neg_lo:[0,0,1] neg_hi:[0,0,1]\n\t"
      "v_mfma_f32_16x16x32_bf16 v[128:131], a[128:131], v[52:55], v[128:131]\n\t"
      "ds_read_b128 a[56:59], v10 offset:4096\n\t"
      "ds_read_b128 a[60:63], v10 offset:5120\n\t"
      "v_exp_f32 v61, v61\n\t"
      "v_exp_f32 v62, v62\n\t"
      "v_pk_fma_f32 v[66:67], v[66:67], s[80:81], v[140:141] op_sel_hi:[1,0,0] neg_lo:[0,0,1] neg_hi:[0,0,1]\n\t"
      "v_exp_f32 v63, v63\n\t"
      "v_mfma_f32_16x16x32_bf16 v[132:135], a[132:135], v[52:55], v[132:135]\n\t"
      "v_exp_f32 v64, v64\n\t"
      "v_exp_f32 v65, v65\n\t"
      "v_exp_f32 v66, v66\n\t"
      "v_exp_f32 v67, v67\n\t"
      "v_mfma_f32_16x16x32_bf16 v[136:139], a[192:195], v[52:55], v[136:139]\n\t"
      "ds_read_b64_tr_b16 a[128:129], v18 offset:36864\n\t"
      "ds_read_b64_tr_b16 a[130:131], v18 offset:37888\n\t"
      "ds_read_b64_tr_b16 a[132:133], v18 offset:36872\n\t"
      "ds_read_b64_tr_b16 a[134:135], v18 offset:37896\n\t"
      "v_cvt_pk_bf16_f32 v60, v60, v61\n\t"
      "v_cvt_pk_bf16_f32 v61, v62, v63\n\t"
      "v_cvt_pk_bf16_f32 v62, v64, v65\n\t"
      "v_cvt_pk_bf16_f32 v63, v66, v67\n\t"
      "buffer_store_dwordx2 v[252:253], v16, s[44:47], s74 offen offset:1536 sc1\n\t"
      "v_pk_fma_f32 v[68:69], v[68:69], s[80:81], v[140:141] op_sel_hi:[1,0,0] neg_lo:[0,0,1] neg_hi:[0,0,1]\n\t"
      "v_pk_fma_f32 v[70:71], v[70:71], s[80:81], v[140:141] op_sel_hi:[1,0,0] neg_lo:[0,0,1] neg_hi:[0,0,1]\n\t"
      "v_exp_f32 v68, v68\n\t"
      "v_pk_fma_f32 v[72:73], v[72:73], s[80:81], v[140:141] op_sel_hi:[1,0,0] neg_lo:[0,0,1] neg_hi:[0,0,1]\n\t"
      "s_waitcnt lgkmcnt(6)\n\t"
      "v_mfma_f32_16x16x32_bf16 v[128:131], a[136:139], v[60:63], v[128:131]\n\t"
      "ds_read_b128 a[64:67], v10 offset:6144\n\t"
      "ds_read_b128 a[68:71], v10 offset:7168\n\t"
      "v_exp_f32 v69, v69\n\t"
      "v_exp_f32 v70, v70\n\t"
      "v_pk_fma_f32 v[74:75], v[74:75], s[80:81], v[140:141] op_sel_hi:[1,0,0] neg_lo:[0,0,1] neg_hi:[0,0,1]\n\t"
      "v_exp_f32 v71, v71\n\t"
      "v_mfma_f32_16x16x32_bf16 v[132:135], a[140:143], v[60:63], v[132:135]\n\t"
      "v_exp_f32 v72, v72\n\t"
      "v_exp_f32 v73, v73\n\t"
      "v_exp_f32 v74, v74\n\t"
      "v_exp_f32 v75, v75\n\t"
      "v_mfma_f32_16x16x32_bf16 v[136:139], a[192:195], v[60:63], v[136:139]\n\t"
      "ds_read_b64_tr_b16 a[136:137], v18 offset:38912\n\t"
      "ds_read_b64_tr_b16 a[138:139], v18 offset:39936\n\t"
      "ds_read_b64_tr_b16 a[140:141], v18 offset:38920\n\t"
      "ds_read_b64_tr_b16 a[142:143], v18 offset:39944\n\t"
      "v_cvt_pk_bf16_f32 v68, v68, v69\n\t"
      "v_cvt_pk_bf16_f32 v69, v70, v71\n\t"
      "v_cvt_pk_bf16_f32 v70, v72, v73\n\t"
      "v_cvt_pk_bf16_f32 v71, v74, v75\n\t"
      "v_pk_fma_f32 v[76:77], v[76:77], s[80:81], v[140:141] op_sel_hi:[1,0,0] neg_lo:[0,0,1] neg_hi:[0,0,1]\n\t"
      "v_pk_fma_f32 v[78:79], v[78:79], s[80:81], v[140:141] op_sel_hi:[1,0,0] neg_lo:[0,0,1] neg_hi:[0,0,1]\n\t"
      "v_exp_f32 v76, v76\n\t"
      "v_pk_fma_f32 v[80:81], v[80:81], s[80:81], v[140:141] op_sel_hi:[1,0,0] neg_lo:[0,0,1] neg_hi:[0,0,1]\n\t"
      "s_waitcnt lgkmcnt(6)\n\t"
      "v_mfma_f32_16x16x32_bf16 v[128:131], a[144:147], v[68:71], v[128:131]\n\t"
      "ds_read_b128 a[72:75], v10 offset:8192\n\t"
      "ds_read_b128 a[76:79], v10 offset:9216\n\t"
      "v_exp_f32 v77, v77\n\t"
      "v_exp_f32 v78, v78\n\t"
      "v_pk_fma_f32 v[82:83], v[82:83], s[80:81], v[140:141] op_sel_hi:[1,0,0] neg_lo:[0,0,1] neg_hi:[0,0,1]\n\t"
      "v_exp_f32 v79, v79\n\t"
      "v_mfma_f32_16x16x32_bf16 v[132:135], a[148:151], v[68:71], v[132:135]\n\t"
      "v_exp_f32 v80, v80\n\t"
      "v_exp_f32 v81, v81\n\t"
      "v_exp_f32 v82, v82\n\t"
      "v_exp_f32 v83, v83\n\t"
      "v_mfma_f32_16x16x32_bf16 v[136:139], a[192:195], v[68:71], v[136:139]\n\t"
      "ds_read_b64_tr_b16 a[144:145], v18 offset:40960\n\t"
      "ds_read_b64_tr_b16 a[146:147], v18 offset:41984\n\t"
      "ds_read_b64_tr_b16 a[148:149], v18 offset:40968\n\t"
      "ds_read_b64_tr_b16 a[150:151], v18 offset:41992\n\t"
      "v_cvt_pk_bf16_f32 v76, v76, v77\n\t"
      "v_cvt_pk_bf16_f32 v77, v78, v79\n\t"
      "v_cvt_pk_bf16_f32 v78, v80, v81\n\t"
      "v_cvt_pk_bf16_f32 v79, v82, v83\n\t"
      "s_waitcnt vmcnt(9)\n\t"
      "s_mov_b64 exec, 0xff\n\t"
      "buffer_store_dwordx4 a[252:255], v1, s[44:47], s70 offen sc1\n\t"
      "s_mov_b64 exec, -1\n\t"
      "v_pk_fma_f32 v[84:85], v[84:85], s[80:81], v[140:141] op_sel_hi:[1,0,0] neg_lo:[0,0,1] neg_hi:[0,0,1]\n\t"
      "v_pk_fma_f32 v[86:87], v[86:87], s[80:81], v[140:141] op_sel_hi:[1,0,0] neg_lo:[0,0,1] neg_hi:[0,0,1]\n\t"
      "v_exp_f32 v84, v84\n\t"
      "v_pk_fma_f32 v[88:89], v[88:89], s[80:81], v[140:141] op_sel_hi:[1,0,0] neg_lo:[0,0,1] neg_hi:[0,0,1]\n\t"
      "s_waitcnt lgkmcnt(6)\n\t"
      "v_mfma_f32_16x16x32_bf16 v[128:131], a[152:155], v[76:79], v[128:131]\n\t"
      "ds_read_b128 a[80:83], v10 offset:10240\n\t"
      "ds_read_b128 a[84:87], v10 offset:11264\n\t"
      "v_exp_f32 v85, v85\n\t"
      "v_exp_f32 v86, v86\n\t"
      "v_pk_fma_f32 v[90:91], v[90:91], s[80:81], v[140:141] op_sel_hi:[1,0,0] neg_lo:[0,0,1] neg_hi:[0,0,1]\n\t"
      "v_exp_f32 v87, v87\n\t"
      "v_mfma_f32_16x16x32_bf16 v[132:135], a[156:159], v[76:79], v[132:135]\n\t"
      "v_exp_f32 v88, v88\n\t"
      "v_exp_f32 v89, v89\n\t"
      "v_exp_f32 v90, v90\n\t"
      "v_exp_f32 v91, v91\n\t"
      "v_mfma_f32_16x16x32_bf16 v[136:139], a[192:195], v[76:79], v[136:139]\n\t"
      "ds_read_b64_tr_b16 a[152:153], v18 offset:43008\n\t"
      "ds_read_b64_tr_b16 a[154:155], v18 offset:44032\n\t"
      "ds_read_b64_tr_b16 a[156:157], v18 offset:43016\n\t"
      "ds_read_b64_tr_b16 a[158:159], v18 offset:44040\n\t"
      "v_cvt_pk_bf16_f32 v84, v84, v85\n\t"
      "v_cvt_pk_bf16_f32 v85, v86, v87\n\t"
      "v_cvt_pk_bf16_f32 v86, v88, v89\n\t"
      "v_cvt_pk_bf16_f32 v87, v90, v91\n\t"
      "s_getpc_b64 s[48:49]\n\t"
      ".Lkm_pc23_%=:\n\t"
      "s_sub_u32 s48, s48, .Lkm_pc23_%=-km_code_begin\n\t"
      "s_subb_u32 s49, s49, 0\n\t"
      "s_mov_b32 s50, km_code_end-km_code_begin\n\t"
      "s_mov_b32 s51, 0x20000\n\t"
      "s_lshr_b32 s64, %15, 3\n\t"
      "s_lshl_b32 s64, s64, 11\n\t"
      "v_and_b32 v19, 15, v0\n\t"
      "v_lshlrev_b32 v19, 7, v19\n\t"
      "v_add_u32 v19, s64, v19\n\t"
      "buffer_load_dword v23, v19, s[48:51], 0 offen\n\t"
      "v_pk_fma_f32 v[92:93], v[92:93], s[80:81], v[140:141] op_sel_hi:[1,0,0] neg_lo:[0,0,1] neg_hi:[0,0,1]\n\t"
      "v_pk_fma_f32 v[94:95], v[94:95], s[80:81], v[140:141] op_sel_hi:[1,0,0] neg_lo:[0,0,1] neg_hi:[0,0,1]\n\t"
      "v_exp_f32 v92, v92\n\t"
      "v_pk_fma_f32 v[96:97], v[96:97], s[80:81], v[140:141] op_sel_hi:[1,0,0] neg_lo:[0,0,1] neg_hi:[0,0,1]\n\t"
      "s_waitcnt lgkmcnt(6)\n\t"
      "v_mfma_f32_16x16x32_bf16 v[128:131], a[160:163], v[84:87], v[128:131]\n\t"
      "ds_read_b128 a[88:91], v10 offset:12288\n\t"
      "ds_read_b128 a[92:95], v10 offset:13312\n\t"
      "v_exp_f32 v93, v93\n\t"
      "v_exp_f32 v94, v94\n\t"
      "v_pk_fma_f32 v[98:99], v[98:99], s[80:81], v[140:141] op_sel_hi:[1,0,0] neg_lo:[0,0,1] neg_hi:[0,0,1]\n\t"
      "v_exp_f32 v95, v95\n\t"
      "v_mfma_f32_16x16x32_bf16 v[132:135], a[164:167], v[84:87], v[132:135]\n\t"
      "v_exp_f32 v96, v96\n\t"
      "v_exp_f32 v97, v97\n\t"
      "v_exp_f32 v98, v98\n\t"
      "v_exp_f32 v99, v99\n\t"
      "v_mfma_f32_16x16x32_bf16 v[136:139], a[192:195], v[84:87], v[136:139]\n\t"
      "ds_read_b64_tr_b16 a[160:161], v18 offset:45056\n\t"
      "ds_read_b64_tr_b16 a[162:163], v18 offset:46080\n\t"
      "ds_read_b64_tr_b16 a[164:165], v18 offset:45064\n\t"
      "ds_read_b64_tr_b16 a[166:167], v18 offset:46088\n\t"
      "v_cvt_pk_bf16_f32 v92, v92, v93\n\t"
      "v_cvt_pk_bf16_f32 v93, v94, v95\n\t"
      "v_cvt_pk_bf16_f32 v94, v96, v97\n\t"
      "v_cvt_pk_bf16_f32 v95, v98, v99\n\t"
      "v_pk_fma_f32 v[100:101], v[100:101], s[80:81], v[140:141] op_sel_hi:[1,0,0] neg_lo:[0,0,1] neg_hi:[0,0,1]\n\t"
      "v_pk_fma_f32 v[102:103], v[102:103], s[80:81], v[140:141] op_sel_hi:[1,0,0] neg_lo:[0,0,1] neg_hi:[0,0,1]\n\t"
      "v_exp_f32 v100, v100\n\t"
      "v_pk_fma_f32 v[104:105], v[104:105], s[80:81], v[140:141] op_sel_hi:[1,0,0] neg_lo:[0,0,1] neg_hi:[0,0,1]\n\t"
      "s_waitcnt lgkmcnt(6)\n\t"
      "v_mfma_f32_16x16x32_bf16 v[128:131], a[168:171], v[92:95], v[128:131]\n\t"
      "ds_read_b128 a[96:99], v10 offset:14336\n\t"
      "ds_read_b128 a[100:103], v10 offset:15360\n\t"
      "v_exp_f32 v101, v101\n\t"
      "v_exp_f32 v102, v102\n\t"
      "v_pk_fma_f32 v[106:107], v[106:107], s[80:81], v[140:141] op_sel_hi:[1,0,0] neg_lo:[0,0,1] neg_hi:[0,0,1]\n\t"
      "v_exp_f32 v103, v103\n\t"
      "v_mfma_f32_16x16x32_bf16 v[132:135], a[172:175], v[92:95], v[132:135]\n\t"
      "v_exp_f32 v104, v104\n\t"
      "v_exp_f32 v105, v105\n\t"
      "v_exp_f32 v106, v106\n\t"
      "v_exp_f32 v107, v107\n\t"
      "v_mfma_f32_16x16x32_bf16 v[136:139], a[192:195], v[92:95], v[136:139]\n\t"
      "ds_read_b64_tr_b16 a[168:169], v18 offset:47104\n\t"
      "ds_read_b64_tr_b16 a[170:171], v18 offset:48128\n\t"
      "ds_read_b64_tr_b16 a[172:173], v18 offset:47112\n\t"
      "ds_read_b64_tr_b16 a[174:175], v18 offset:48136\n\t"
      "v_cvt_pk_bf16_f32 v100, v100, v101\n\t"
      "v_cvt_pk_bf16_f32 v101, v102, v103\n\t"
      "v_cvt_pk_bf16_f32 v102, v104, v105\n\t"
      "v_cvt_pk_bf16_f32 v103, v106, v107\n\t"
      "s_add_u32 m0, s62, 0\n\t"
      "s_nop 0\n\t"
      "global_load_lds_dwordx4 v2, s[40:41]\n\t"
      "s_add_u32 m0, s62, 1024\n\t"
      "s_nop 0\n\t"
      "global_load_lds_dwordx4 v3, s[40:41]\n\t"
      "s_add_u32 s40, s40, 262144\n\t"
      "s_addc_u32 s41, s41, 0\n\t"
      "v_pk_fma_f32 v[108:109], v[108:109], s[80:81], v[140:141] op_sel_hi:[1,0,0] neg_lo:[0,0,1] neg_hi:[0,0,1]\n\t"
      "v_pk_fma_f32 v[110:111], v[110:111], s[80:81], v[140:141] op_sel_hi:[1,0,0] neg_lo:[0,0,1] neg_hi:[0,0,1]\n\t"
      "v_exp_f32 v108, v108\n\t"
      "v_pk_fma_f32 v[112:113], v[112:113], s[80:81], v[140:141] op_sel_hi:[1,0,0] neg_lo:[0,0,1] neg_hi:[0,0,1]\n\t"
      "s_waitcnt lgkmcnt(6)\n\t"
      "v_mfma_f32_16x16x32_bf16 v[128:131], a[176:179], v[100:103], v[128:131]\n\t"
      "ds_read_b128 a[104:107], v10 offset:16384\n\t"
      "ds_read_b128 a[108:111], v10 offset:17408\n\t"
      "v_exp_f32 v109, v109\n\t"
      "v_exp_f32 v110, v110\n\t"
      "v_pk_fma_f32 v[114:115], v[114:115], s[80:81], v[140:141] op_sel_hi:[1,0,0] neg_lo:[0,0,1] neg_hi:[0,0,1]\n\t"
      "v_exp_f32 v111, v111\n\t"
      "v_mfma_f32_16x16x32_bf16 v[132:135], a[180:183], v[100:103], v[132:135]\n\t"
      "v_exp_f32 v112, v112\n\t"
      "v_exp_f32 v113, v113\n\t"
      "v_exp_f32 v114, v114\n\t"
      "v_exp_f32 v115, v115\n\t"
      "v_mfma_f32_16x16x32_bf16 v[136:139], a[192:195], v[100:103], v[136:139]\n\t"
      "ds_read_b64_tr_b16 a[176:177], v18 offset:49152\n\t"
      "ds_read_b64_tr_b16 a[178:179], v18 offset:50176\n\t"
      "ds_read_b64_tr_b16 a[180:181], v18 offset:49160\n\t"
      "ds_read_b64_tr_b16 a[182:183], v18 offset:50184\n\t"
      "v_cvt_pk_bf16_f32 v108, v108, v109\n\t"
      "v_cvt_pk_bf16_f32 v109, v110, v111\n\t"
      "v_cvt_pk_bf16_f32 v110, v112, v113\n\t"
      "v_cvt_pk_bf16_f32 v111, v114, v115\n\t"
      "s_add_u32 m0, s62, 8192\n\t"
      "s_nop 0\n\t"
      "global_load_lds_dwordx4 v4, s[42:43]\n\t"
      "s_waitcnt lgkmcnt(6)\n\t"
      "v_mfma_f32_16x16x32_bf16 v[128:131], a[184:187], v[108:111], v[128:131]\n\t"
      "ds_read_b128 a[112:115], v10 offset:18432\n\t"
      "ds_read_b128 a[116:119], v10 offset:19456\n\t"
      "v_mfma_f32_16x16x32_bf16 v[132:135], a[188:191], v[108:111], v[132:135]\n\t"
      "v_mfma_f32_16x16x32_bf16 v[136:139], a[192:195], v[108:111], v[136:139]\n\t"
      "ds_read_b64_tr_b16 a[184:185], v18 offset:51200\n\t"
      "ds_read_b64_tr_b16 a[186:187], v18 offset:52224\n\t"
      "ds_read_b64_tr_b16 a[188:189], v18 offset:51208\n\t"
      "ds_read_b64_tr_b16 a[190:191], v18 offset:52232\n\t"
      "s_waitcnt lgkmcnt(4)\n\t"
      "s_cmp_eq_u32 %17, 0\n\t"
      "s_cbranch_scc0 .Lkm_n0_2_%=\n\t"
      "v_mfma_f32_16x16x32_bf16 v[48:51], a[48:51], v[40:43], v[124:127]\n\t"
      "v_mfma_f32_16x16x32_bf16 v[52:55], a[52:55], v[40:43], v[124:127]\n\t"
      "s_cmp_eq_u32 s60, 6\n\t"
      "s_cbranch_scc1 .Lkm_n1_2_%=\n\t"
      "v_mfma_f32_16x16x32_bf16 v[56:59], a[56:59], v[40:43], v[124:127]\n\t"
      "v_mfma_f32_16x16x32_bf16 v[60:63], a[60:63], v[40:43], v[124:127]\n\t"
      "s_nop 2\n\t"
      "v_max_f32 v140, v48, v49\n\t"
      "v_max_f32 v141, v50, v51\n\t"
      "v_mfma_f32_16x16x32_bf16 v[64:67], a[64:67], v[40:43], v[124:127]\n\t"
      "v_max3_f32 v140, v52, v53, v140\n\t"
      "v_max3_f32 v141, v54, v55, v141\n\t"
      "v_mfma_f32_16x16x32_bf16 v[68:71], a[68:71], v[40:43], v[124:127]\n\t"
      "v_max3_f32 v140, v56, v57, v140\n\t"
      "v_max3_f32 v141, v58, v59, v141\n\t"
      "s_cmp_eq_u32 s60, 4\n\t"
      "s_cbranch_scc1 .Lkm_n3_2_%=\n\t"
      "v_mfma_f32_16x16x32_bf16 v[72:75], a[72:75], v[40:43], v[124:127]\n\t"
      "v_max3_f32 v140, v60, v61, v140\n\t"
      "v_max3_f32 v141, v62, v63, v141\n\t"
      "v_mfma_f32_16x16x32_bf16 v[76:79], a[76:79], v[40:43], v[124:127]\n\t"
      "v_max3_f32 v140, v64, v65, v140\n\t"
      "v_max3_f32 v141, v66, v67, v141\n\t"
      "v_mfma_f32_16x16x32_bf16 v[80:83], a[80:83], v[40:43], v[124:127]\n\t"
      "v_max3_f32 v140, v68, v69, v140\n\t"
      "v_max3_f32 v141, v70, v71, v141\n\t"
      "v_mfma_f32_16x16x32_bf16 v[84:87], a[84:87], v[40:43], v[124:127]\n\t"
      "v_max3_f32 v140, v72, v73, v140\n\t"
      "v_max3_f32 v141, v74, v75, v141\n\t"
      "s_cmp_eq_u32 s60, 2\n\t"
      "s_cbranch_scc1 .Lkm_n5_2_%=\n\t"
      "v_mfma_f32_16x16x32_bf16 v[88:91], a[88:91], v[40:43], v[124:127]\n\t"
      "v_max3_f32 v140, v76, v77, v140\n\t"
      "v_max3_f32 v141, v78, v79, v141\n\t"
      "v_mfma_f32_16x16x32_bf16 v[92:95], a[92:95], v[40:43], v[124:127]\n\t"
      "v_max3_f32 v140, v80, v81, v140\n\t"
      "v_max3_f32 v141, v82, v83, v141\n\t"
      "v_mfma_f32_16x16x32_bf16 v[96:99], a[96:99], v[40:43], v[124:127]\n\t"
      "v_max3_f32 v140, v84, v85, v140\n\t"
      "v_max3_f32 v141, v86, v87, v141\n\t"
      "v_mfma_f32_16x16x32_bf16 v[100:103], a[100:103], v[40:43], v[124:127]\n\t"
      "v_max3_f32 v140, v88, v89, v140\n\t"
      "v_max3_f32 v141, v90, v91, v141\n\t"
      "s_cmp_eq_u32 s60, 0\n\t"
      "s_cbranch_scc1 .Lkm_n7_2_%=\n\t"
      "v_mfma_f32_16x16x32_bf16 v[104:107], a[104:107], v[40:43], v[124:127]\n\t"
      "v_max3_f32 v140, v92, v93, v140\n\t"
      "v_max3_f32 v141, v94, v95, v141\n\t"
      "v_mfma_f32_16x16x32_bf16 v[108:111], a[108:111], v[40:43], v[124:127]\n\t"
      "v_max3_f32 v140, v96, v97, v140\n\t"
      "v_max3_f32 v141, v98, v99, v141\n\t"
      "s_branch .Lkm_n8_2_%=\n\t"
      ".Lkm_n0_2_%=:\n\t"
      "v_mfma_f32_16x16x32_bf16 v[48:51], a[48:51], v[40:43], v[116:119]\n\t"
      "v_mfma_f32_16x16x32_bf16 v[52:55], a[52:55], v[40:43], 0\n\t"
      ".Lkm_n1_2_%=:\n\t"
      "v_mfma_f32_16x16x32_bf16 v[56:59], a[56:59], v[40:43], 0\n\t"
      "v_mfma_f32_16x16x32_bf16 v[60:63], a[60:63], v[40:43], 0\n\t"
      "s_nop 4\n\t"
      "v_max_f32 v140, v48, v49\n\t"
      "v_max_f32 v141, v50, v51\n\t"
      ".Lkm_n2_2_%=:\n\t"
      "v_mfma_f32_16x16x32_bf16 v[64:67], a[64:67], v[40:43], 0\n\t"
      "v_max3_f32 v140, v52, v53, v140\n\t"
      "v_max3_f32 v141, v54, v55, v141\n\t"
      "v_mfma_f32_16x16x32_bf16 v[68:71], a[68:71], v[40:43], 0\n\t"
      "v_max3_f32 v140, v56, v57, v140\n\t"
      "v_max3_f32 v141, v58, v59, v141\n\t"
      ".Lkm_n3_2_%=:\n\t"
      "v_mfma_f32_16x16x32_bf16 v[72:75], a[72:75], v[40:43], 0\n\t"
      "v_max3_f32 v140, v60, v61, v140\n\t"
      "v_max3_f32 v141, v62, v63, v141\n\t"
      "v_mfma_f32_16x16x32_bf16 v[76:79], a[76:79], v[40:43], 0\n\t"
      "v_max3_f32 v140, v64, v65, v140\n\t"
      "v_max3_f32 v141, v66, v67, v141\n\t"
      ".Lkm_n4_2_%=:\n\t"
      "v_mfma_f32_16x16x32_bf16 v[80:83], a[80:83], v[40:43], 0\n\t"
      "v_max3_f32 v140, v68, v69, v140\n\t"
      "v_max3_f32 v141, v70, v71, v141\n\t"
      "v_mfma_f32_16x16x32_bf16 v[84:87], a[84:87], v[40:43], 0\n\t"
      "v_max3_f32 v140, v72, v73, v140\n\t"
      "v_max3_f32 v141, v74, v75, v141\n\t"
      ".Lkm_n5_2_%=:\n\t"
      "v_mfma_f32_16x16x32_bf16 v[88:91], a[88:91], v[40:43], 0\n\t"
      "v_max3_f32 v140, v76, v77, v140\n\t"
      "v_max3_f32 v141, v78, v79, v141\n\t"
      "v_mfma_f32_16x16x32_bf16 v[92:95], a[92:95], v[40:43], 0\n\t"
      "v_max3_f32 v140, v80, v81, v140\n\t"
      "v_max3_f32 v141, v82, v83, v141\n\t"
      ".Lkm_n6_2_%=:\n\t"
      "v_mfma_f32_16x16x32_bf16 v[96:99], a[96:99], v[40:43], 0\n\t"
      "v_max3_f32 v140, v84, v85, v140\n\t"
      "v_max3_f32 v141, v86, v87, v141\n\t"
      "v_mfma_f32_16x16x32_bf16 v[100:103], a[100:103], v[40:43], 0\n\t"
      "v_max3_f32 v140, v88, v89, v140\n\t"
      "v_max3_f32 v141, v90, v91, v141\n\t"
      ".Lkm_n7_2_%=:\n\t"
      "v_mfma_f32_16x16x32_bf16 v[104:107], a[104:107], v[40:43], 0\n\t"
      "v_max3_f32 v140, v92, v93, v140\n\t"
      "v_max3_f32 v141, v94, v95, v141\n\t"
      "v_mfma_f32_16x16x32_bf16 v[108:111], a[108:111], v[40:43], 0\n\t"
      "v_max3_f32 v140, v96, v97, v140\n\t"
      "v_max3_f32 v141, v98, v99, v141\n\t"
      ".Lkm_n8_2_%=:\n\t"
      "v_mfma_f32_16x16x32_bf16 v[112:115], a[112:115], v[40:43], v[120:123]\n\t"
      "v_max3_f32 v140, v100, v101, v140\n\t"
      "v_max3_f32 v141, v102, v103, v141\n\t"
      "v_max3_f32 v140, v104, v105, v140\n\t"
      "v_rcp_f32 v142, v136\n\t"
      "v_max3_f32 v141, v106, v107, v141\n\t"
      "v_pk_mul_f32 v[128:129], v[128:129], v[142:143] op_sel_hi:[1,0]\n\t"
      "v_max3_f32 v140, v108, v109, v140\n\t"
      "v_pk_mul_f32 v[130:131], v[130:131], v[142:143] op_sel_hi:[1,0]\n\t"
      "v_max3_f32 v141, v110, v111, v141\n\t"
      "v_pk_mul_f32 v[132:133], v[132:133], v[142:143] op_sel_hi:[1,0]\n\t"
      "v_max3_f32 v140, v112, v113, v140\n\t"
      "v_pk_mul_f32 v[134:135], v[134:135], v[142:143] op_sel_hi:[1,0]\n\t"
      "v_max3_f32 v141, v114, v115, v141\n\t"
      "v_cvt_pk_bf16_f32 v128, v128, v129\n\t"
      "v_max_f32 v140, v140, v141\n\t"
      "v_mov_b32 v141, v140\n\t"
      "v_cvt_pk_bf16_f32 v129, v130, v131\n\t"
      "v_cvt_pk_bf16_f32 v130, v132, v133\n\t"
      "v_permlane32_swap_b32 v140, v141\n\t"
      "v_cvt_pk_bf16_f32 v131, v134, v135\n\t"
      "v_max_f32 v140, v140, v141\n\t"
      "v_mov_b32 v141, v140\n\t"
      "buffer_store_dwordx4 v[128:131], v1, s[44:47], s82 offen offset:1024 sc1\n\t"
      "s_nop 0\n\t"
      "v_permlane16_swap_b32 v140, v141\n\t"
      "v_max_f32 v140, v140, v141\n\t"
      "v_mul_f32 v140, s80, v140\n\t"
      "s_waitcnt lgkmcnt(0)\n\t"
      "v_pk_fma_f32 v[48:49], v[48:49], s[80:81], v[140:141] op_sel_hi:[1,0,0] neg_lo:[0,0,1] neg_hi:[0,0,1]\n\t"
      "v_pk_fma_f32 v[50:51], v[50:51], s[80:81], v[140:141] op_sel_hi:[1,0,0] neg_lo:[0,0,1] neg_hi:[0,0,1]\n\t"
      "v_exp_f32 v48, v48\n\t"
      "v_pk_fma_f32 v[52:53], v[52:53], s[80:81], v[140:141] op_sel_hi:[1,0,0] neg_lo:[0,0,1] neg_hi:[0,0,1]\n\t"
      "v_exp_f32 v49, v49\n\t"
      "v_exp_f32 v50, v50\n\t"
      "v_pk_fma_f32 v[54:55], v[54:55], s[80:81], v[140:141] op_sel_hi:[1,0,0] neg_lo:[0,0,1] neg_hi:[0,0,1]\n\t"
      "v_exp_f32 v51, v51\n\t"
      "v_exp_f32 v52, v52\n\t"
      "v_exp_f32 v53, v53\n\t"
      "v_exp_f32 v54, v54\n\t"
      "v_exp_f32 v55, v55\n\t"
      "v_cvt_pk_bf16_f32 v48, v48, v49\n\t"
      "v_cvt_pk_bf16_f32 v49, v50, v51\n\t"
      "v_cvt_pk_bf16_f32 v50, v52, v53\n\t"
      "v_cvt_pk_bf16_f32 v51, v54, v55\n\t"
      "s_add_u32 m0, s62, 9216\n\t"
      "s_nop 0\n\t"
      "global_load_lds_dwordx4 v5, s[42:43]\n\t"
      "s_add_u32 s42, s42, 262144\n\t"
      "s_addc_u32 s43, s43, 0\n\t"
      "v_pk_fma_f32 v[56:57], v[56:57], s[80:81], v[140:141] op_sel_hi:[1,0,0] neg_lo:[0,0,1] neg_hi:[0,0,1]\n\t"
      "v_pk_fma_f32 v[58:59], v[58:59], s[80:81], v[140:141] op_sel_hi:[1,0,0] neg_lo:[0,0,1] neg_hi:[0,0,1]\n\t"
      "v_exp_f32 v56, v56\n\t"
      "v_pk_fma_f32 v[60:61], v[60:61], s[80:81], v[140:141] op_sel_hi:[1,0,0] neg_lo:[0,0,1] neg_hi:[0,0,1]\n\t"
      "v_mfma_f32_16x16x32_bf16 v[128:131], a[120:123], v[48:51], 0\n\t"
      "v_exp_f32 v57, v57\n\t"
      "v_exp_f32 v58, v58\n\t"
      "v_pk_fma_f32 v[62:63], v[62:63], s[80:81], v[140:141] op_sel_hi:[1,0,0] neg_lo:[0,0,1] neg_hi:[0,0,1]\n\t"
      "v_exp_f32 v59, v59\n\t"
      "v_mfma_f32_16x16x32_bf16 v[132:135], a[124:127], v[48:51], 0\n\t"
      "v_exp_f32 v60, v60\n\t"
      "v_exp_f32 v61, v61\n\t"
      "v_exp_f32 v62, v62\n\t"
      "v_exp_f32 v63, v63\n\t"
      "v_mfma_f32_16x16x32_bf16 v[136:139], a[192:195], v[48:51], 0\n\t"
      "v_cvt_pk_bf16_f32 v56, v56, v57\n\t"
      "v_cvt_pk_bf16_f32 v57, v58, v59\n\t"
      "v_cvt_pk_bf16_f32 v58, v60, v61\n\t"
      "v_cvt_pk_bf16_f32 v59, v62, v63\n\t"
      "s_add_u32 m0, s62, 16384\n\t"
      "s_nop 0\n\t"
      "global_load_lds_dwordx4 v2, s[40:41]\n\t"
      "s_add_u32 m0, s62, 17408\n\t"
      "s_nop 0\n\t"
      "global_load_lds_dwordx4 v3, s[40:41]\n\t"
      "s_add_u32 s40, s40, 262144\n\t"
      "s_addc_u32 s41, s41, 0\n\t"
      "v_pk_fma_f32 v[64:65], v[64:65], s[80:81], v[140:141] op_sel_hi:[1,0,0] neg_lo:[0,0,1] neg_hi:[0,0,1]\n\t"
      "v_pk_fma_f32 v[66:67], v[66:67], s[80:81], v[140:141] op_sel_hi:[1,0,0] neg_lo:[0,0,1] neg_hi:[0,0,1]\n\t"
      "v_exp_f32 v64, v64\n\t"
      "v_pk_fma_f32 v[68:69], v[68:69], s[80:81], v[140:141] op_sel_hi:[1,0,0] neg_lo:[0,0,1] neg_hi:[0,0,1]\n\t"
      "v_mfma_f32_16x16x32_bf16 v[128:131], a[128:131], v[56:59], v[128:131]\n\t"
      "v_exp_f32 v65, v65\n\t"
      "v_exp_f32 v66, v66\n\t"
      "v_pk_fma_f32 v[70:71], v[70:71], s[80:81], v[140:141] op_sel_hi:[1,0,0] neg_lo:[0,0,1] neg_hi:[0,0,1]\n\t"
      "v_exp_f32 v67, v67\n\t"
      "v_mfma_f32_16x16x32_bf16 v[132:135], a[132:135], v[56:59], v[132:135]\n\t"
      "v_exp_f32 v68, v68\n\t"
      "v_exp_f32 v69, v69\n\t"
      "v_exp_f32 v70, v70\n\t"
      "v_exp_f32 v71, v71\n\t"
      "v_mfma_f32_16x16x32_bf16 v[136:139], a[192:195], v[56:59], v[136:139]\n\t"
      "v_cvt_pk_bf16_f32 v64, v64, v65\n\t"
      "v_cvt_pk_bf16_f32 v65, v66, v67\n\t"
      "v_cvt_pk_bf16_f32 v66, v68, v69\n\t"
      "v_cvt_pk_bf16_f32 v67, v70, v71\n\t"
      "s_add_u32 m0, s62, 24576\n\t"
      "s_nop 0\n\t"
      "global_load_lds_dwordx4 v4, s[42:43]\n\t"
      "v_pk_fma_f32 v[72:73], v[72:73], s[80:81], v[140:141] op_sel_hi:[1,0,0] neg_lo:[0,0,1] neg_hi:[0,0,1]\n\t"
      "v_pk_fma_f32 v[74:75], v[74:75], s[80:81], v[140:141] op_sel_hi:[1,0,0] neg_lo:[0,0,1] neg_hi:[0,0,1]\n\t"
      "v_exp_f32 v72, v72\n\t"
      "v_pk_fma_f32 v[76:77], v[76:77], s[80:81], v[140:141] op_sel_hi:[1,0,0] neg_lo:[0,0,1] neg_hi:[0,0,1]\n\t"
      "v_mfma_f32_16x16x32_bf16 v[128:131], a[136:139], v[64:67], v[128:131]\n\t"
      "v_exp_f32 v73, v73\n\t"
      "v_exp_f32 v74, v74\n\t"
      "v_pk_fma_f32 v[78:79], v[78:79], s[80:81], v[140:141] op_sel_hi:[1,0,0] neg_lo:[0,0,1] neg_hi:[0,0,1]\n\t"
      "v_exp_f32 v75, v75\n\t"
      "v_mfma_f32_16x16x32_bf16 v[132:135], a[140:143], v[64:67], v[132:135]\n\t"
      "v_exp_f32 v76, v76\n\t"
      "v_exp_f32 v77, v77\n\t"
      "v_exp_f32 v78, v78\n\t"
      "v_exp_f32 v79, v79\n\t"
      "v_mfma_f32_16x16x32_bf16 v[136:139], a[192:195], v[64:67], v[136:139]\n\t"
      "v_cvt_pk_bf16_f32 v72, v72, v73\n\t"
      "v_cvt_pk_bf16_f32 v73, v74, v75\n\t"
      "v_cvt_pk_bf16_f32 v74, v76, v77\n\t"
      "v_cvt_pk_bf16_f32 v75, v78, v79\n\t"
      "s_waitcnt vmcnt(10)\n\t"
      "s_add_u32 s70, s70, 4294574080\n\t"
      "s_mov_b64 exec, 0xff\n\t"
      "buffer_store_dwordx4 a[252:255], v1, s[44:47], s70 offen sc1\n\t"
      "s_mov_b64 exec, -1\n\t"
      "s_add_u32 m0, s62, 25600\n\t"
      "s_nop 0\n\t"
      "global_load_lds_dwordx4 v5, s[42:43]\n\t"
      "s_add_u32 s42, s42, 262144\n\t"
      "s_addc_u32 s43, s43, 0\n\t"
      "v_pk_fma_f32 v[80:81], v[80:81], s[80:81], v[140:141] op_sel_hi:[1,0,0] neg_lo:[0,0,1] neg_hi:[0,0,1]\n\t"
      "v_pk_fma_f32 v[82:83], v[82:83], s[80:81], v[140:141] op_sel_hi:[1,0,0] neg_lo:[0,0,1] neg_hi:[0,0,1]\n\t"
      "v_exp_f32 v80, v80\n\t"
      "v_pk_fma_f32 v[84:85], v[84:85], s[80:81], v[140:141] op_sel_hi:[1,0,0] neg_lo:[0,0,1] neg_hi:[0,0,1]\n\t"
      "v_mfma_f32_16x16x32_bf16 v[128:131], a[144:147], v[72:75], v[128:131]\n\t"
      "v_exp_f32 v81, v81\n\t"
      "v_exp_f32 v82, v82\n\t"
      "v_pk_fma_f32 v[86:87], v[86:87], s[80:81], v[140:141] op_sel_hi:[1,0,0] neg_lo:[0,0,1] neg_hi:[0,0,1]\n\t"
      "v_exp_f32 v83, v83\n\t"
      "v_mfma_f32_16x16x32_bf16 v[132:135], a[148:151], v[72:75], v[132:135]\n\t"
      "v_exp_f32 v84, v84\n\t"
      "v_exp_f32 v85, v85\n\t"
      "v_exp_f32 v86, v86\n\t"
      "v_exp_f32 v87, v87\n\t"
      "v_mfma_f32_16x16x32_bf16 v[136:139], a[192:195], v[72:75], v[136:139]\n\t"
      "v_cvt_pk_bf16_f32 v80, v80, v81\n\t"
      "v_cvt_pk_bf16_f32 v81, v82, v83\n\t"
      "v_cvt_pk_bf16_f32 v82, v84, v85\n\t"
      "v_cvt_pk_bf16_f32 v83, v86, v87\n\t"
      "s_add_u32 m0, s62, 32768\n\t"
      "s_nop 0\n\t"
      "global_load_lds_dwordx4 v2, s[40:41]\n\t"
      "s_add_u32 m0, s62, 33792\n\t"
      "s_nop 0\n\t"
      "global_load_lds_dwordx4 v3, s[40:41]\n\t"
      "s_add_u32 s40, s40, 262144\n\t"
      "s_addc_u32 s41, s41, 0\n\t"
      "v_pk_fma_f32 v[88:89], v[88:89], s[80:81], v[140:141] op_sel_hi:[1,0,0] neg_lo:[0,0,1] neg_hi:[0,0,1]\n\t"
      "v_pk_fma_f32 v[90:91], v[90:91], s[80:81], v[140:141] op_sel_hi:[1,0,0] neg_lo:[0,0,1] neg_hi:[0,0,1]\n\t"
      "v_exp_f32 v88, v88\n\t"
      "v_pk_fma_f32 v[92:93], v[92:93], s[80:81], v[140:141] op_sel_hi:[1,0,0] neg_lo:[0,0,1] neg_hi:[0,0,1]\n\t"
      "v_mfma_f32_16x16x32_bf16 v[128:131], a[152:155], v[80:83], v[128:131]\n\t"
      "v_exp_f32 v89, v89\n\t"
      "v_exp_f32 v90, v90\n\t"
      "v_pk_fma_f32 v[94:95], v[94:95], s[80:81], v[140:141] op_sel_hi:[1,0,0] neg_lo:[0,0,1] neg_hi:[0,0,1]\n\t"
      "v_exp_f32 v91, v91\n\t"
      "v_mfma_f32_16x16x32_bf16 v[132:135], a[156:159], v[80:83], v[132:135]\n\t"
      "v_exp_f32 v92, v92\n\t"
      "v_exp_f32 v93, v93\n\t"
      "v_exp_f32 v94, v94\n\t"
      "v_exp_f32 v95, v95\n\t"
      "v_mfma_f32_16x16x32_bf16 v[136:139], a[192:195], v[80:83], v[136:139]\n\t"
      "v_cvt_pk_bf16_f32 v88, v88, v89\n\t"
      "v_cvt_pk_bf16_f32 v89, v90, v91\n\t"
      "v_cvt_pk_bf16_f32 v90, v92, v93\n\t"
      "v_cvt_pk_bf16_f32 v91, v94, v95\n\t"
      "buffer_load_dwordx2 v[14:15], v20, s[44:47], s83 offen sc1\n\t"
      "v_pk_fma_f32 v[96:97], v[96:97], s[80:81], v[140:141] op_sel_hi:[1,0,0] neg_lo:[0,0,1] neg_hi:[0,0,1]\n\t"
      "v_pk_fma_f32 v[98:99], v[98:99], s[80:81], v[140:141] op_sel_hi:[1,0,0] neg_lo:[0,0,1] neg_hi:[0,0,1]\n\t"
      "v_exp_f32 v96, v96\n\t"
      "v_pk_fma_f32 v[100:101], v[100:101], s[80:81], v[140:141] op_sel_hi:[1,0,0] neg_lo:[0,0,1] neg_hi:[0,0,1]\n\t"
      "v_mfma_f32_16x16x32_bf16 v[128:131], a[160:163], v[88:91], v[128:131]\n\t"
      "v_exp_f32 v97, v97\n\t"
      "v_exp_f32 v98, v98\n\t"
      "v_pk_fma_f32 v[102:103], v[102:103], s[80:81], v[140:141] op_sel_hi:[1,0,0] neg_lo:[0,0,1] neg_hi:[0,0,1]\n\t"
      "v_exp_f32 v99, v99\n\t"
      "v_mfma_f32_16x16x32_bf16 v[132:135], a[164:167], v[88:91], v[132:135]\n\t"
      "v_exp_f32 v100, v100\n\t"
      "v_exp_f32 v101, v101\n\t"
      "v_exp_f32 v102, v102\n\t"
      "v_exp_f32 v103, v103\n\t"
      "v_mfma_f32_16x16x32_bf16 v[136:139], a[192:195], v[88:91], v[136:139]\n\t"
      "v_cvt_pk_bf16_f32 v96, v96, v97\n\t"
      "v_cvt_pk_bf16_f32 v97, v98, v99\n\t"
      "v_cvt_pk_bf16_f32 v98, v100, v101\n\t"
      "v_cvt_pk_bf16_f32 v99, v102, v103\n\t"
      "s_add_u32 m0, s62, 40960\n\t"
      "s_nop 0\n\t"
      "global_load_lds_dwordx4 v4, s[42:43]\n\t"
      "v_pk_fma_f32 v[104:105], v[104:105], s[80:81], v[140:141] op_sel_hi:[1,0,0] neg_lo:[0,0,1] neg_hi:[0,0,1]\n\t"
      "v_pk_fma_f32 v[106:107], v[106:107], s[80:81], v[140:141] op_sel_hi:[1,0,0] neg_lo:[0,0,1] neg_hi:[0,0,1]\n\t"
      "v_exp_f32 v104, v104\n\t"
      "v_pk_fma_f32 v[108:109], v[108:109], s[80:81], v[140:141] op_sel_hi:[1,0,0] neg_lo:[0,0,1] neg_hi:[0,0,1]\n\t"
      "v_mfma_f32_16x16x32_bf16 v[128:131], a[168:171], v[96:99], v[128:131]\n\t"
      "v_exp_f32 v105, v105\n\t"
      "v_exp_f32 v106, v106\n\t"
      "v_pk_fma_f32 v[110:111], v[110:111], s[80:81], v[140:141] op_sel_hi:[1,0,0] neg_lo:[0,0,1] neg_hi:[0,0,1]\n\t"
      "v_exp_f32 v107, v107\n\t"
      "v_mfma_f32_16x16x32_bf16 v[132:135], a[172:175], v[96:99], v[132:135]\n\t"
      "v_exp_f32 v108, v108\n\t"
      "v_exp_f32 v109, v109\n\t"
      "v_exp_f32 v110, v110\n\t"
      "v_exp_f32 v111, v111\n\t"
      "v_mfma_f32_16x16x32_bf16 v[136:139], a[192:195], v[96:99], v[136:139]\n\t"
      "v_cvt_pk_bf16_f32 v104, v104, v105\n\t"
      "v_cvt_pk_bf16_f32 v105, v106, v107\n\t"
      "v_cvt_pk_bf16_f32 v106, v108, v109\n\t"
      "v_cvt_pk_bf16_f32 v107, v110, v111\n\t"
      "s_add_u32 m0, s62, 41984\n\t"
      "s_nop 0\n\t"
      "global_load_lds_dwordx4 v5, s[42:43]\n\t"
      "s_add_u32 s42, s42, 262144\n\t"
      "s_addc_u32 s43, s43, 0\n\t"
      "v_pk_fma_f32 v[112:113], v[112:113], s[80:81], v[140:141] op_sel_hi:[1,0,0] neg_lo:[0,0,1] neg_hi:[0,0,1]\n\t"
      "v_pk_fma_f32 v[114:115], v[114:115], s[80:81], v[140:141] op_sel_hi:[1,0,0] neg_lo:[0,0,1] neg_hi:[0,0,1]\n\t"
      "v_mfma_f32_16x16x32_bf16 v[128:131], a[176:179], v[104:107], v[128:131]\n\t"
      "v_exp_f32 v112, v112\n\t"
      "v_exp_f32 v113, v113\n\t"
      "v_mfma_f32_16x16x32_bf16 v[132:135], a[180:183], v[104:107], v[132:135]\n\t"
      "v_exp_f32 v114, v114\n\t"
      "v_exp_f32 v115, v115\n\t"
      "v_mfma_f32_16x16x32_bf16 v[136:139], a[192:195], v[104:107], v[136:139]\n\t"
      "v_cvt_pk_bf16_f32 v112, v112, v113\n\t"
      "v_cvt_pk_bf16_f32 v113, v114, v115\n\t"
      "v_mov_b64 v[114:115], 0\n\t"
      "s_add_u32 m0, s62, 49152\n\t"
      "s_nop 0\n\t"
      "global_load_lds_dwordx4 v2, s[40:41]\n\t"
      "s_add_u32 m0, s62, 50176\n\t"
      "s_nop 0\n\t"
      "global_load_lds_dwordx4 v3, s[40:41]\n\t"
      "s_add_u32 s40, s40, 131072\n\t"
      "s_addc_u32 s41, s41, 0\n\t"
      "v_mfma_f32_16x16x32_bf16 v[128:131], a[184:187], v[112:115], v[128:131]\n\t"
      "v_mfma_f32_16x16x32_bf16 v[132:135], a[188:191], v[112:115], v[132:135]\n\t"
      "v_mfma_f32_16x16x32_bf16 v[136:139], a[192:195], v[112:115], v[136:139]\n\t"
      "s_cmp_eq_u32 %17, 0\n\t"
      "s_cbranch_scc0 .Lkm_n0_3_%=\n\t"
      "v_mfma_f32_16x16x32_bf16 v[48:51], a[52:55], v[44:47], v[124:127]\n\t"
      "s_cmp_eq_u32 s60, 6\n\t"
      "s_cbranch_scc1 .Lkm_n1_3_%=\n\t"
      "v_mfma_f32_16x16x32_bf16 v[52:55], a[56:59], v[44:47], v[124:127]\n\t"
      "v_mfma_f32_16x16x32_bf16 v[56:59], a[60:63], v[44:47], v[124:127]\n\t"
      "v_mfma_f32_16x16x32_bf16 v[60:63], a[64:67], v[44:47], v[124:127]\n\t"
      "s_nop 2\n\t"
      "v_max_f32 v140, v48, v49\n\t"
      "v_max_f32 v141, v50, v51\n\t"
      "v_mfma_f32_16x16x32_bf16 v[64:67], a[68:71], v[44:47], v[124:127]\n\t"
      "v_max3_f32 v140, v52, v53, v140\n\t"
      "v_max3_f32 v141, v54, v55, v141\n\t"
      "s_cmp_eq_u32 s60, 4\n\t"
      "s_cbranch_scc1 .Lkm_n3_3_%=\n\t"
      "v_mfma_f32_16x16x32_bf16 v[68:71], a[72:75], v[44:47], v[124:127]\n\t"
      "v_max3_f32 v140, v56, v57, v140\n\t"
      "v_max3_f32 v141, v58, v59, v141\n\t"
      "v_mfma_f32_16x16x32_bf16 v[72:75], a[76:79], v[44:47], v[124:127]\n\t"
      "v_max3_f32 v140, v60, v61, v140\n\t"
      "v_max3_f32 v141, v62, v63, v141\n\t"
      "v_mfma_f32_16x16x32_bf16 v[76:79], a[80:83], v[44:47], v[124:127]\n\t"
      "v_max3_f32 v140, v64, v65, v140\n\t"
      "v_max3_f32 v141, v66, v67, v141\n\t"
      "v_mfma_f32_16x16x32_bf16 v[80:83], a[84:87], v[44:47], v[124:127]\n\t"
      "v_max3_f32 v140, v68, v69, v140\n\t"
      "v_max3_f32 v141, v70, v71, v141\n\t"
      "s_cmp_eq_u32 s60, 2\n\t"
      "s_cbranch_scc1 .Lkm_n5_3_%=\n\t"
      "v_mfma_f32_16x16x32_bf16 v[84:87], a[88:91], v[44:47], v[124:127]\n\t"
      "v_max3_f32 v140, v72, v73, v140\n\t"
      "v_max3_f32 v141, v74, v75, v141\n\t"
      "v_mfma_f32_16x16x32_bf16 v[88:91], a[92:95], v[44:47], v[124:127]\n\t"
      "v_max3_f32 v140, v76, v77, v140\n\t"
      "v_max3_f32 v141, v78, v79, v141\n\t"
      "v_mfma_f32_16x16x32_bf16 v[92:95], a[96:99], v[44:47], v[124:127]\n\t"
      "v_max3_f32 v140, v80, v81, v140\n\t"
      "v_max3_f32 v141, v82, v83, v141\n\t"
      "v_mfma_f32_16x16x32_bf16 v[96:99], a[100:103], v[44:47], v[124:127]\n\t"
      "v_max3_f32 v140, v84, v85, v140\n\t"
      "v_max3_f32 v141, v86, v87, v141\n\t"
      "s_cmp_eq_u32 s60, 0\n\t"
      "s_cbranch_scc1 .Lkm_n7_3_%=\n\t"
      "v_mfma_f32_16x16x32_bf16 v[100:103], a[104:107], v[44:47], v[124:127]\n\t"
      "v_max3_f32 v140, v88, v89, v140\n\t"
      "v_max3_f32 v141, v90, v91, v141\n\t"
      "v_mfma_f32_16x16x32_bf16 v[104:107], a[108:111], v[44:47], v[124:127]\n\t"
      "v_max3_f32 v140, v92, v93, v140\n\t"
      "v_max3_f32 v141, v94, v95, v141\n\t"
      "s_branch .Lkm_n8_3_%=\n\t"
      ".Lkm_n0_3_%=:\n\t"
      "v_mfma_f32_16x16x32_bf16 v[48:51], a[52:55], v[44:47], v[116:119]\n\t"
      ".Lkm_n1_3_%=:\n\t"
      "v_mfma_f32_16x16x32_bf16 v[52:55], a[56:59], v[44:47], 0\n\t"
      "v_mfma_f32_16x16x32_bf16 v[56:59], a[60:63], v[44:47], 0\n\t"
      ".Lkm_n2_3_%=:\n\t"
      "v_mfma_f32_16x16x32_bf16 v[60:63], a[64:67], v[44:47], 0\n\t"
      "s_nop 4\n\t"
      "v_max_f32 v140, v48, v49\n\t"
      "v_max_f32 v141, v50, v51\n\t"
      "v_mfma_f32_16x16x32_bf16 v[64:67], a[68:71], v[44:47], 0\n\t"
      "v_max3_f32 v140, v52, v53, v140\n\t"
      "v_max3_f32 v141, v54, v55, v141\n\t"
      ".Lkm_n3_3_%=:\n\t"
      "v_mfma_f32_16x16x32_bf16 v[68:71], a[72:75], v[44:47], 0\n\t"
      "v_max3_f32 v140, v56, v57, v140\n\t"
      "v_max3_f32 v141, v58, v59, v141\n\t"
      "v_mfma_f32_16x16x32_bf16 v[72:75], a[76:79], v[44:47], 0\n\t"
      "v_max3_f32 v140, v60, v61, v140\n\t"
      "v_max3_f32 v141, v62, v63, v141\n\t"
      ".Lkm_n4_3_%=:\n\t"
      "v_mfma_f32_16x16x32_bf16 v[76:79], a[80:83], v[44:47], 0\n\t"
      "v_max3_f32 v140, v64, v65, v140\n\t"
      "v_max3_f32 v141, v66, v67, v141\n\t"
      "v_mfma_f32_16x16x32_bf16 v[80:83], a[84:87], v[44:47], 0\n\t"
      "v_max3_f32 v140, v68, v69, v140\n\t"
      "v_max3_f32 v141, v70, v71, v141\n\t"
      ".Lkm_n5_3_%=:\n\t"
      "v_mfma_f32_16x16x32_bf16 v[84:87], a[88:91], v[44:47], 0\n\t"
      "v_max3_f32 v140, v72, v73, v140\n\t"
      "v_max3_f32 v141, v74, v75, v141\n\t"
      "v_mfma_f32_16x16x32_bf16 v[88:91], a[92:95], v[44:47], 0\n\t"
      "v_max3_f32 v140, v76, v77, v140\n\t"
      "v_max3_f32 v141, v78, v79, v141\n\t"
      ".Lkm_n6_3_%=:\n\t"
      "v_mfma_f32_16x16x32_bf16 v[92:95], a[96:99], v[44:47], 0\n\t"
      "v_max3_f32 v140, v80, v81, v140\n\t"
      "v_max3_f32 v141, v82, v83, v141\n\t"
      "v_mfma_f32_16x16x32_bf16 v[96:99], a[100:103], v[44:47], 0\n\t"
      "v_max3_f32 v140, v84, v85, v140\n\t"
      "v_max3_f32 v141, v86, v87, v141\n\t"
      ".Lkm_n7_3_%=:\n\t"
      "v_mfma_f32_16x16x32_bf16 v[100:103], a[104:107], v[44:47], 0\n\t"
      "v_max3_f32 v140, v88, v89, v140\n\t"
      "v_max3_f32 v141, v90, v91, v141\n\t"
      "v_mfma_f32_16x16x32_bf16 v[104:107], a[108:111], v[44:47], 0\n\t"
      "v_max3_f32 v140, v92, v93, v140\n\t"
      "v_max3_f32 v141, v94, v95, v141\n\t"
      ".Lkm_n8_3_%=:\n\t"
      "v_mfma_f32_16x16x32_bf16 v[108:111], a[112:115], v[44:47], 0\n\t"
      "v_max3_f32 v140, v96, v97, v140\n\t"
      "v_max3_f32 v141, v98, v99, v141\n\t"
      "v_mfma_f32_16x16x32_bf16 v[112:115], a[116:119], v[44:47], v[120:123]\n\t"
      "v_max3_f32 v140, v100, v101, v140\n\t"
      "v_max3_f32 v141, v102, v103, v141\n\t"
      "v_max3_f32 v140, v104, v105, v140\n\t"
      "v_rcp_f32 v142, v136\n\t"
      "v_max3_f32 v141, v106, v107, v141\n\t"
      "v_pk_mul_f32 v[128:129], v[128:129], v[142:143] op_sel_hi:[1,0]\n\t"
      "v_max3_f32 v140, v108, v109, v140\n\t"
      "v_pk_mul_f32 v[130:131], v[130:131], v[142:143] op_sel_hi:[1,0]\n\t"
      "v_max3_f32 v141, v110, v111, v141\n\t"
      "v_pk_mul_f32 v[132:133], v[132:133], v[142:143] op_sel_hi:[1,0]\n\t"
      "v_max3_f32 v140, v112, v113, v140\n\t"
      "v_pk_mul_f32 v[134:135], v[134:135], v[142:143] op_sel_hi:[1,0]\n\t"
      "v_max3_f32 v141, v114, v115, v141\n\t"
      "v_cvt_pk_bf16_f32 v128, v128, v129\n\t"
      "v_max_f32 v140, v140, v141\n\t"
      "v_mov_b32 v141, v140\n\t"
      "v_cvt_pk_bf16_f32 v129, v130, v131\n\t"
      "v_cvt_pk_bf16_f32 v130, v132, v133\n\t"
      "v_permlane32_swap_b32 v140, v141\n\t"
      "v_cvt_pk_bf16_f32 v131, v134, v135\n\t"
      "v_max_f32 v140, v140, v141\n\t"
      "v_mov_b32 v141, v140\n\t"
      "buffer_store_dwordx4 v[128:131], v1, s[44:47], s82 offen offset:2048 sc1\n\t"
      "s_nop 0\n\t"
      "v_permlane16_swap_b32 v140, v141\n\t"
      "v_max_f32 v140, v140, v141\n\t"
      "v_mul_f32 v140, s80, v140\n\t"
      "v_pk_fma_f32 v[48:49], v[48:49], s[80:81], v[140:141] op_sel_hi:[1,0,0] neg_lo:[0,0,1] neg_hi:[0,0,1]\n\t"
      "v_pk_fma_f32 v[50:51], v[50:51], s[80:81], v[140:141] op_sel_hi:[1,0,0] neg_lo:[0,0,1] neg_hi:[0,0,1]\n\t"
      "v_exp_f32 v48, v48\n\t"
      "v_exp_f32 v49, v49\n\t"
      "v_exp_f32 v50, v50\n\t"
      "v_exp_f32 v51, v51\n\t"
      "s_nop 0\n\t"
      "v_cvt_pk_bf16_f32 v51, v50, v51\n\t"
      "v_cvt_pk_bf16_f32 v50, v48, v49\n\t"
      "v_mov_b64 v[48:49], 0\n\t"
      "s_add_u32 m0, s62, 57344\n\t"
      "s_nop 0\n\t"
      "global_load_lds_dwordx4 v2, s[40:41]\n\t"
      "v_pk_fma_f32 v[52:53], v[52:53], s[80:81], v[140:141] op_sel_hi:[1,0,0] neg_lo:[0,0,1] neg_hi:[0,0,1]\n\t"
      "v_pk_fma_f32 v[54:55], v[54:55], s[80:81], v[140:141] op_sel_hi:[1,0,0] neg_lo:[0,0,1] neg_hi:[0,0,1]\n\t"
      "v_exp_f32 v52, v52\n\t"
      "v_pk_fma_f32 v[56:57], v[56:57], s[80:81], v[140:141] op_sel_hi:[1,0,0] neg_lo:[0,0,1] neg_hi:[0,0,1]\n\t"
      "v_mfma_f32_16x16x32_bf16 v[128:131], a[120:123], v[48:51], 0\n\t"
      "v_exp_f32 v53, v53\n\t"
      "v_exp_f32 v54, v54\n\t"
      "v_pk_fma_f32 v[58:59], v[58:59], s[80:81], v[140:141] op_sel_hi:[1,0,0] neg_lo:[0,0,1] neg_hi:[0,0,1]\n\t"
      "v_exp_f32 v55, v55\n\t"
      "v_mfma_f32_16x16x32_bf16 v[132:135], a[124:127], v[48:51], 0\n\t"
      "v_exp_f32 v56, v56\n\t"
      "v_exp_f32 v57, v57\n\t"
      "v_exp_f32 v58, v58\n\t"
      "v_exp_f32 v59, v59\n\t"
      "v_mfma_f32_16x16x32_bf16 v[136:139], a[192:195], v[48:51], 0\n\t"
      "v_cvt_pk_bf16_f32 v52, v52, v53\n\t"
      "v_cvt_pk_bf16_f32 v53, v54, v55\n\t"
      "v_cvt_pk_bf16_f32 v54, v56, v57\n\t"
      "v_cvt_pk_bf16_f32 v55, v58, v59\n\t"
      "s_add_u32 m0, s62, 58368\n\t"
      "s_nop 0\n\t"
      "global_load_lds_dwordx4 v3, s[40:41]\n\t"
      "s_add_u32 s40, s40, 262144\n\t"
      "s_addc_u32 s41, s41, 0\n\t"
      "v_pk_fma_f32 v[60:61], v[60:61], s[80:81], v[140:141] op_sel_hi:[1,0,0] neg_lo:[0,0,1] neg_hi:[0,0,1]\n\t"
      "v_pk_fma_f32 v[62:63], v[62:63], s[80:81], v[140:141] op_sel_hi:[1,0,0] neg_lo:[0,0,1] neg_hi:[0,0,1]\n\t"
      "v_exp_f32 v60, v60\n\t"
      "v_pk_fma_f32 v[64:65], v[64:65], s[80:81], v[140:141] op_sel_hi:[1,0,0] neg_lo:[0,0,1] neg_hi:[0,0,1]\n\t"
      "v_mfma_f32_16x16x32_bf16 v[128:131], a[128:131], v[52:55], v[128:131]\n\t"
      "v_exp_f32 v61, v61\n\t"
      "v_exp_f32 v62, v62\n\t"
      "v_pk_fma_f32 v[66:67], v[66:67], s[80:81], v[140:141] op_sel_hi:[1,0,0] neg_lo:[0,0,1] neg_hi:[0,0,1]\n\t"
      "v_exp_f32 v63, v63\n\t"
      "v_mfma_f32_16x16x32_bf16 v[132:135], a[132:135], v[52:55], v[132:135]\n\t"
      "v_exp_f32 v64, v64\n\t"
      "v_exp_f32 v65, v65\n\t"
      "v_exp_f32 v66, v66\n\t"
      "v_exp_f32 v67, v67\n\t"
      "v_mfma_f32_16x16x32_bf16 v[136:139], a[192:195], v[52:55], v[136:139]\n\t"
      "v_cvt_pk_bf16_f32 v60, v60, v61\n\t"
      "v_cvt_pk_bf16_f32 v61, v62, v63\n\t"
      "v_cvt_pk_bf16_f32 v62, v64, v65\n\t"
      "v_cvt_pk_bf16_f32 v63, v66, v67\n\t"
      "s_waitcnt vmcnt(7)\n\t"
      "v_cmp_eq_u64 vcc, s[58:59], v[14:15]\n\t"
      "s_nop 1\n\t"
      "s_cmp_eq_u64 vcc, -1\n\t"
      "s_cbranch_scc1 .Lkm_h_ok_%=\n\t"
      "s_mov_b32 s67, 0\n\t"
      ".Lkm_h_spin_%=:\n\t"
      "s_sleep 1\n\t"
      "buffer_load_dwordx2 v[14:15], v20, s[44:47], s83 offen sc1\n\t"
      "s_add_u32 s67, s67, 1\n\t"
      "s_cmp_ge_u32 s67, 262144\n\t"
      "s_cbranch_scc1 .Lkm_h_ok_%=\n\t"
      "s_waitcnt vmcnt(0)\n\t"
      "v_cmp_eq_u64 vcc, s[58:59], v[14:15]\n\t"
      "s_nop 1\n\t"
      "s_cmp_eq_u64 vcc, -1\n\t"
      "s_cbranch_scc0 .Lkm_h_spin_%=\n\t"
      ".Lkm_h_ok_%=:\n\t"
      "v_pk_fma_f32 v[68:69], v[68:69], s[80:81], v[140:141] op_sel_hi:[1,0,0] neg_lo:[0,0,1] neg_hi:[0,0,1]\n\t"
      "v_pk_fma_f32 v[70:71], v[70:71], s[80:81], v[140:141] op_sel_hi:[1,0,0] neg_lo:[0,0,1] neg_hi:[0,0,1]\n\t"
      "v_exp_f32 v68, v68\n\t"
      "v_pk_fma_f32 v[72:73], v[72:73], s[80:81], v[140:141] op_sel_hi:[1,0,0] neg_lo:[0,0,1] neg_hi:[0,0,1]\n\t"
      "v_mfma_f32_16x16x32_bf16 v[128:131], a[136:139], v[60:63], v[128:131]\n\t"
      "v_exp_f32 v69, v69\n\t"
      "v_exp_f32 v70, v70\n\t"
      "v_pk_fma_f32 v[74:75], v[74:75], s[80:81], v[140:141] op_sel_hi:[1,0,0] neg_lo:[0,0,1] neg_hi:[0,0,1]\n\t"
      "v_exp_f32 v71, v71\n\t"
      "v_mfma_f32_16x16x32_bf16 v[132:135], a[140:143], v[60:63], v[132:135]\n\t"
      "v_exp_f32 v72, v72\n\t"
      "v_exp_f32 v73, v73\n\t"
      "v_exp_f32 v74, v74\n\t"
      "v_exp_f32 v75, v75\n\t"
      "v_mfma_f32_16x16x32_bf16 v[136:139], a[192:195], v[60:63], v[136:139]\n\t"
      "v_cvt_pk_bf16_f32 v68, v68, v69\n\t"
      "v_cvt_pk_bf16_f32 v69, v70, v71\n\t"
      "v_cvt_pk_bf16_f32 v70, v72, v73\n\t"
      "v_cvt_pk_bf16_f32 v71, v74, v75\n\t"
      "global_load_dwordx4 a[0:3], v1, s[36:37] offset:0 sc1\n\t"
      "global_load_dwordx4 a[4:7], v1, s[36:37] offset:1024 sc1\n\t"
      "global_load_dwordx4 a[8:11], v1, s[36:37] offset:2048 sc1\n\t"
      "global_load_dwordx4 a[12:15], v1, s[36:37] offset:3072 sc1\n\t"
      "v_pk_fma_f32 v[76:77], v[76:77], s[80:81], v[140:141] op_sel_hi:[1,0,0] neg_lo:[0,0,1] neg_hi:[0,0,1]\n\t"
      "v_pk_fma_f32 v[78:79], v[78:79], s[80:81], v[140:141] op_sel_hi:[1,0,0] neg_lo:[0,0,1] neg_hi:[0,0,1]\n\t"
      "v_exp_f32 v76, v76\n\t"
      "v_pk_fma_f32 v[80:81], v[80:81], s[80:81], v[140:141] op_sel_hi:[1,0,0] neg_lo:[0,0,1] neg_hi:[0,0,1]\n\t"
      "v_mfma_f32_16x16x32_bf16 v[128:131], a[144:147], v[68:71], v[128:131]\n\t"
      "v_exp_f32 v77, v77\n\t"
      "v_exp_f32 v78, v78\n\t"
      "v_pk_fma_f32 v[82:83], v[82:83], s[80:81], v[140:141] op_sel_hi:[1,0,0] neg_lo:[0,0,1] neg_hi:[0,0,1]\n\t"
      "v_exp_f32 v79, v79\n\t"
      "v_mfma_f32_16x16x32_bf16 v[132:135], a[148:151], v[68:71], v[132:135]\n\t"
      "v_exp_f32 v80, v80\n\t"
      "v_exp_f32 v81, v81\n\t"
      "v_exp_f32 v82, v82\n\t"
      "v_exp_f32 v83, v83\n\t"
      "v_mfma_f32_16x16x32_bf16 v[136:139], a[192:195], v[68:71], v[136:139]\n\t"
      "v_cvt_pk_bf16_f32 v76, v76, v77\n\t"
      "v_cvt_pk_bf16_f32 v77, v78, v79\n\t"
      "v_cvt_pk_bf16_f32 v78, v80, v81\n\t"
      "v_cvt_pk_bf16_f32 v79, v82, v83\n\t"
      "s_add_u32 s36, s36, 4096\n\t"
      "s_addc_u32 s37, s37, 0\n\t"
      "global_load_dwordx4 a[16:19], v1, s[36:37] offset:0 sc1\n\t"
      "global_load_dwordx4 a[20:23], v1, s[36:37] offset:1024 sc1\n\t"
      "v_pk_fma_f32 v[84:85], v[84:85], s[80:81], v[140:141] op_sel_hi:[1,0,0] neg_lo:[0,0,1] neg_hi:[0,0,1]\n\t"
      "v_pk_fma_f32 v[86:87], v[86:87], s[80:81], v[140:141] op_sel_hi:[1,0,0] neg_lo:[0,0,1] neg_hi:[0,0,1]\n\t"
      "v_exp_f32 v84, v84\n\t"
      "v_pk_fma_f32 v[88:89], v[88:89], s[80:81], v[140:141] op_sel_hi:[1,0,0] neg_lo:[0,0,1] neg_hi:[0,0,1]\n\t"
      "v_mfma_f32_16x16x32_bf16 v[128:131], a[152:155], v[76:79], v[128:131]\n\t"
      "v_exp_f32 v85, v85\n\t"
      "v_exp_f32 v86, v86\n\t"
      "v_pk_fma_f32 v[90:91], v[90:91], s[80:81], v[140:141] op_sel_hi:[1,0,0] neg_lo:[0,0,1] neg_hi:[0,0,1]\n\t"
      "v_exp_f32 v87, v87\n\t"
      "v_mfma_f32_16x16x32_bf16 v[132:135], a[156:159], v[76:79], v[132:135]\n\t"
      "v_exp_f32 v88, v88\n\t"
      "v_exp_f32 v89, v89\n\t"
      "v_exp_f32 v90, v90\n\t"
      "v_exp_f32 v91, v91\n\t"
      "v_mfma_f32_16x16x32_bf16 v[136:139], a[192:195], v[76:79], v[136:139]\n\t"
      "v_cvt_pk_bf16_f32 v84, v84, v85\n\t"
      "v_cvt_pk_bf16_f32 v85, v86, v87\n\t"
      "v_cvt_pk_bf16_f32 v86, v88, v89\n\t"
      "v_cvt_pk_bf16_f32 v87, v90, v91\n\t"
      "global_load_dwordx4 a[24:27], v1, s[36:37] offset:2048 sc1\n\t"
      "global_load_dwordx4 a[28:31], v1, s[36:37] offset:3072 sc1\n\t"
      "s_add_u32 s36, s36, 12288\n\t"
      "s_addc_u32 s37, s37, 0\n\t"
      "v_pk_fma_f32 v[92:93], v[92:93], s[80:81], v[140:141] op_sel_hi:[1,0,0] neg_lo:[0,0,1] neg_hi:[0,0,1]\n\t"
      "v_pk_fma_f32 v[94:95], v[94:95], s[80:81], v[140:141] op_sel_hi:[1,0,0] neg_lo:[0,0,1] neg_hi:[0,0,1]\n\t"
      "v_exp_f32 v92, v92\n\t"
      "v_pk_fma_f32 v[96:97], v[96:97], s[80:81], v[140:141] op_sel_hi:[1,0,0] neg_lo:[0,0,1] neg_hi:[0,0,1]\n\t"
      "v_mfma_f32_16x16x32_bf16 v[128:131], a[160:163], v[84:87], v[128:131]\n\t"
      "v_exp_f32 v93, v93\n\t"
      "v_exp_f32 v94, v94\n\t"
      "v_pk_fma_f32 v[98:99], v[98:99], s[80:81], v[140:141] op_sel_hi:[1,0,0] neg_lo:[0,0,1] neg_hi:[0,0,1]\n\t"
      "v_exp_f32 v95, v95\n\t"
      "v_mfma_f32_16x16x32_bf16 v[132:135], a[164:167], v[84:87], v[132:135]\n\t"
      "v_exp_f32 v96, v96\n\t"
      "v_exp_f32 v97, v97\n\t"
      "v_exp_f32 v98, v98\n\t"
      "v_exp_f32 v99, v99\n\t"
      "v_mfma_f32_16x16x32_bf16 v[136:139], a[192:195], v[84:87], v[136:139]\n\t"
      "v_cvt_pk_bf16_f32 v92, v92, v93\n\t"
      "v_cvt_pk_bf16_f32 v93, v94, v95\n\t"
      "v_cvt_pk_bf16_f32 v94, v96, v97\n\t"
      "v_cvt_pk_bf16_f32 v95, v98, v99\n\t"
      "global_load_dwordx4 a[32:35], v1, s[36:37] offset:0 sc1\n\t"
      "global_load_dwordx4 a[36:39], v1, s[36:37] offset:1024 sc1\n\t"
      "global_load_dwordx4 a[40:43], v1, s[36:37] offset:2048 sc1\n\t"
      "global_load_dwordx4 a[44:47], v1, s[36:37] offset:3072 sc1\n\t"
      "v_pk_fma_f32 v[100:101], v[100:101], s[80:81], v[140:141] op_sel_hi:[1,0,0] neg_lo:[0,0,1] neg_hi:[0,0,1]\n\t"
      "v_pk_fma_f32 v[102:103], v[102:103], s[80:81], v[140:141] op_sel_hi:[1,0,0] neg_lo:[0,0,1] neg_hi:[0,0,1]\n\t"
      "v_exp_f32 v100, v100\n\t"
      "v_pk_fma_f32 v[104:105], v[104:105], s[80:81], v[140:141] op_sel_hi:[1,0,0] neg_lo:[0,0,1] neg_hi:[0,0,1]\n\t"
      "v_mfma_f32_16x16x32_bf16 v[128:131], a[168:171], v[92:95], v[128:131]\n\t"
      "v_exp_f32 v101, v101\n\t"
      "v_exp_f32 v102, v102\n\t"
      "v_pk_fma_f32 v[106:107], v[106:107], s[80:81], v[140:141] op_sel_hi:[1,0,0] neg_lo:[0,0,1] neg_hi:[0,0,1]\n\t"
      "v_exp_f32 v103, v103\n\t"
      "v_mfma_f32_16x16x32_bf16 v[132:135], a[172:175], v[92:95], v[132:135]\n\t"
      "v_exp_f32 v104, v104\n\t"
      "v_exp_f32 v105, v105\n\t"
      "v_exp_f32 v106, v106\n\t"
      "v_exp_f32 v107, v107\n\t"
      "v_mfma_f32_16x16x32_bf16 v[136:139], a[192:195], v[92:95], v[136:139]\n\t"
      "v_cvt_pk_bf16_f32 v100, v100, v101\n\t"
      "v_cvt_pk_bf16_f32 v101, v102, v103\n\t"
      "v_cvt_pk_bf16_f32 v102, v104, v105\n\t"
      "v_cvt_pk_bf16_f32 v103, v106, v107\n\t"
      "s_add_u32 s36, s36, 4096\n\t"
      "s_addc_u32 s37, s37, 0\n\t"
      "global_load_dwordx4 a[48:51], v1, s[36:37] offset:0 sc1\n\t"
      "global_load_dwordx4 a[52:55], v1, s[36:37] offset:1024 sc1\n\t"
      "v_pk_fma_f32 v[108:109], v[108:109], s[80:81], v[140:141] op_sel_hi:[1,0,0] neg_lo:[0,0,1] neg_hi:[0,0,1]\n\t"
      "v_pk_fma_f32 v[110:111], v[110:111], s[80:81], v[140:141] op_sel_hi:[1,0,0] neg_lo:[0,0,1] neg_hi:[0,0,1]\n\t"
      "v_exp_f32 v108, v108\n\t"
      "v_pk_fma_f32 v[112:113], v[112:113], s[80:81], v[140:141] op_sel_hi:[1,0,0] neg_lo:[0,0,1] neg_hi:[0,0,1]\n\t"
      "v_mfma_f32_16x16x32_bf16 v[128:131], a[176:179], v[100:103], v[128:131]\n\t"
      "v_exp_f32 v109, v109\n\t"
      "v_exp_f32 v110, v110\n\t"
      "v_pk_fma_f32 v[114:115], v[114:115], s[80:81], v[140:141] op_sel_hi:[1,0,0] neg_lo:[0,0,1] neg_hi:[0,0,1]\n\t"
      "v_exp_f32 v111, v111\n\t"
      "v_mfma_f32_16x16x32_bf16 v[132:135], a[180:183], v[100:103], v[132:135]\n\t"
      "v_exp_f32 v112, v112\n\t"
      "v_exp_f32 v113, v113\n\t"
      "v_exp_f32 v114, v114\n\t"
      "v_exp_f32 v115, v115\n\t"
      "v_mfma_f32_16x16x32_bf16 v[136:139], a[192:195], v[100:103], v[136:139]\n\t"
      "v_cvt_pk_bf16_f32 v108, v108, v109\n\t"
      "v_cvt_pk_bf16_f32 v109, v110, v111\n\t"
      "v_cvt_pk_bf16_f32 v110, v112, v113\n\t"
      "v_cvt_pk_bf16_f32 v111, v114, v115\n\t"
      "global_load_dwordx4 a[56:59], v1, s[36:37] offset:2048 sc1\n\t"
      "global_load_dwordx4 a[60:63], v1, s[36:37] offset:3072 sc1\n\t"
      "s_add_u32 s36, s36, 12288\n\t"
      "s_addc_u32 s37, s37, 0\n\t"
      "v_mfma_f32_16x16x32_bf16 v[128:131], a[184:187], v[108:111], v[128:131]\n\t"
      "v_mfma_f32_16x16x32_bf16 v[132:135], a[188:191], v[108:111], v[132:135]\n\t"
      "v_mfma_f32_16x16x32_bf16 v[136:139], a[192:195], v[108:111], v[136:139]\n\t"
      "s_waitcnt vmcnt(12)\n\t"
      "s_barrier\n\t"
      "ds_read_b64_tr_b16 a[224:225], v6 offset:0\n\t"
      "ds_read_b64_tr_b16 a[226:227], v6 offset:2048\n\t"
      "ds_read_b64_tr_b16 a[228:229], v7 offset:0\n\t"
      "ds_read_b64_tr_b16 a[230:231], v7 offset:2048\n\t"
      "ds_read_b64_tr_b16 a[232:233], v8 offset:0\n\t"
      "ds_read_b64_tr_b16 a[234:235], v8 offset:2048\n\t"
      "v_rcp_f32 v142, v136\n\t"
      "s_nop 0\n\t"
      "v_pk_mul_f32 v[128:129], v[128:129], v[142:143] op_sel_hi:[1,0]\n\t"
      "v_pk_mul_f32 v[130:131], v[130:131], v[142:143] op_sel_hi:[1,0]\n\t"
      "v_pk_mul_f32 v[132:133], v[132:133], v[142:143] op_sel_hi:[1,0]\n\t"
      "v_pk_mul_f32 v[134:135], v[134:135], v[142:143] op_sel_hi:[1,0]\n\t"
      "v_cvt_pk_bf16_f32 v128, v128, v129\n\t"
      "v_cvt_pk_bf16_f32 v129, v130, v131\n\t"
      "v_cvt_pk_bf16_f32 v130, v132, v133\n\t"
      "v_cvt_pk_bf16_f32 v131, v134, v135\n\t"
      "buffer_store_dwordx4 v[128:131], v1, s[44:47], s82 offen offset:3072 sc1\n\t"
      "s_waitcnt lgkmcnt(4)\n\t"
      "v_mfma_f32_16x16x32_bf16 v[24:27], a[224:227], a[0:3], 0\n\t"
      "ds_read_b64_tr_b16 a[236:237], v9 offset:0\n\t"
      "v_mfma_f32_16x16x32_bf16 v[28:31], a[224:227], a[4:7], 0\n\t"
      "ds_read_b64_tr_b16 a[238:239], v9 offset:2048\n\t"
      "v_mfma_f32_16x16x32_bf16 v[32:35], a[224:227], a[8:11], 0\n\t"
      "v_add_u32 v152, 0x10000, v6\n\t"
      "global_load_dwordx4 a[64:67], v1, s[36:37] offset:0 sc1\n\t"
      "v_mfma_f32_16x16x32_bf16 v[36:39], a[224:227], a[12:15], 0\n\t"
      "s_waitcnt lgkmcnt(4)\n\t"
      "v_mfma_f32_16x16x32_bf16 v[40:43], a[228:231], a[0:3], 0\n\t"
      "ds_read_b64_tr_b16 a[224:225], v6 offset:4096\n\t"
      "v_mfma_f32_16x16x32_bf16 v[44:47], a[228:231], a[4:7], 0\n\t"
      "ds_read_b64_tr_b16 a[226:227], v6 offset:6144\n\t"
      "v_mfma_f32_16x16x32_bf16 v[48:51], a[228:231], a[8:11], 0\n\t"
      "v_add_u32 v153, 0x10000, v7\n\t"
      "global_load_dwordx4 a[68:71], v1, s[36:37] offset:1024 sc1\n\t"
      "v_mfma_f32_16x16x32_bf16 v[52:55], a[228:231], a[12:15], 0\n\t"
      "s_waitcnt lgkmcnt(4)\n\t"
      "v_mfma_f32_16x16x32_bf16 v[56:59], a[232:235], a[0:3], 0\n\t"
      "ds_read_b64_tr_b16 a[228:229], v7 offset:4096\n\t"
      "v_mfma_f32_16x16x32_bf16 v[60:63], a[232:235], a[4:7], 0\n\t"
      "ds_read_b64_tr_b16 a[230:231], v7 offset:6144\n\t"
      "v_mfma_f32_16x16x32_bf16 v[64:67], a[232:235], a[8:11], 0\n\t"
      "v_add_u32 v154, 0x10000, v8\n\t"
      "global_load_dwordx4 a[72:75], v1, s[36:37] offset:2048 sc1\n\t"
      "v_mfma_f32_16x16x32_bf16 v[68:71], a[232:235], a[12:15], 0\n\t"
      "s_waitcnt lgkmcnt(4)\n\t"
      "v_mfma_f32_16x16x32_bf16 v[72:75], a[236:239], a[0:3], 0\n\t"
      "ds_read_b64_tr_b16 a[232:233], v8 offset:4096\n\t"
      "v_mfma_f32_16x16x32_bf16 v[76:79], a[236:239], a[4:7], 0\n\t"
      "ds_read_b64_tr_b16 a[234:235], v8 offset:6144\n\t"
      "v_mfma_f32_16x16x32_bf16 v[80:83], a[236:239], a[8:11], 0\n\t"
      "v_add_u32 v155, 0x10000, v9\n\t"
      "global_load_dwordx4 a[76:79], v1, s[36:37] offset:3072 sc1\n\t"
      "v_mfma_f32_16x16x32_bf16 v[84:87], a[236:239], a[12:15], 0\n\t"
      "s_add_u32 s36, s36, 4096\n\t"
      "s_addc_u32 s37, s37, 0\n\t"
      "s_waitcnt vmcnt(13) lgkmcnt(4)\n\t"
      "v_mfma_f32_16x16x32_bf16 v[24:27], a[224:227], a[16:19], v[24:27]\n\t"
      "ds_read_b64_tr_b16 a[236:237], v9 offset:4096\n\t"
      "v_mfma_f32_16x16x32_bf16 v[28:31], a[224:227], a[20:23], v[28:31]\n\t"
      "ds_read_b64_tr_b16 a[238:239], v9 offset:6144\n\t"
      "v_mfma_f32_16x16x32_bf16 v[32:35], a[224:227], a[24:27], v[32:35]\n\t"
      "global_load_dwordx4 a[80:83], v1, s[36:37] offset:0 sc1\n\t"
      "v_mfma_f32_16x16x32_bf16 v[36:39], a[224:227], a[28:31], v[36:39]\n\t"
      "s_waitcnt lgkmcnt(4)\n\t"
      "v_mfma_f32_16x16x32_bf16 v[40:43], a[228:231], a[16:19], v[40:43]\n\t"
      "ds_read_b64_tr_b16 a[224:225], v6 offset:8192\n\t"
      "v_mfma_f32_16x16x32_bf16 v[44:47], a[228:231], a[20:23], v[44:47]\n\t"
      "ds_read_b64_tr_b16 a[226:227], v6 offset:10240\n\t"
      "v_mfma_f32_16x16x32_bf16 v[48:51], a[228:231], a[24:27], v[48:51]\n\t"
      "global_load_dwordx4 a[84:87], v1, s[36:37] offset:1024 sc1\n\t"
      "v_mfma_f32_16x16x32_bf16 v[52:55], a[228:231], a[28:31], v[52:55]\n\t"
      "s_waitcnt lgkmcnt(4)\n\t"
      "v_mfma_f32_16x16x32_bf16 v[56:59], a[232:235], a[16:19], v[56:59]\n\t"
      "ds_read_b64_tr_b16 a[228:229], v7 offset:8192\n\t"
      "v_mfma_f32_16x16x32_bf16 v[60:63], a[232:235], a[20:23], v[60:63]\n\t"
      "ds_read_b64_tr_b16 a[230:231], v7 offset:10240\n\t"
      "v_mfma_f32_16x16x32_bf16 v[64:67], a[232:235], a[24:27], v[64:67]\n\t"
      "global_load_dwordx4 a[88:91], v1, s[36:37] offset:2048 sc1\n\t"
      "v_mfma_f32_16x16x32_bf16 v[68:71], a[232:235], a[28:31], v[68:71]\n\t"
      "s_waitcnt lgkmcnt(4)\n\t"
      "v_mfma_f32_16x16x32_bf16 v[72:75], a[236:239], a[16:19], v[72:75]\n\t"
      "ds_read_b64_tr_b16 a[232:233], v8 offset:8192\n\t"
      "v_mfma_f32_16x16x32_bf16 v[76:79], a[236:239], a[20:23], v[76:79]\n\t"
      "ds_read_b64_tr_b16 a[234:235], v8 offset:10240\n\t"
      "v_mfma_f32_16x16x32_bf16 v[80:83], a[236:239], a[24:27], v[80:83]\n\t"
      "global_load_dwordx4 a[92:95], v1, s[36:37] offset:3072 sc1\n\t"
      "v_mfma_f32_16x16x32_bf16 v[84:87], a[236:239], a[28:31], v[84:87]\n\t"
      "s_add_u32 s36, s36, 12288\n\t"
      "s_addc_u32 s37, s37, 0\n\t"
      "s_waitcnt lgkmcnt(4)\n\t"
      "v_mfma_f32_16x16x32_bf16 v[88:91], a[224:227], a[0:3], 0\n\t"
      "ds_read_b64_tr_b16 a[236:237], v9 offset:8192\n\t"
      "v_mfma_f32_16x16x32_bf16 v[92:95], a[224:227], a[4:7], 0\n\t"
      "ds_read_b64_tr_b16 a[238:239], v9 offset:10240\n\t"
      "v_mfma_f32_16x16x32_bf16 v[96:99], a[224:227], a[8:11], 0\n\t"
      "v_mfma_f32_16x16x32_bf16 v[100:103], a[224:227], a[12:15], 0\n\t"
      "s_waitcnt lgkmcnt(4)\n\t"
      "v_mfma_f32_16x16x32_bf16 v[104:107], a[228:231], a[0:3], 0\n\t"
      "ds_read_b64_tr_b16 a[224:225], v6 offset:12288\n\t"
      "v_mfma_f32_16x16x32_bf16 v[108:111], a[228:231], a[4:7], 0\n\t"
      "ds_read_b64_tr_b16 a[226:227], v6 offset:14336\n\t"
      "v_mfma_f32_16x16x32_bf16 v[112:115], a[228:231], a[8:11], 0\n\t"
      "v_mfma_f32_16x16x32_bf16 v[116:119], a[228:231], a[12:15], 0\n\t"
      "s_waitcnt lgkmcnt(4)\n\t"
      "v_mfma_f32_16x16x32_bf16 v[120:123], a[232:235], a[0:3], 0\n\t"
      "ds_read_b64_tr_b16 a[228:229], v7 offset:12288\n\t"
      "v_mfma_f32_16x16x32_bf16 v[124:127], a[232:235], a[4:7], 0\n\t"
      "ds_read_b64_tr_b16 a[230:231], v7 offset:14336\n\t"
      "v_mfma_f32_16x16x32_bf16 v[128:131], a[232:235], a[8:11], 0\n\t"
      "v_mfma_f32_16x16x32_bf16 v[132:135], a[232:235], a[12:15], 0\n\t"
      "s_waitcnt lgkmcnt(4)\n\t"
      "v_mfma_f32_16x16x32_bf16 v[136:139], a[236:239], a[0:3], 0\n\t"
      "ds_read_b64_tr_b16 a[232:233], v8 offset:12288\n\t"
      "v_mfma_f32_16x16x32_bf16 v[140:143], a[236:239], a[4:7], 0\n\t"
      "ds_read_b64_tr_b16 a[234:235], v8 offset:14336\n\t"
      "v_mfma_f32_16x16x32_bf16 v[144:147], a[236:239], a[8:11], 0\n\t"
      "v_mfma_f32_16x16x32_bf16 v[148:151], a[236:239], a[12:15], 0\n\t"
      "s_waitcnt vmcnt(8) lgkmcnt(4)\n\t"
      "s_mov_b64 exec, 0xff\n\t"
      "buffer_store_dwordx4 a[252:255], v1, s[44:47], s75 offen sc1\n\t"
      "s_mov_b64 exec, -1\n\t"
      "v_mfma_f32_16x16x32_bf16 v[88:91], a[224:227], a[16:19], v[88:91]\n\t"
      "ds_read_b64_tr_b16 a[236:237], v9 offset:12288\n\t"
      "v_mfma_f32_16x16x32_bf16 v[92:95], a[224:227], a[20:23], v[92:95]\n\t"
      "ds_read_b64_tr_b16 a[238:239], v9 offset:14336\n\t"
      "v_mfma_f32_16x16x32_bf16 v[96:99], a[224:227], a[24:27], v[96:99]\n\t"
      "global_load_dwordx4 a[96:99], v1, s[36:37] offset:0 sc1\n\t"
      "v_mfma_f32_16x16x32_bf16 v[100:103], a[224:227], a[28:31], v[100:103]\n\t"
      "s_waitcnt lgkmcnt(4)\n\t"
      "v_mfma_f32_16x16x32_bf16 v[104:107], a[228:231], a[16:19], v[104:107]\n\t"
      "ds_read_b64_tr_b16 a[224:225], v6 offset:16384\n\t"
      "v_mfma_f32_16x16x32_bf16 v[108:111], a[228:231], a[20:23], v[108:111]\n\t"
      "ds_read_b64_tr_b16 a[226:227], v6 offset:18432\n\t"
      "v_mfma_f32_16x16x32_bf16 v[112:115], a[228:231], a[24:27], v[112:115]\n\t"
      "global_load_dwordx4 a[100:103], v1, s[36:37] offset:1024 sc1\n\t"
      "v_mfma_f32_16x16x32_bf16 v[116:119], a[228:231], a[28:31], v[116:119]\n\t"
      "s_waitcnt lgkmcnt(4)\n\t"
      "v_mfma_f32_16x16x32_bf16 v[120:123], a[232:235], a[16:19], v[120:123]\n\t"
      "ds_read_b64_tr_b16 a[228:229], v7 offset:16384\n\t"
      "v_mfma_f32_16x16x32_bf16 v[124:127], a[232:235], a[20:23], v[124:127]\n\t"
      "ds_read_b64_tr_b16 a[230:231], v7 offset:18432\n\t"
      "v_mfma_f32_16x16x32_bf16 v[128:131], a[232:235], a[24:27], v[128:131]\n\t"
      "global_load_dwordx4 a[104:107], v1, s[36:37] offset:2048 sc1\n\t"
      "v_mfma_f32_16x16x32_bf16 v[132:135], a[232:235], a[28:31], v[132:135]\n\t"
      "s_waitcnt lgkmcnt(4)\n\t"
      "v_mfma_f32_16x16x32_bf16 v[136:139], a[236:239], a[16:19], v[136:139]\n\t"
      "ds_read_b64_tr_b16 a[232:233], v8 offset:16384\n\t"
      "v_mfma_f32_16x16x32_bf16 v[140:143], a[236:239], a[20:23], v[140:143]\n\t"
      "ds_read_b64_tr_b16 a[234:235], v8 offset:18432\n\t"
      "v_mfma_f32_16x16x32_bf16 v[144:147], a[236:239], a[24:27], v[144:147]\n\t"
      "global_load_dwordx4 a[108:111], v1, s[36:37] offset:3072 sc1\n\t"
      "v_mfma_f32_16x16x32_bf16 v[148:151], a[236:239], a[28:31], v[148:151]\n\t"
      "s_add_u32 s36, s36, 4096\n\t"
      "s_addc_u32 s37, s37, 0\n\t"
      "s_waitcnt vmcnt(18) lgkmcnt(4)\n\t"
      "v_mfma_f32_16x16x32_bf16 v[24:27], a[224:227], a[32:35], v[24:27]\n\t"
      "ds_read_b64_tr_b16 a[236:237], v9 offset:16384\n\t"
      "s_lshl_b32 s64, %20, 8\n\t"
      "v_mfma_f32_16x16x32_bf16 v[28:31], a[224:227], a[36:39], v[28:31]\n\t"
      "ds_read_b64_tr_b16 a[238:239], v9 offset:18432\n\t"
      "v_mfma_f32_16x16x32_bf16 v[32:35], a[224:227], a[40:43], v[32:35]\n\t"
      "global_load_dwordx4 a[112:115], v1, s[36:37] offset:0 sc1\n\t"
      "v_mfma_f32_16x16x32_bf16 v[36:39], a[224:227], a[44:47], v[36:39]\n\t"
      "s_lshl_b32 s65, %19, 19\n\t"
      "s_waitcnt lgkmcnt(4)\n\t"
      "v_mfma_f32_16x16x32_bf16 v[40:43], a[228:231], a[32:35], v[40:43]\n\t"
      "ds_read_b64_tr_b16 a[224:225], v6 offset:20480\n\t"
      "v_mfma_f32_16x16x32_bf16 v[44:47], a[228:231], a[36:39], v[44:47]\n\t"
      "ds_read_b64_tr_b16 a[226:227], v6 offset:22528\n\t"
      "v_mfma_f32_16x16x32_bf16 v[48:51], a[228:231], a[40:43], v[48:51]\n\t"
      "global_load_dwordx4 a[116:119], v1, s[36:37] offset:1024 sc1\n\t"
      "v_mfma_f32_16x16x32_bf16 v[52:55], a[228:231], a[44:47], v[52:55]\n\t"
      "s_add_u32 s63, s64, s65\n\t"
      "s_waitcnt lgkmcnt(4)\n\t"
      "v_mfma_f32_16x16x32_bf16 v[56:59], a[232:235], a[32:35], v[56:59]\n\t"
      "ds_read_b64_tr_b16 a[228:229], v7 offset:20480\n\t"
      "v_mfma_f32_16x16x32_bf16 v[60:63], a[232:235], a[36:39], v[60:63]\n\t"
      "ds_read_b64_tr_b16 a[230:231], v7 offset:22528\n\t"
      "v_mfma_f32_16x16x32_bf16 v[64:67], a[232:235], a[40:43], v[64:67]\n\t"
      "global_load_dwordx4 a[120:123], v1, s[36:37] offset:2048 sc1\n\t"
      "v_mfma_f32_16x16x32_bf16 v[68:71], a[232:235], a[44:47], v[68:71]\n\t"
      "s_lshl_b32 s64, %17, 2\n\t"
      "s_waitcnt lgkmcnt(4)\n\t"
      "v_mfma_f32_16x16x32_bf16 v[72:75], a[236:239], a[32:35], v[72:75]\n\t"
      "ds_read_b64_tr_b16 a[232:233], v8 offset:20480\n\t"
      "v_mfma_f32_16x16x32_bf16 v[76:79], a[236:239], a[36:39], v[76:79]\n\t"
      "ds_read_b64_tr_b16 a[234:235], v8 offset:22528\n\t"
      "v_mfma_f32_16x16x32_bf16 v[80:83], a[236:239], a[40:43], v[80:83]\n\t"
      "global_load_dwordx4 a[124:127], v1, s[36:37] offset:3072 sc1\n\t"
      "v_mfma_f32_16x16x32_bf16 v[84:87], a[236:239], a[44:47], v[84:87]\n\t"
      "s_add_u32 s36, s36, 4096\n\t"
      "s_addc_u32 s37, s37, 0\n\t"
      "s_waitcnt vmcnt(18) lgkmcnt(4)\n\t"
      "v_mfma_f32_16x16x32_bf16 v[24:27], a[224:227], a[48:51], v[24:27]\n\t"
      "ds_read_b64_tr_b16 a[236:237], v9 offset:20480\n\t"
      "s_add_u32 s64, s64, %14\n\t"
      "v_mfma_f32_16x16x32_bf16 v[28:31], a[224:227], a[52:55], v[28:31]\n\t"
      "ds_read_b64_tr_b16 a[238:239], v9 offset:22528\n\t"
      "v_mfma_f32_16x16x32_bf16 v[32:35], a[224:227], a[56:59], v[32:35]\n\t"
      "global_load_dwordx4 a[128:131], v1, s[36:37] offset:0 sc1\n\t"
      "v_mfma_f32_16x16x32_bf16 v[36:39], a[224:227], a[60:63], v[36:39]\n\t"
      "s_lshl_b32 s64, s64, 17\n\t"
      "s_waitcnt lgkmcnt(4)\n\t"
      "v_mfma_f32_16x16x32_bf16 v[40:43], a[228:231], a[48:51], v[40:43]\n\t"
      "ds_read_b64_tr_b16 a[224:225], v6 offset:24576\n\t"
      "v_mfma_f32_16x16x32_bf16 v[44:47], a[228:231], a[52:55], v[44:47]\n\t"
      "ds_read_b64_tr_b16 a[226:227], v6 offset:26624\n\t"
      "v_mfma_f32_16x16x32_bf16 v[48:51], a[228:231], a[56:59], v[48:51]\n\t"
      "global_load_dwordx4 a[132:135], v1, s[36:37] offset:1024 sc1\n\t"
      "v_mfma_f32_16x16x32_bf16 v[52:55], a[228:231], a[60:63], v[52:55]\n\t"
      "s_lshl_b32 s65, %20, 2\n\t"
      "s_waitcnt lgkmcnt(4)\n\t"
      "v_mfma_f32_16x16x32_bf16 v[56:59], a[232:235], a[48:51], v[56:59]\n\t"
      "ds_read_b64_tr_b16 a[228:229], v7 offset:24576\n\t"
      "v_mfma_f32_16x16x32_bf16 v[60:63], a[232:235], a[52:55], v[60:63]\n\t"
      "ds_read_b64_tr_b16 a[230:231], v7 offset:26624\n\t"
      "s_add_u32 s65, s65, %19\n\t"
      "v_mfma_f32_16x16x32_bf16 v[64:67], a[232:235], a[56:59], v[64:67]\n\t"
      "global_load_dwordx4 a[136:139], v1, s[36:37] offset:2048 sc1\n\t"
      "v_mfma_f32_16x16x32_bf16 v[68:71], a[232:235], a[60:63], v[68:71]\n\t"
      "s_waitcnt lgkmcnt(4)\n\t"
      "v_mfma_f32_16x16x32_bf16 v[72:75], a[236:239], a[48:51], v[72:75]\n\t"
      "ds_read_b64_tr_b16 a[232:233], v8 offset:24576\n\t"
      "v_mfma_f32_16x16x32_bf16 v[76:79], a[236:239], a[52:55], v[76:79]\n\t"
      "ds_read_b64_tr_b16 a[234:235], v8 offset:26624\n\t"
      "s_lshl_b32 s65, s65, 6\n\t"
      "v_mfma_f32_16x16x32_bf16 v[80:83], a[236:239], a[56:59], v[80:83]\n\t"
      "global_load_dwordx4 a[140:143], v1, s[36:37] offset:3072 sc1\n\t"
      "v_mfma_f32_16x16x32_bf16 v[84:87], a[236:239], a[60:63], v[84:87]\n\t"
      "s_add_u32 s36, s36, 4096\n\t"
      "s_addc_u32 s37, s37, 0\n\t"
      "s_waitcnt lgkmcnt(4)\n\t"
      "v_mfma_f32_16x16x32_bf16 v[88:91], a[224:227], a[32:35], v[88:91]\n\t"
      "ds_read_b64_tr_b16 a[236:237], v9 offset:24576\n\t"
      "v_mfma_f32_16x16x32_bf16 v[92:95], a[224:227], a[36:39], v[92:95]\n\t"
      "ds_read_b64_tr_b16 a[238:239], v9 offset:26624\n\t"
      "s_add_u32 s52, s64, s65\n\t"
      "v_mfma_f32_16x16x32_bf16 v[96:99], a[224:227], a[40:43], v[96:99]\n\t"
      "global_load_dwordx4 a[144:147], v1, s[36:37] offset:0 sc1\n\t"
      "v_mfma_f32_16x16x32_bf16 v[100:103], a[224:227], a[44:47], v[100:103]\n\t"
      "s_waitcnt lgkmcnt(4)\n\t"
      "v_mfma_f32_16x16x32_bf16 v[104:107], a[228:231], a[32:35], v[104:107]\n\t"
      "ds_read_b64_tr_b16 a[224:225], v6 offset:28672\n\t"
      "v_mfma_f32_16x16x32_bf16 v[108:111], a[228:231], a[36:39], v[108:111]\n\t"
      "ds_read_b64_tr_b16 a[226:227], v6 offset:30720\n\t"
      "s_add_u32 s53, s52, 32768\n\t"
      "v_mfma_f32_16x16x32_bf16 v[112:115], a[228:231], a[40:43], v[112:115]\n\t"
      "global_load_dwordx4 a[148:151], v1, s[36:37] offset:1024 sc1\n\t"
      "v_mfma_f32_16x16x32_bf16 v[116:119], a[228:231], a[44:47], v[116:119]\n\t"
      "s_waitcnt lgkmcnt(4)\n\t"
      "v_mfma_f32_16x16x32_bf16 v[120:123], a[232:235], a[32:35], v[120:123]\n\t"
      "ds_read_b64_tr_b16 a[228:229], v7 offset:28672\n\t"
      "s_add_u32 s54, s52, 65536\n\t"
      "v_mfma_f32_16x16x32_bf16 v[124:127], a[232:235], a[36:39], v[124:127]\n\t"
      "ds_read_b64_tr_b16 a[230:231], v7 offset:30720\n\t"
      "v_mfma_f32_16x16x32_bf16 v[128:131], a[232:235], a[40:43], v[128:131]\n\t"
      "global_load_dwordx4 a[152:155], v1, s[36:37] offset:2048 sc1\n\t"
      "v_mfma_f32_16x16x32_bf16 v[132:135], a[232:235], a[44:47], v[132:135]\n\t"
      "s_waitcnt lgkmcnt(4)\n\t"
      "v_mfma_f32_16x16x32_bf16 v[136:139], a[236:239], a[32:35], v[136:139]\n\t"
      "ds_read_b64_tr_b16 a[232:233], v8 offset:28672\n\t"
      "s_add_u32 s55, s52, 98304\n\t"
      "v_mfma_f32_16x16x32_bf16 v[140:143], a[236:239], a[36:39], v[140:143]\n\t"
      "ds_read_b64_tr_b16 a[234:235], v8 offset:30720\n\t"
      "v_mfma_f32_16x16x32_bf16 v[144:147], a[236:239], a[40:43], v[144:147]\n\t"
      "global_load_dwordx4 a[156:159], v1, s[36:37] offset:3072 sc1\n\t"
      "v_mfma_f32_16x16x32_bf16 v[148:151], a[236:239], a[44:47], v[148:151]\n\t"
      "s_add_u32 s36, s36, 12288\n\t"
      "s_addc_u32 s37, s37, 0\n\t"
      "s_waitcnt lgkmcnt(4)\n\t"
      "v_mfma_f32_16x16x32_bf16 v[88:91], a[224:227], a[48:51], v[88:91]\n\t"
      "ds_read_b64_tr_b16 a[236:237], v9 offset:28672\n\t"
      "s_mov_b32 s48, %10\n\t"
      "v_mfma_f32_16x16x32_bf16 v[92:95], a[224:227], a[52:55], v[92:95]\n\t"
      "ds_read_b64_tr_b16 a[238:239], v9 offset:30720\n\t"
      "v_mfma_f32_16x16x32_bf16 v[96:99], a[224:227], a[56:59], v[96:99]\n\t"
      "global_load_dwordx4 a[160:163], v1, s[36:37] offset:0 sc1\n\t"
      "v_mfma_f32_16x16x32_bf16 v[100:103], a[224:227], a[60:63], v[100:103]\n\t"
      "s_mov_b32 s49, %11\n\t"
      "s_waitcnt vmcnt(43) lgkmcnt(4)\n\t"
      "s_barrier\n\t"
      "v_mfma_f32_16x16x32_bf16 v[104:107], a[228:231], a[48:51], v[104:107]\n\t"
      "ds_read_b64_tr_b16 a[224:225], v6 offset:32768\n\t"
      "v_mfma_f32_16x16x32_bf16 v[108:111], a[228:231], a[52:55], v[108:111]\n\t"
      "ds_read_b64_tr_b16 a[226:227], v6 offset:34816\n\t"
      "s_add_u32 m0, s62, 65536\n\t"
      "v_mfma_f32_16x16x32_bf16 v[112:115], a[228:231], a[56:59], v[112:115]\n\t"
      "global_load_lds_dwordx4 v2, s[40:41]\n\t"
      "v_mfma_f32_16x16x32_bf16 v[116:119], a[228:231], a[60:63], v[116:119]\n\t"
      "s_mov_b32 s50, 4194304\n\t"
      "s_waitcnt lgkmcnt(4)\n\t"
      "v_mfma_f32_16x16x32_bf16 v[120:123], a[232:235], a[48:51], v[120:123]\n\t"
      "ds_read_b64_tr_b16 a[228:229], v7 offset:32768\n\t"
      "v_mfma_f32_16x16x32_bf16 v[124:127], a[232:235], a[52:55], v[124:127]\n\t"
      "ds_read_b64_tr_b16 a[230:231], v7 offset:34816\n\t"
      "s_add_u32 m0, s62, 66560\n\t"
      "v_mfma_f32_16x16x32_bf16 v[128:131], a[232:235], a[56:59], v[128:131]\n\t"
      "global_load_lds_dwordx4 v3, s[40:41]\n\t"
      "v_mfma_f32_16x16x32_bf16 v[132:135], a[232:235], a[60:63], v[132:135]\n\t"
      "s_add_u32 s40, s40, 262144\n\t"
      "s_addc_u32 s41, s41, 0\n\t"
      "s_waitcnt lgkmcnt(4)\n\t"
      "v_mfma_f32_16x16x32_bf16 v[136:139], a[236:239], a[48:51], v[136:139]\n\t"
      "ds_read_b64_tr_b16 a[232:233], v8 offset:32768\n\t"
      "s_mov_b32 s51, 0x20000\n\t"
      "v_mfma_f32_16x16x32_bf16 v[140:143], a[236:239], a[52:55], v[140:143]\n\t"
      "ds_read_b64_tr_b16 a[234:235], v8 offset:34816\n\t"
      "s_add_u32 m0, s62, 73728\n\t"
      "v_mfma_f32_16x16x32_bf16 v[144:147], a[236:239], a[56:59], v[144:147]\n\t"
      "global_load_lds_dwordx4 v4, s[42:43]\n\t"
      "v_mfma_f32_16x16x32_bf16 v[148:151], a[236:239], a[60:63], v[148:151]\n\t"
      "s_lshl_b32 s61, %14, 12\n\t"
      "s_waitcnt vmcnt(25) lgkmcnt(4)\n\t"
      "v_mfma_f32_16x16x32_bf16 v[24:27], a[224:227], a[64:67], v[24:27]\n\t"
      "ds_read_b64_tr_b16 a[236:237], v9 offset:32768\n\t"
      "v_mfma_f32_16x16x32_bf16 v[28:31], a[224:227], a[68:71], v[28:31]\n\t"
      "ds_read_b64_tr_b16 a[238:239], v9 offset:34816\n\t"
      "s_add_u32 m0, s62, 74752\n\t"
      "v_mfma_f32_16x16x32_bf16 v[32:35], a[224:227], a[72:75], v[32:35]\n\t"
      "global_load_lds_dwordx4 v5, s[42:43]\n\t"
      "v_mfma_f32_16x16x32_bf16 v[36:39], a[224:227], a[76:79], v[36:39]\n\t"
      "s_add_u32 s42, s42, 131072\n\t"
      "s_addc_u32 s43, s43, 0\n\t"
      "s_waitcnt lgkmcnt(4)\n\t"
      "v_mfma_f32_16x16x32_bf16 v[40:43], a[228:231], a[64:67], v[40:43]\n\t"
      "ds_read_b64_tr_b16 a[224:225], v6 offset:36864\n\t"
      "s_add_u32 s61, s61, %16\n\t"
      "v_mfma_f32_16x16x32_bf16 v[44:47], a[228:231], a[68:71], v[44:47]\n\t"
      "ds_read_b64_tr_b16 a[226:227], v6 offset:38912\n\t"
      "s_add_u32 m0, s62, 81920\n\t"
      "v_mfma_f32_16x16x32_bf16 v[48:51], a[228:231], a[72:75], v[48:51]\n\t"
      "global_load_lds_dwordx4 v2, s[40:41]\n\t"
      "v_mfma_f32_16x16x32_bf16 v[52:55], a[228:231], a[76:79], v[52:55]\n\t"
      "v_lshrrev_b32 v246, 2, v0\n\t"
      "s_waitcnt lgkmcnt(4)\n\t"
      "v_mfma_f32_16x16x32_bf16 v[56:59], a[232:235], a[64:67], v[56:59]\n\t"
      "ds_read_b64_tr_b16 a[228:229], v7 offset:36864\n\t"
      "v_mfma_f32_16x16x32_bf16 v[60:63], a[232:235], a[68:71], v[60:63]\n\t"
      "ds_read_b64_tr_b16 a[230:231], v7 offset:38912\n\t"
      "s_add_u32 m0, s62, 82944\n\t"
      "v_mfma_f32_16x16x32_bf16 v[64:67], a[232:235], a[72:75], v[64:67]\n\t"
      "global_load_lds_dwordx4 v3, s[40:41]\n\t"
      "v_mfma_f32_16x16x32_bf16 v[68:71], a[232:235], a[76:79], v[68:71]\n\t"
      "s_add_u32 s40, s40, 262144\n\t"
      "s_addc_u32 s41, s41, 0\n\t"
      "s_waitcnt lgkmcnt(4)\n\t"
      "v_mfma_f32_16x16x32_bf16 v[72:75], a[236:239], a[64:67], v[72:75]\n\t"
      "ds_read_b64_tr_b16 a[232:233], v8 offset:36864\n\t"
      "v_and_b32 v247, 3, v0\n\t"
      "v_mfma_f32_16x16x32_bf16 v[76:79], a[236:239], a[68:71], v[76:79]\n\t"
      "ds_read_b64_tr_b16 a[234:235], v8 offset:38912\n\t"
      "v_lshlrev_b32 v246, 11, v246\n\t"
      "v_mfma_f32_16x16x32_bf16 v[80:83], a[236:239], a[72:75], v[80:83]\n\t"
      "global_load_dwordx4 a[164:167], v1, s[36:37] offset:1024 sc1\n\t"
      "v_mfma_f32_16x16x32_bf16 v[84:87], a[236:239], a[76:79], v[84:87]\n\t"
      "s_waitcnt vmcnt(25) lgkmcnt(4)\n\t"
      "v_mfma_f32_16x16x32_bf16 v[24:27], a[224:227], a[80:83], v[24:27]\n\t"
      "ds_read_b64_tr_b16 a[236:237], v9 offset:36864\n\t"
      "v_mfma_f32_16x16x32_bf16 v[28:31], a[224:227], a[84:87], v[28:31]\n\t"
      "ds_read_b64_tr_b16 a[238:239], v9 offset:38912\n\t"
      "s_add_u32 m0, s62, 90112\n\t"
      "v_mfma_f32_16x16x32_bf16 v[32:35], a[224:227], a[88:91], v[32:35]\n\t"
      "global_load_lds_dwordx4 v4, s[42:43]\n\t"
      "v_mfma_f32_16x16x32_bf16 v[36:39], a[224:227], a[92:95], v[36:39]\n\t"
      "v_lshl_add_u32 v246, v247, 4, v246\n\t"
      "s_waitcnt lgkmcnt(4)\n\t"
      "v_mfma_f32_16x16x32_bf16 v[40:43], a[228:231], a[80:83], v[40:43]\n\t"
      "ds_read_b64_tr_b16 a[224:225], v6 offset:40960\n\t"
      "v_mfma_f32_16x16x32_bf16 v[44:47], a[228:231], a[84:87], v[44:47]\n\t"
      "ds_read_b64_tr_b16 a[226:227], v6 offset:43008\n\t"
      "v_and_b32 v247, 15, v0\n\t"
      "v_mfma_f32_16x16x32_bf16 v[48:51], a[228:231], a[88:91], v[48:51]\n\t"
      "global_load_dwordx4 a[168:171], v1, s[36:37] offset:2048 sc1\n\t"
      "v_mfma_f32_16x16x32_bf16 v[52:55], a[228:231], a[92:95], v[52:55]\n\t"
      "s_waitcnt lgkmcnt(4)\n\t"
      "v_mfma_f32_16x16x32_bf16 v[56:59], a[232:235], a[80:83], v[56:59]\n\t"
      "ds_read_b64_tr_b16 a[228:229], v7 offset:40960\n\t"
      "v_mfma_f32_16x16x32_bf16 v[60:63], a[232:235], a[84:87], v[60:63]\n\t"
      "ds_read_b64_tr_b16 a[230:231], v7 offset:43008\n\t"
      "v_lshl_add_u32 v247, v247, 6, s61\n\t"
      "v_mfma_f32_16x16x32_bf16 v[64:67], a[232:235], a[88:91], v[64:67]\n\t"
      "global_load_dwordx4 a[172:175], v1, s[36:37] offset:3072 sc1\n\t"
      "v_mfma_f32_16x16x32_bf16 v[68:71], a[232:235], a[92:95], v[68:71]\n\t"
      "s_add_u32 s36, s36, 4096\n\t"
      "s_addc_u32 s37, s37, 0\n\t"
      "s_waitcnt lgkmcnt(4)\n\t"
      "v_mfma_f32_16x16x32_bf16 v[72:75], a[236:239], a[80:83], v[72:75]\n\t"
      "ds_read_b64_tr_b16 a[232:233], v8 offset:40960\n\t"
      "v_lshrrev_b32 v248, 4, v0\n\t"
      "v_mfma_f32_16x16x32_bf16 v[76:79], a[236:239], a[84:87], v[76:79]\n\t"
      "ds_read_b64_tr_b16 a[234:235], v8 offset:43008\n\t"
      "v_mfma_f32_16x16x32_bf16 v[80:83], a[236:239], a[88:91], v[80:83]\n\t"
      "global_load_dwordx4 a[176:179], v1, s[36:37] offset:0 sc1\n\t"
      "v_mfma_f32_16x16x32_bf16 v[84:87], a[236:239], a[92:95], v[84:87]\n\t"
      "s_waitcnt lgkmcnt(4)\n\t"
      "v_mfma_f32_16x16x32_bf16 v[88:91], a[224:227], a[64:67], v[88:91]\n\t"
      "ds_read_b64_tr_b16 a[236:237], v9 offset:40960\n\t"
      "v_lshl_add_u32 v247, v248, 3, v247\n\t"
      "v_mfma_f32_16x16x32_bf16 v[92:95], a[224:227], a[68:71], v[92:95]\n\t"
      "ds_read_b64_tr_b16 a[238:239], v9 offset:43008\n\t"
      "s_add_u32 m0, s62, 91136\n\t"
      "v_mfma_f32_16x16x32_bf16 v[96:99], a[224:227], a[72:75], v[96:99]\n\t"
      "global_load_lds_dwordx4 v5, s[42:43]\n\t"
      "v_mfma_f32_16x16x32_bf16 v[100:103], a[224:227], a[76:79], v[100:103]\n\t"
      "s_add_u32 s42, s42, 262144\n\t"
      "s_addc_u32 s43, s43, 0\n\t"
      "s_waitcnt lgkmcnt(4)\n\t"
      "v_mfma_f32_16x16x32_bf16 v[104:107], a[228:231], a[64:67], v[104:107]\n\t"
      "ds_read_b64_tr_b16 a[224:225], v6 offset:45056\n\t"
      "s_xor_b32 s64, %15, 3\n\t"
      "v_mfma_f32_16x16x32_bf16 v[108:111], a[228:231], a[68:71], v[108:111]\n\t"
      "ds_read_b64_tr_b16 a[226:227], v6 offset:47104\n\t"
      "v_mfma_f32_16x16x32_bf16 v[112:115], a[228:231], a[72:75], v[112:115]\n\t"
      "global_load_dwordx4 a[180:183], v1, s[36:37] offset:1024 sc1\n\t"
      "v_mfma_f32_16x16x32_bf16 v[116:119], a[228:231], a[76:79], v[116:119]\n\t"
      "s_lshl_b32 s64, s64, 2\n\t"
      "s_waitcnt lgkmcnt(4)\n\t"
      "v_mfma_f32_16x16x32_bf16 v[120:123], a[232:235], a[64:67], v[120:123]\n\t"
      "ds_read_b64_tr_b16 a[228:229], v7 offset:45056\n\t"
      "v_mfma_f32_16x16x32_bf16 v[124:127], a[232:235], a[68:71], v[124:127]\n\t"
      "ds_read_b64_tr_b16 a[230:231], v7 offset:47104\n\t"
      "v_mfma_f32_16x16x32_bf16 v[128:131], a[232:235], a[72:75], v[128:131]\n\t"
      "global_load_dwordx4 a[184:187], v1, s[36:37] offset:2048 sc1\n\t"
      "v_mfma_f32_16x16x32_bf16 v[132:135], a[232:235], a[76:79], v[132:135]\n\t"
      "s_add_u32 s64, s64, %14\n\t"
      "s_waitcnt lgkmcnt(4)\n\t"
      "v_mfma_f32_16x16x32_bf16 v[136:139], a[236:239], a[64:67], v[136:139]\n\t"
      "ds_read_b64_tr_b16 a[232:233], v8 offset:45056\n\t"
      "v_mfma_f32_16x16x32_bf16 v[140:143], a[236:239], a[68:71], v[140:143]\n\t"
      "ds_read_b64_tr_b16 a[234:235], v8 offset:47104\n\t"
      "v_mfma_f32_16x16x32_bf16 v[144:147], a[236:239], a[72:75], v[144:147]\n\t"
      "global_load_dwordx4 a[188:191], v1, s[36:37] offset:3072 sc1\n\t"
      "v_mfma_f32_16x16x32_bf16 v[148:151], a[236:239], a[76:79], v[148:151]\n\t"
      "s_add_u32 s36, s36, 12288\n\t"
      "s_addc_u32 s37, s37, 0\n\t"
      "s_waitcnt lgkmcnt(4)\n\t"
      "v_mfma_f32_16x16x32_bf16 v[88:91], a[224:227], a[80:83], v[88:91]\n\t"
      "ds_read_b64_tr_b16 a[236:237], v9 offset:45056\n\t"
      "s_mul_i32 s68, s64, 12288\n\t"
      "v_mfma_f32_16x16x32_bf16 v[92:95], a[224:227], a[84:87], v[92:95]\n\t"
      "ds_read_b64_tr_b16 a[238:239], v9 offset:47104\n\t"
      "v_mfma_f32_16x16x32_bf16 v[96:99], a[224:227], a[88:91], v[96:99]\n\t"
      "global_load_dwordx4 a[192:195], v1, s[36:37] offset:0 sc1\n\t"
      "v_mfma_f32_16x16x32_bf16 v[100:103], a[224:227], a[92:95], v[100:103]\n\t"
      "s_add_u32 s68, s68, 35651584\n\t"
      "s_waitcnt lgkmcnt(4)\n\t"
      "v_mfma_f32_16x16x32_bf16 v[104:107], a[228:231], a[80:83], v[104:107]\n\t"
      "ds_read_b64_tr_b16 a[224:225], v6 offset:49152\n\t"
      "v_mfma_f32_16x16x32_bf16 v[108:111], a[228:231], a[84:87], v[108:111]\n\t"
      "ds_read_b64_tr_b16 a[226:227], v6 offset:51200\n\t"
      "v_mfma_f32_16x16x32_bf16 v[112:115], a[228:231], a[88:91], v[112:115]\n\t"
      "global_load_dwordx4 a[196:199], v1, s[36:37] offset:1024 sc1\n\t"
      "v_mfma_f32_16x16x32_bf16 v[116:119], a[228:231], a[92:95], v[116:119]\n\t"
      "s_lshl_b32 s71, s64, 9\n\t"
      "s_waitcnt lgkmcnt(4)\n\t"
      "v_mfma_f32_16x16x32_bf16 v[120:123], a[232:235], a[80:83], v[120:123]\n\t"
      "ds_read_b64_tr_b16 a[228:229], v7 offset:49152\n\t"
      "v_mfma_f32_16x16x32_bf16 v[124:127], a[232:235], a[84:87], v[124:127]\n\t"
      "ds_read_b64_tr_b16 a[230:231], v7 offset:51200\n\t"
      "v_mfma_f32_16x16x32_bf16 v[128:131], a[232:235], a[88:91], v[128:131]\n\t"
      "global_load_dwordx4 a[200:203], v1, s[36:37] offset:2048 sc1\n\t"
      "v_mfma_f32_16x16x32_bf16 v[132:135], a[232:235], a[92:95], v[132:135]\n\t"
      "s_add_u32 s71, s71, 52953088\n\t"
      "s_waitcnt lgkmcnt(4)\n\t"
      "v_mfma_f32_16x16x32_bf16 v[136:139], a[236:239], a[80:83], v[136:139]\n\t"
      "ds_read_b64_tr_b16 a[232:233], v8 offset:49152\n\t"
      "v_mfma_f32_16x16x32_bf16 v[140:143], a[236:239], a[84:87], v[140:143]\n\t"
      "ds_read_b64_tr_b16 a[234:235], v8 offset:51200\n\t"
      "v_mfma_f32_16x16x32_bf16 v[144:147], a[236:239], a[88:91], v[144:147]\n\t"
      "global_load_dwordx4 a[204:207], v1, s[36:37] offset:3072 sc1\n\t"
      "v_mfma_f32_16x16x32_bf16 v[148:151], a[236:239], a[92:95], v[148:151]\n\t"
      "s_add_u32 s36, s36, 4096\n\t"
      "s_addc_u32 s37, s37, 0\n\t"
      "s_waitcnt vmcnt(32) lgkmcnt(4)\n\t"
      "v_mfma_f32_16x16x32_bf16 v[24:27], a[224:227], a[96:99], v[24:27]\n\t"
      "ds_read_b64_tr_b16 a[236:237], v9 offset:49152\n\t"
      "s_xor_b32 s64, %15, 2\n\t"
      "v_mfma_f32_16x16x32_bf16 v[28:31], a[224:227], a[100:103], v[28:31]\n\t"
      "ds_read_b64_tr_b16 a[238:239], v9 offset:51200\n\t"
      "s_lshl_b32 s64, s64, 2\n\t"
      "v_mfma_f32_16x16x32_bf16 v[32:35], a[224:227], a[104:107], v[32:35]\n\t"
      "global_load_dwordx4 a[208:211], v1, s[36:37] offset:0 sc1\n\t"
      "v_mfma_f32_16x16x32_bf16 v[36:39], a[224:227], a[108:111], v[36:39]\n\t"
      "s_waitcnt lgkmcnt(4)\n\t"
      "v_mfma_f32_16x16x32_bf16 v[40:43], a[228:231], a[96:99], v[40:43]\n\t"
      "ds_read_b64_tr_b16 a[224:225], v6 offset:53248\n\t"
      "v_mfma_f32_16x16x32_bf16 v[44:47], a[228:231], a[100:103], v[44:47]\n\t"
      "ds_read_b64_tr_b16 a[226:227], v6 offset:55296\n\t"
      "s_add_u32 s64, s64, %14\n\t"
      "v_mfma_f32_16x16x32_bf16 v[48:51], a[228:231], a[104:107], v[48:51]\n\t"
      "global_load_dwordx4 a[212:215], v1, s[36:37] offset:1024 sc1\n\t"
      "v_mfma_f32_16x16x32_bf16 v[52:55], a[228:231], a[108:111], v[52:55]\n\t"
      "s_waitcnt lgkmcnt(4)\n\t"
      "v_mfma_f32_16x16x32_bf16 v[56:59], a[232:235], a[96:99], v[56:59]\n\t"
      "ds_read_b64_tr_b16 a[228:229], v7 offset:53248\n\t"
      "v_mfma_f32_16x16x32_bf16 v[60:63], a[232:235], a[100:103], v[60:63]\n\t"
      "ds_read_b64_tr_b16 a[230:231], v7 offset:55296\n\t"
      "s_mul_i32 s69, s64, 12288\n\t"
      "v_mfma_f32_16x16x32_bf16 v[64:67], a[232:235], a[104:107], v[64:67]\n\t"
      "global_load_dwordx4 a[216:219], v1, s[36:37] offset:2048 sc1\n\t"
      "v_mfma_f32_16x16x32_bf16 v[68:71], a[232:235], a[108:111], v[68:71]\n\t"
      "s_waitcnt lgkmcnt(4)\n\t"
      "v_mfma_f32_16x16x32_bf16 v[72:75], a[236:239], a[96:99], v[72:75]\n\t"
      "ds_read_b64_tr_b16 a[232:233], v8 offset:53248\n\t"
      "s_add_u32 s69, s69, 35655680\n\t"
      "v_mfma_f32_16x16x32_bf16 v[76:79], a[236:239], a[100:103], v[76:79]\n\t"
      "ds_read_b64_tr_b16 a[234:235], v8 offset:55296\n\t"
      "v_mfma_f32_16x16x32_bf16 v[80:83], a[236:239], a[104:107], v[80:83]\n\t"
      "global_load_dwordx4 a[220:223], v1, s[36:37] offset:3072 sc1\n\t"
      "v_mfma_f32_16x16x32_bf16 v[84:87], a[236:239], a[108:111], v[84:87]\n\t"
      "s_add_u32 s36, s36, 12288\n\t"
      "s_addc_u32 s37, s37, 0\n\t"
      "s_waitcnt vmcnt(32) lgkmcnt(4)\n\t"
      "v_mfma_f32_16x16x32_bf16 v[24:27], a[224:227], a[112:115], v[24:27]\n\t"
      "ds_read_b64_tr_b16 a[236:237], v9 offset:53248\n\t"
      "v_mfma_f32_16x16x32_bf16 v[28:31], a[224:227], a[116:119], v[28:31]\n\t"
      "ds_read_b64_tr_b16 a[238:239], v9 offset:55296\n\t"
      "v_mfma_f32_16x16x32_bf16 v[32:35], a[224:227], a[120:123], v[32:35]\n\t"
      "s_lshl_b32 s72, s64, 9\n\t"
      "v_mfma_f32_16x16x32_bf16 v[36:39], a[224:227], a[124:127], v[36:39]\n\t"
      "s_waitcnt lgkmcnt(4)\n\t"
      "v_mfma_f32_16x16x32_bf16 v[40:43], a[228:231], a[112:115], v[40:43]\n\t"
      "ds_read_b64_tr_b16 a[224:225], v6 offset:57344\n\t"
      "v_mfma_f32_16x16x32_bf16 v[44:47], a[228:231], a[116:119], v[44:47]\n\t"
      "ds_read_b64_tr_b16 a[226:227], v6 offset:59392\n\t"
      "v_mfma_f32_16x16x32_bf16 v[48:51], a[228:231], a[120:123], v[48:51]\n\t"
      "s_add_u32 s72, s72, 52953216\n\t"
      "v_mfma_f32_16x16x32_bf16 v[52:55], a[228:231], a[124:127], v[52:55]\n\t"
      "s_waitcnt lgkmcnt(4)\n\t"
      "v_mfma_f32_16x16x32_bf16 v[56:59], a[232:235], a[112:115], v[56:59]\n\t"
      "ds_read_b64_tr_b16 a[228:229], v7 offset:57344\n\t"
      "v_mfma_f32_16x16x32_bf16 v[60:63], a[232:235], a[116:119], v[60:63]\n\t"
      "ds_read_b64_tr_b16 a[230:231], v7 offset:59392\n\t"
      "v_mfma_f32_16x16x32_bf16 v[64:67], a[232:235], a[120:123], v[64:67]\n\t"
      "s_xor_b32 s64, %15, 1\n\t"
      "v_mfma_f32_16x16x32_bf16 v[68:71], a[232:235], a[124:127], v[68:71]\n\t"
      "s_waitcnt lgkmcnt(4)\n\t"
      "v_mfma_f32_16x16x32_bf16 v[72:75], a[236:239], a[112:115], v[72:75]\n\t"
      "ds_read_b64_tr_b16 a[232:233], v8 offset:57344\n\t"
      "v_mfma_f32_16x16x32_bf16 v[76:79], a[236:239], a[116:119], v[76:79]\n\t"
      "ds_read_b64_tr_b16 a[234:235], v8 offset:59392\n\t"
      "v_mfma_f32_16x16x32_bf16 v[80:83], a[236:239], a[120:123], v[80:83]\n\t"
      "s_lshl_b32 s64, s64, 2\n\t"
      "v_mfma_f32_16x16x32_bf16 v[84:87], a[236:239], a[124:127], v[84:87]\n\t"
      "s_waitcnt vmcnt(28) lgkmcnt(4)\n\t"
      "v_mfma_f32_16x16x32_bf16 v[24:27], a[224:227], a[128:131], v[24:27]\n\t"
      "ds_read_b64_tr_b16 a[236:237], v9 offset:57344\n\t"
      "v_mfma_f32_16x16x32_bf16 v[28:31], a[224:227], a[132:135], v[28:31]\n\t"
      "ds_read_b64_tr_b16 a[238:239], v9 offset:59392\n\t"
      "v_mfma_f32_16x16x32_bf16 v[32:35], a[224:227], a[136:139], v[32:35]\n\t"
      "s_add_u32 s64, s64, %14\n\t"
      "v_mfma_f32_16x16x32_bf16 v[36:39], a[224:227], a[140:143], v[36:39]\n\t"
      "s_waitcnt lgkmcnt(4)\n\t"
      "v_mfma_f32_16x16x32_bf16 v[40:43], a[228:231], a[128:131], v[40:43]\n\t"
      "ds_read_b64_tr_b16 a[224:225], v6 offset:61440\n\t"
      "s_add_u32 s83, s83, 4294574080\n\t"
      "v_mfma_f32_16x16x32_bf16 v[44:47], a[228:231], a[132:135], v[44:47]\n\t"
      "ds_read_b64_tr_b16 a[226:227], v6 offset:63488\n\t"
      "v_mfma_f32_16x16x32_bf16 v[48:51], a[228:231], a[136:139], v[48:51]\n\t"
      "s_mul_i32 s70, s64, 12288\n\t"
      "v_mfma_f32_16x16x32_bf16 v[52:55], a[228:231], a[140:143], v[52:55]\n\t"
      "s_add_u32 s70, s70, 35659776\n\t"
      "s_waitcnt lgkmcnt(4)\n\t"
      "v_mfma_f32_16x16x32_bf16 v[56:59], a[232:235], a[128:131], v[56:59]\n\t"
      "ds_read_b64_tr_b16 a[228:229], v7 offset:61440\n\t"
      "v_mfma_f32_16x16x32_bf16 v[60:63], a[232:235], a[132:135], v[60:63]\n\t"
      "ds_read_b64_tr_b16 a[230:231], v7 offset:63488\n\t"
      "v_mfma_f32_16x16x32_bf16 v[64:67], a[232:235], a[136:139], v[64:67]\n\t"
      "s_lshl_b32 s73, s64, 9\n\t"
      "v_mfma_f32_16x16x32_bf16 v[68:71], a[232:235], a[140:143], v[68:71]\n\t"
      "buffer_load_dwordx2 v[14:15], v20, s[44:47], s83 offen sc1\n\t"
      "s_waitcnt lgkmcnt(4)\n\t"
      "v_mfma_f32_16x16x32_bf16 v[72:75], a[236:239], a[128:131], v[72:75]\n\t"
      "ds_read_b64_tr_b16 a[232:233], v8 offset:61440\n\t"
      "v_mfma_f32_16x16x32_bf16 v[76:79], a[236:239], a[132:135], v[76:79]\n\t"
      "ds_read_b64_tr_b16 a[234:235], v8 offset:63488\n\t"
      "v_mfma_f32_16x16x32_bf16 v[80:83], a[236:239], a[136:139], v[80:83]\n\t"
      "s_add_u32 s73, s73, 52953344\n\t"
      "v_mfma_f32_16x16x32_bf16 v[84:87], a[236:239], a[140:143], v[84:87]\n\t"
      "s_waitcnt vmcnt(25) lgkmcnt(4)\n\t"
      "v_mfma_f32_16x16x32_bf16 v[24:27], a[224:227], a[144:147], v[24:27]\n\t"
      "ds_read_b64_tr_b16 a[236:237], v9 offset:61440\n\t"
      "v_mfma_f32_16x16x32_bf16 v[28:31], a[224:227], a[148:151], v[28:31]\n\t"
      "ds_read_b64_tr_b16 a[238:239], v9 offset:63488\n\t"
      "v_mfma_f32_16x16x32_bf16 v[32:35], a[224:227], a[152:155], v[32:35]\n\t"
      "global_load_dwordx4 a[0:3], v1, s[36:37] offset:0 sc1\n\t"
      "v_mfma_f32_16x16x32_bf16 v[36:39], a[224:227], a[156:159], v[36:39]\n\t"
      "s_xor_b32 s64, %15, 0\n\t"
      "s_waitcnt vmcnt(13) lgkmcnt(4)\n\t"
      "s_barrier\n\t"
      "v_mfma_f32_16x16x32_bf16 v[40:43], a[228:231], a[144:147], v[40:43]\n\t"
      "ds_read_b64_tr_b16 a[224:225], v152 offset:0\n\t"
      "v_mfma_f32_16x16x32_bf16 v[44:47], a[228:231], a[148:151], v[44:47]\n\t"
      "ds_read_b64_tr_b16 a[226:227], v152 offset:2048\n\t"
      "s_add_u32 m0, s62, 0\n\t"
      "v_mfma_f32_16x16x32_bf16 v[48:51], a[228:231], a[152:155], v[48:51]\n\t"
      "global_load_lds_dwordx4 v2, s[40:41]\n\t"
      "v_mfma_f32_16x16x32_bf16 v[52:55], a[228:231], a[156:159], v[52:55]\n\t"
      "s_lshl_b32 s64, s64, 2\n\t"
      "s_waitcnt lgkmcnt(4)\n\t"
      "v_mfma_f32_16x16x32_bf16 v[56:59], a[232:235], a[144:147], v[56:59]\n\t"
      "ds_read_b64_tr_b16 a[228:229], v153 offset:0\n\t"
      "v_mfma_f32_16x16x32_bf16 v[60:63], a[232:235], a[148:151], v[60:63]\n\t"
      "ds_read_b64_tr_b16 a[230:231], v153 offset:2048\n\t"
      "s_add_u32 m0, s62, 1024\n\t"
      "v_mfma_f32_16x16x32_bf16 v[64:67], a[232:235], a[152:155], v[64:67]\n\t"
      "global_load_lds_dwordx4 v3, s[40:41]\n\t"
      "v_mfma_f32_16x16x32_bf16 v[68:71], a[232:235], a[156:159], v[68:71]\n\t"
      "s_sub_u32 s40, s40, 1572864\n\t"
      "s_subb_u32 s41, s41, 0\n\t"
      "s_waitcnt lgkmcnt(4)\n\t"
      "v_mfma_f32_16x16x32_bf16 v[72:75], a[236:239], a[144:147], v[72:75]\n\t"
      "ds_read_b64_tr_b16 a[232:233], v154 offset:0\n\t"
      "s_add_u32 s64, s64, %14\n\t"
      "v_mfma_f32_16x16x32_bf16 v[76:79], a[236:239], a[148:151], v[76:79]\n\t"
      "ds_read_b64_tr_b16 a[234:235], v154 offset:2048\n\t"
      "s_add_u32 m0, s62, 8192\n\t"
      "v_mfma_f32_16x16x32_bf16 v[80:83], a[236:239], a[152:155], v[80:83]\n\t"
      "global_load_lds_dwordx4 v4, s[42:43]\n\t"
      "v_mfma_f32_16x16x32_bf16 v[84:87], a[236:239], a[156:159], v[84:87]\n\t"
      "s_mul_i32 s74, s64, 12288\n\t"
      "s_waitcnt vmcnt(18) lgkmcnt(4)\n\t"
      "v_mfma_f32_16x16x32_bf16 v[24:27], a[224:227], a[160:163], v[24:27]\n\t"
      "ds_read_b64_tr_b16 a[236:237], v155 offset:0\n\t"
      "s_add_u32 s74, s74, 35651584\n\t"
      "v_mfma_f32_16x16x32_bf16 v[28:31], a[224:227], a[164:167], v[28:31]\n\t"
      "ds_read_b64_tr_b16 a[238:239], v155 offset:2048\n\t"
      "s_add_u32 m0, s62, 9216\n\t"
      "v_mfma_f32_16x16x32_bf16 v[32:35], a[224:227], a[168:171], v[32:35]\n\t"
      "global_load_lds_dwordx4 v5, s[42:43]\n\t"
      "v_mfma_f32_16x16x32_bf16 v[36:39], a[224:227], a[172:175], v[36:39]\n\t"
      "s_add_u32 s42, s42, 262144\n\t"
      "s_addc_u32 s43, s43, 0\n\t"
      "s_waitcnt lgkmcnt(4)\n\t"
      "v_mfma_f32_16x16x32_bf16 v[40:43], a[228:231], a[160:163], v[40:43]\n\t"
      "ds_read_b64_tr_b16 a[224:225], v152 offset:4096\n\t"
      "s_lshl_b32 s76, s64, 9\n\t"
      "v_mfma_f32_16x16x32_bf16 v[44:47], a[228:231], a[164:167], v[44:47]\n\t"
      "ds_read_b64_tr_b16 a[226:227], v152 offset:6144\n\t"
      "s_add_u32 m0, s62, 16384\n\t"
      "v_mfma_f32_16x16x32_bf16 v[48:51], a[228:231], a[168:171], v[48:51]\n\t"
      "global_load_lds_dwordx4 v2, s[40:41]\n\t"
      "v_mfma_f32_16x16x32_bf16 v[52:55], a[228:231], a[172:175], v[52:55]\n\t"
      "s_waitcnt lgkmcnt(4)\n\t"
      "v_mfma_f32_16x16x32_bf16 v[56:59], a[232:235], a[160:163], v[56:59]\n\t"
      "ds_read_b64_tr_b16 a[228:229], v153 offset:4096\n\t"
      "s_add_u32 s76, s76, 52953088\n\t"
      "v_mfma_f32_16x16x32_bf16 v[60:63], a[232:235], a[164:167], v[60:63]\n\t"
      "ds_read_b64_tr_b16 a[230:231], v153 offset:6144\n\t"
      "s_add_u32 m0, s62, 17408\n\t"
      "v_mfma_f32_16x16x32_bf16 v[64:67], a[232:235], a[168:171], v[64:67]\n\t"
      "global_load_lds_dwordx4 v3, s[40:41]\n\t"
      "v_mfma_f32_16x16x32_bf16 v[68:71], a[232:235], a[172:175], v[68:71]\n\t"
      "s_add_u32 s40, s40, 262144\n\t"
      "s_addc_u32 s41, s41, 0\n\t"
      "s_waitcnt lgkmcnt(4)\n\t"
      "v_mfma_f32_16x16x32_bf16 v[72:75], a[236:239], a[160:163], v[72:75]\n\t"
      "ds_read_b64_tr_b16 a[232:233], v154 offset:4096\n\t"
      "s_add_u32 s64, s74, 4096\n\t"
      "v_mfma_f32_16x16x32_bf16 v[76:79], a[236:239], a[164:167], v[76:79]\n\t"
      "ds_read_b64_tr_b16 a[234:235], v154 offset:6144\n\t"
      "v_mfma_f32_16x16x32_bf16 v[80:83], a[236:239], a[168:171], v[80:83]\n\t"
      "global_load_dwordx4 a[4:7], v1, s[36:37] offset:1024 sc1\n\t"
      "v_mfma_f32_16x16x32_bf16 v[84:87], a[236:239], a[172:175], v[84:87]\n\t"
      "s_add_u32 s65, s74, 8192\n\t"
      "s_waitcnt vmcnt(17) lgkmcnt(4)\n\t"
      "v_mfma_f32_16x16x32_bf16 v[24:27], a[224:227], a[176:179], v[24:27]\n\t"
      "ds_read_b64_tr_b16 a[236:237], v155 offset:4096\n\t"
      "v_mfma_f32_16x16x32_bf16 v[28:31], a[224:227], a[180:183], v[28:31]\n\t"
      "ds_read_b64_tr_b16 a[238:239], v155 offset:6144\n\t"
      "s_add_u32 m0, s62, 24576\n\t"
      "v_mfma_f32_16x16x32_bf16 v[32:35], a[224:227], a[184:187], v[32:35]\n\t"
      "global_load_lds_dwordx4 v4, s[42:43]\n\t"
      "v_mfma_f32_16x16x32_bf16 v[36:39], a[224:227], a[188:191], v[36:39]\n\t"
      "v_mov_b32 v18, 0x3f80\n\t"
      "s_waitcnt lgkmcnt(4)\n\t"
      "v_mfma_f32_16x16x32_bf16 v[40:43], a[228:231], a[176:179], v[40:43]\n\t"
      "ds_read_b64_tr_b16 a[224:225], v152 offset:8192\n\t"
      "v_mfma_f32_16x16x32_bf16 v[44:47], a[228:231], a[180:183], v[44:47]\n\t"
      "ds_read_b64_tr_b16 a[226:227], v152 offset:10240\n\t"
      "v_mfma_f32_16x16x32_bf16 v[48:51], a[228:231], a[184:187], v[48:51]\n\t"
      "global_load_dwordx4 a[8:11], v1, s[36:37] offset:2048 sc1\n\t"
      "v_mfma_f32_16x16x32_bf16 v[52:55], a[228:231], a[188:191], v[52:55]\n\t"
      "v_mov_b32 v19, 0x3f800000\n\t"
      "s_waitcnt lgkmcnt(4)\n\t"
      "v_mfma_f32_16x16x32_bf16 v[56:59], a[232:235], a[176:179], v[56:59]\n\t"
      "ds_read_b64_tr_b16 a[228:229], v153 offset:8192\n\t"
      "v_mfma_f32_16x16x32_bf16 v[60:63], a[232:235], a[180:183], v[60:63]\n\t"
      "ds_read_b64_tr_b16 a[230:231], v153 offset:10240\n\t"
      "v_mfma_f32_16x16x32_bf16 v[64:67], a[232:235], a[184:187], v[64:67]\n\t"
      "global_load_dwordx4 a[12:15], v1, s[36:37] offset:3072 sc1\n\t"
      "v_mfma_f32_16x16x32_bf16 v[68:71], a[232:235], a[188:191], v[68:71]\n\t"
      "s_add_u32 s36, s36, 4096\n\t"
      "s_addc_u32 s37, s37, 0\n\t"
      "s_waitcnt lgkmcnt(4)\n\t"
      "v_mfma_f32_16x16x32_bf16 v[72:75], a[236:239], a[176:179], v[72:75]\n\t"
      "ds_read_b64_tr_b16 a[232:233], v154 offset:8192\n\t"
      "v_mov_b64 v[184:185], 0\n\t"
      "v_mfma_f32_16x16x32_bf16 v[76:79], a[236:239], a[180:183], v[76:79]\n\t"
      "ds_read_b64_tr_b16 a[234:235], v154 offset:10240\n\t"
      "v_mfma_f32_16x16x32_bf16 v[80:83], a[236:239], a[184:187], v[80:83]\n\t"
      "global_load_dwordx4 a[16:19], v1, s[36:37] offset:0 sc1\n\t"
      "v_mfma_f32_16x16x32_bf16 v[84:87], a[236:239], a[188:191], v[84:87]\n\t"
      "v_mov_b64 v[188:189], 0\n\t"
      "s_waitcnt lgkmcnt(4)\n\t"
      "v_mfma_f32_16x16x32_bf16 v[88:91], a[224:227], a[96:99], v[88:91]\n\t"
      "ds_read_b64_tr_b16 a[236:237], v155 offset:8192\n\t"
      "v_mfma_f32_16x16x32_bf16 v[92:95], a[224:227], a[100:103], v[92:95]\n\t"
      "ds_read_b64_tr_b16 a[238:239], v155 offset:10240\n\t"
      "s_add_u32 m0, s62, 25600\n\t"
      "v_mfma_f32_16x16x32_bf16 v[96:99], a[224:227], a[104:107], v[96:99]\n\t"
      "global_load_lds_dwordx4 v5, s[42:43]\n\t"
      "v_mfma_f32_16x16x32_bf16 v[100:103], a[224:227], a[108:111], v[100:103]\n\t"
      "s_add_u32 s42, s42, 262144\n\t"
      "s_addc_u32 s43, s43, 0\n\t"
      "s_waitcnt lgkmcnt(4)\n\t"
      "v_mfma_f32_16x16x32_bf16 v[104:107], a[228:231], a[96:99], v[104:107]\n\t"
      "ds_read_b64_tr_b16 a[224:225], v152 offset:12288\n\t"
      "v_cmp_eq_u32 s[56:57], 0, v17\n\t"
      "v_mfma_f32_16x16x32_bf16 v[108:111], a[228:231], a[100:103], v[108:111]\n\t"
      "ds_read_b64_tr_b16 a[226:227], v152 offset:14336\n\t"
      "v_mfma_f32_16x16x32_bf16 v[112:115], a[228:231], a[104:107], v[112:115]\n\t"
      "global_load_dwordx4 a[20:23], v1, s[36:37] offset:1024 sc1\n\t"
      "v_mfma_f32_16x16x32_bf16 v[116:119], a[228:231], a[108:111], v[116:119]\n\t"
      "v_cndmask_b32 v186, v13, v18, s[56:57]\n\t"
      "s_waitcnt lgkmcnt(4)\n\t"
      "v_mfma_f32_16x16x32_bf16 v[120:123], a[232:235], a[96:99], v[120:123]\n\t"
      "ds_read_b64_tr_b16 a[228:229], v153 offset:12288\n\t"
      "v_mfma_f32_16x16x32_bf16 v[124:127], a[232:235], a[100:103], v[124:127]\n\t"
      "ds_read_b64_tr_b16 a[230:231], v153 offset:14336\n\t"
      "v_cmp_eq_u32 s[56:57], 1, v17\n\t"
      "v_mfma_f32_16x16x32_bf16 v[128:131], a[232:235], a[104:107], v[128:131]\n\t"
      "global_load_dwordx4 a[24:27], v1, s[36:37] offset:2048 sc1\n\t"
      "v_mfma_f32_16x16x32_bf16 v[132:135], a[232:235], a[108:111], v[132:135]\n\t"
      "s_waitcnt lgkmcnt(4)\n\t"
      "v_mfma_f32_16x16x32_bf16 v[136:139], a[236:239], a[96:99], v[136:139]\n\t"
      "ds_read_b64_tr_b16 a[232:233], v154 offset:12288\n\t"
      "v_mfma_f32_16x16x32_bf16 v[140:143], a[236:239], a[100:103], v[140:143]\n\t"
      "ds_read_b64_tr_b16 a[234:235], v154 offset:14336\n\t"
      "v_cndmask_b32 v186, v186, v19, s[56:57]\n\t"
      "v_mfma_f32_16x16x32_bf16 v[144:147], a[236:239], a[104:107], v[144:147]\n\t"
      "global_load_dwordx4 a[28:31], v1, s[36:37] offset:3072 sc1\n\t"
      "v_mfma_f32_16x16x32_bf16 v[148:151], a[236:239], a[108:111], v[148:151]\n\t"
      "s_sub_u32 s36, s36, 102400\n\t"
      "s_subb_u32 s37, s37, 0\n\t"
      "s_waitcnt lgkmcnt(4)\n\t"
      "s_waitcnt vmcnt(16)\n\t"
      "v_cmp_eq_u64 vcc, s[58:59], v[14:15]\n\t"
      "s_nop 1\n\t"
      "s_cmp_eq_u64 vcc, -1\n\t"
      "s_cbranch_scc1 .Lkm_a16_ok_%=\n\t"
      "s_mov_b32 s67, 0\n\t"
      ".Lkm_a16_spin_%=:\n\t"
      "s_sleep 1\n\t"
      "buffer_load_dwordx2 v[14:15], v20, s[44:47], s83 offen sc1\n\t"
      "s_add_u32 s67, s67, 1\n\t"
      "s_cmp_ge_u32 s67, 262144\n\t"
      "s_cbranch_scc1 .Lkm_a16_ok_%=\n\t"
      "s_waitcnt vmcnt(0)\n\t"
      "v_cmp_eq_u64 vcc, s[58:59], v[14:15]\n\t"
      "s_nop 1\n\t"
      "s_cmp_eq_u64 vcc, -1\n\t"
      "s_cbranch_scc0 .Lkm_a16_spin_%=\n\t"
      ".Lkm_a16_ok_%=:\n\t"
      "v_mfma_f32_16x16x32_bf16 v[88:91], a[224:227], a[112:115], v[88:91]\n\t"
      "ds_read_b64_tr_b16 a[236:237], v155 offset:12288\n\t"
      "v_mfma_f32_16x16x32_bf16 v[92:95], a[224:227], a[116:119], v[92:95]\n\t"
      "ds_read_b64_tr_b16 a[238:239], v155 offset:14336\n\t"
      "v_cmp_eq_u32 s[56:57], 2, v17\n\t"
      "v_mfma_f32_16x16x32_bf16 v[96:99], a[224:227], a[120:123], v[96:99]\n\t"
      "global_load_dwordx4 a[32:35], v1, s[36:37] offset:0 sc1\n\t"
      "v_mfma_f32_16x16x32_bf16 v[100:103], a[224:227], a[124:127], v[100:103]\n\t"
      "s_waitcnt lgkmcnt(4)\n\t"
      "v_mfma_f32_16x16x32_bf16 v[104:107], a[228:231], a[112:115], v[104:107]\n\t"
      "ds_read_b64_tr_b16 a[224:225], v152 offset:16384\n\t"
      "v_cndmask_b32 v187, v13, v18, s[56:57]\n\t"
      "v_mfma_f32_16x16x32_bf16 v[108:111], a[228:231], a[116:119], v[108:111]\n\t"
      "ds_read_b64_tr_b16 a[226:227], v152 offset:18432\n\t"
      "v_mfma_f32_16x16x32_bf16 v[112:115], a[228:231], a[120:123], v[112:115]\n\t"
      "global_load_dwordx4 a[36:39], v1, s[36:37] offset:1024 sc1\n\t"
      "v_mfma_f32_16x16x32_bf16 v[116:119], a[228:231], a[124:127], v[116:119]\n\t"
      "s_waitcnt lgkmcnt(4)\n\t"
      "v_mfma_f32_16x16x32_bf16 v[120:123], a[232:235], a[112:115], v[120:123]\n\t"
      "ds_read_b64_tr_b16 a[228:229], v153 offset:16384\n\t"
      "v_cmp_eq_u32 s[56:57], 3, v17\n\t"
      "v_mfma_f32_16x16x32_bf16 v[124:127], a[232:235], a[116:119], v[124:127]\n\t"
      "ds_read_b64_tr_b16 a[230:231], v153 offset:18432\n\t"
      "v_mfma_f32_16x16x32_bf16 v[128:131], a[232:235], a[120:123], v[128:131]\n\t"
      "global_load_dwordx4 a[40:43], v1, s[36:37] offset:2048 sc1\n\t"
      "v_mfma_f32_16x16x32_bf16 v[132:135], a[232:235], a[124:127], v[132:135]\n\t"
      "s_waitcnt lgkmcnt(4)\n\t"
      "v_mfma_f32_16x16x32_bf16 v[136:139], a[236:239], a[112:115], v[136:139]\n\t"
      "ds_read_b64_tr_b16 a[232:233], v154 offset:16384\n\t"
      "v_cndmask_b32 v187, v187, v19, s[56:57]\n\t"
      "v_mfma_f32_16x16x32_bf16 v[140:143], a[236:239], a[116:119], v[140:143]\n\t"
      "ds_read_b64_tr_b16 a[234:235], v154 offset:18432\n\t"
      "v_mfma_f32_16x16x32_bf16 v[144:147], a[236:239], a[120:123], v[144:147]\n\t"
      "global_load_dwordx4 a[44:47], v1, s[36:37] offset:3072 sc1\n\t"
      "v_mfma_f32_16x16x32_bf16 v[148:151], a[236:239], a[124:127], v[148:151]\n\t"
      "s_add_u32 s36, s36, 4096\n\t"
      "s_addc_u32 s37, s37, 0\n\t"
      "s_waitcnt vmcnt(25) lgkmcnt(4)\n\t"
      "v_mfma_f32_16x16x32_bf16 v[24:27], a[224:227], a[192:195], v[24:27]\n\t"
      "ds_read_b64_tr_b16 a[236:237], v155 offset:16384\n\t"
      "v_mfma_f32_16x16x32_bf16 v[28:31], a[224:227], a[196:199], v[28:31]\n\t"
      "ds_read_b64_tr_b16 a[238:239], v155 offset:18432\n\t"
      "v_mfma_f32_16x16x32_bf16 v[32:35], a[224:227], a[200:203], v[32:35]\n\t"
      "global_load_dwordx2 a[48:49], v16, s[36:37] offset:0 sc1\n\t"
      "v_mfma_f32_16x16x32_bf16 v[36:39], a[224:227], a[204:207], v[36:39]\n\t"
      "s_waitcnt lgkmcnt(4)\n\t"
      "v_mfma_f32_16x16x32_bf16 v[40:43], a[228:231], a[192:195], v[40:43]\n\t"
      "ds_read_b64_tr_b16 a[224:225], v152 offset:20480\n\t"
      "v_mfma_f32_16x16x32_bf16 v[44:47], a[228:231], a[196:199], v[44:47]\n\t"
      "ds_read_b64_tr_b16 a[226:227], v152 offset:22528\n\t"
      "v_mfma_f32_16x16x32_bf16 v[48:51], a[228:231], a[200:203], v[48:51]\n\t"
      "global_load_dwordx2 a[50:51], v16, s[36:37] offset:2048 sc1\n\t"
      "v_mfma_f32_16x16x32_bf16 v[52:55], a[228:231], a[204:207], v[52:55]\n\t"
      "s_waitcnt lgkmcnt(4)\n\t"
      "v_mfma_f32_16x16x32_bf16 v[56:59], a[232:235], a[192:195], v[56:59]\n\t"
      "ds_read_b64_tr_b16 a[228:229], v153 offset:20480\n\t"
      "v_mfma_f32_16x16x32_bf16 v[60:63], a[232:235], a[196:199], v[60:63]\n\t"
      "ds_read_b64_tr_b16 a[230:231], v153 offset:22528\n\t"
      "v_mfma_f32_16x16x32_bf16 v[64:67], a[232:235], a[200:203], v[64:67]\n\t"
      "global_load_dwordx2 a[52:53], v16, s[36:37] offset:512 sc1\n\t"
      "v_mfma_f32_16x16x32_bf16 v[68:71], a[232:235], a[204:207], v[68:71]\n\t"
      "s_waitcnt lgkmcnt(4)\n\t"
      "v_mfma_f32_16x16x32_bf16 v[72:75], a[236:239], a[192:195], v[72:75]\n\t"
      "ds_read_b64_tr_b16 a[232:233], v154 offset:20480\n\t"
      "v_mfma_f32_16x16x32_bf16 v[76:79], a[236:239], a[196:199], v[76:79]\n\t"
      "ds_read_b64_tr_b16 a[234:235], v154 offset:22528\n\t"
      "v_mfma_f32_16x16x32_bf16 v[80:83], a[236:239], a[200:203], v[80:83]\n\t"
      "global_load_dwordx2 a[54:55], v16, s[36:37] offset:2560 sc1\n\t"
      "v_mfma_f32_16x16x32_bf16 v[84:87], a[236:239], a[204:207], v[84:87]\n\t"
      "s_waitcnt vmcnt(25) lgkmcnt(4)\n\t"
      "v_mfma_f32_16x16x32_bf16 v[24:27], a[224:227], a[208:211], v[24:27]\n\t"
      "ds_read_b64_tr_b16 a[236:237], v155 offset:20480\n\t"
      "v_mfma_f32_16x16x32_bf16 v[28:31], a[224:227], a[212:215], v[28:31]\n\t"
      "ds_read_b64_tr_b16 a[238:239], v155 offset:22528\n\t"
      "v_mfma_f32_16x16x32_bf16 v[32:35], a[224:227], a[216:219], v[32:35]\n\t"
      "global_load_dwordx2 a[56:57], v16, s[36:37] offset:1024 sc1\n\t"
      "v_mfma_f32_16x16x32_bf16 v[36:39], a[224:227], a[220:223], v[36:39]\n\t"
      "s_waitcnt lgkmcnt(4)\n\t"
      "v_mfma_f32_16x16x32_bf16 v[40:43], a[228:231], a[208:211], v[40:43]\n\t"
      "ds_read_b64_tr_b16 a[224:225], v152 offset:24576\n\t"
      "v_mfma_f32_16x16x32_bf16 v[44:47], a[228:231], a[212:215], v[44:47]\n\t"
      "ds_read_b64_tr_b16 a[226:227], v152 offset:26624\n\t"
      "v_mfma_f32_16x16x32_bf16 v[48:51], a[228:231], a[216:219], v[48:51]\n\t"
      "global_load_dwordx2 a[58:59], v16, s[36:37] offset:3072 sc1\n\t"
      "v_mfma_f32_16x16x32_bf16 v[52:55], a[228:231], a[220:223], v[52:55]\n\t"
      "s_waitcnt lgkmcnt(4)\n\t"
      "v_mfma_f32_16x16x32_bf16 v[56:59], a[232:235], a[208:211], v[56:59]\n\t"
      "ds_read_b64_tr_b16 a[228:229], v153 offset:24576\n\t"
      "v_mfma_f32_16x16x32_bf16 v[60:63], a[232:235], a[212:215], v[60:63]\n\t"
      "ds_read_b64_tr_b16 a[230:231], v153 offset:26624\n\t"
      "v_mfma_f32_16x16x32_bf16 v[64:67], a[232:235], a[216:219], v[64:67]\n\t"
      "global_load_dwordx2 a[60:61], v16, s[36:37] offset:1536 sc1\n\t"
      "v_mfma_f32_16x16x32_bf16 v[68:71], a[232:235], a[220:223], v[68:71]\n\t"
      "s_waitcnt lgkmcnt(4)\n\t"
      "v_mfma_f32_16x16x32_bf16 v[72:75], a[236:239], a[208:211], v[72:75]\n\t"
      "ds_read_b64_tr_b16 a[232:233], v154 offset:24576\n\t"
      "v_mfma_f32_16x16x32_bf16 v[76:79], a[236:239], a[212:215], v[76:79]\n\t"
      "ds_read_b64_tr_b16 a[234:235], v154 offset:26624\n\t"
      "v_mfma_f32_16x16x32_bf16 v[80:83], a[236:239], a[216:219], v[80:83]\n\t"
      "global_load_dwordx2 a[62:63], v16, s[36:37] offset:3584 sc1\n\t"
      "v_mfma_f32_16x16x32_bf16 v[84:87], a[236:239], a[220:223], v[84:87]\n\t"
      "s_add_u32 s36, s36, 12288\n\t"
      "s_addc_u32 s37, s37, 0\n\t"
      "s_waitcnt lgkmcnt(4)\n\t"
      "v_mfma_f32_16x16x32_bf16 v[88:91], a[224:227], a[128:131], v[88:91]\n\t"
      "ds_read_b64_tr_b16 a[236:237], v155 offset:24576\n\t"
      "v_mfma_f32_16x16x32_bf16 v[92:95], a[224:227], a[132:135], v[92:95]\n\t"
      "ds_read_b64_tr_b16 a[238:239], v155 offset:26624\n\t"
      "v_mfma_f32_16x16x32_bf16 v[96:99], a[224:227], a[136:139], v[96:99]\n\t"
      "v_mfma_f32_16x16x32_bf16 v[100:103], a[224:227], a[140:143], v[100:103]\n\t"
      "s_waitcnt lgkmcnt(4)\n\t"
      "v_mfma_f32_16x16x32_bf16 v[104:107], a[228:231], a[128:131], v[104:107]\n\t"
      "ds_read_b64_tr_b16 a[224:225], v152 offset:28672\n\t"
      "v_mfma_f32_16x16x32_bf16 v[108:111], a[228:231], a[132:135], v[108:111]\n\t"
      "ds_read_b64_tr_b16 a[226:227], v152 offset:30720\n\t"
      "v_mfma_f32_16x16x32_bf16 v[112:115], a[228:231], a[136:139], v[112:115]\n\t"
      "v_mfma_f32_16x16x32_bf16 v[116:119], a[228:231], a[140:143], v[116:119]\n\t"
      "s_waitcnt lgkmcnt(4)\n\t"
      "v_mfma_f32_16x16x32_bf16 v[120:123], a[232:235], a[128:131], v[120:123]\n\t"
      "ds_read_b64_tr_b16 a[228:229], v153 offset:28672\n\t"
      "v_mfma_f32_16x16x32_bf16 v[124:127], a[232:235], a[132:135], v[124:127]\n\t"
      "ds_read_b64_tr_b16 a[230:231], v153 offset:30720\n\t"
      "v_mfma_f32_16x16x32_bf16 v[128:131], a[232:235], a[136:139], v[128:131]\n\t"
      "v_mfma_f32_16x16x32_bf16 v[132:135], a[232:235], a[140:143], v[132:135]\n\t"
      "s_waitcnt lgkmcnt(4)\n\t"
      "v_mfma_f32_16x16x32_bf16 v[136:139], a[236:239], a[128:131], v[136:139]\n\t"
      "ds_read_b64_tr_b16 a[232:233], v154 offset:28672\n\t"
      "v_mfma_f32_16x16x32_bf16 v[140:143], a[236:239], a[132:135], v[140:143]\n\t"
      "ds_read_b64_tr_b16 a[234:235], v154 offset:30720\n\t"
      "v_mfma_f32_16x16x32_bf16 v[144:147], a[236:239], a[136:139], v[144:147]\n\t"
      "v_mfma_f32_16x16x32_bf16 v[148:151], a[236:239], a[140:143], v[148:151]\n\t"
      "s_waitcnt lgkmcnt(4)\n\t"
      "v_mfma_f32_16x16x32_bf16 v[88:91], a[224:227], a[144:147], v[88:91]\n\t"
      "ds_read_b64_tr_b16 a[236:237], v155 offset:28672\n\t"
      "v_mfma_f32_16x16x32_bf16 v[92:95], a[224:227], a[148:151], v[92:95]\n\t"
      "ds_read_b64_tr_b16 a[238:239], v155 offset:30720\n\t"
      "v_mfma_f32_16x16x32_bf16 v[96:99], a[224:227], a[152:155], v[96:99]\n\t"
      "global_load_dwordx4 a[64:67], v1, s[36:37] offset:0 sc1\n\t"
      "v_mfma_f32_16x16x32_bf16 v[100:103], a[224:227], a[156:159], v[100:103]\n\t"
      "s_waitcnt vmcnt(16) lgkmcnt(4)\n\t"
      "s_barrier\n\t"
      "v_mfma_f32_16x16x32_bf16 v[104:107], a[228:231], a[144:147], v[104:107]\n\t"
      "ds_read_b64_tr_b16 a[224:225], v6 offset:0\n\t"
      "v_mfma_f32_16x16x32_bf16 v[108:111], a[228:231], a[148:151], v[108:111]\n\t"
      "ds_read_b64_tr_b16 a[226:227], v6 offset:2048\n\t"
      "s_add_u32 m0, s62, 32768\n\t"
      "v_mfma_f32_16x16x32_bf16 v[112:115], a[228:231], a[152:155], v[112:115]\n\t"
      "global_load_lds_dwordx4 v2, s[40:41]\n\t"
      "v_mfma_f32_16x16x32_bf16 v[116:119], a[228:231], a[156:159], v[116:119]\n\t"
      "s_waitcnt lgkmcnt(4)\n\t"
      "v_mfma_f32_16x16x32_bf16 v[120:123], a[232:235], a[144:147], v[120:123]\n\t"
      "ds_read_b64_tr_b16 a[228:229], v7 offset:0\n\t"
      "v_mfma_f32_16x16x32_bf16 v[124:127], a[232:235], a[148:151], v[124:127]\n\t"
      "ds_read_b64_tr_b16 a[230:231], v7 offset:2048\n\t"
      "s_add_u32 m0, s62, 33792\n\t"
      "v_mfma_f32_16x16x32_bf16 v[128:131], a[232:235], a[152:155], v[128:131]\n\t"
      "global_load_lds_dwordx4 v3, s[40:41]\n\t"
      "v_mfma_f32_16x16x32_bf16 v[132:135], a[232:235], a[156:159], v[132:135]\n\t"
      "s_add_u32 s40, s40, 262144\n\t"
      "s_addc_u32 s41, s41, 0\n\t"
      "s_waitcnt lgkmcnt(4)\n\t"
      "v_mfma_f32_16x16x32_bf16 v[136:139], a[236:239], a[144:147], v[136:139]\n\t"
      "ds_read_b64_tr_b16 a[232:233], v8 offset:0\n\t"
      "v_mfma_f32_16x16x32_bf16 v[140:143], a[236:239], a[148:151], v[140:143]\n\t"
      "ds_read_b64_tr_b16 a[234:235], v8 offset:2048\n\t"
      "s_add_u32 m0, s62, 40960\n\t"
      "v_mfma_f32_16x16x32_bf16 v[144:147], a[236:239], a[152:155], v[144:147]\n\t"
      "global_load_lds_dwordx4 v4, s[42:43]\n\t"
      "v_mfma_f32_16x16x32_bf16 v[148:151], a[236:239], a[156:159], v[148:151]\n\t"
      "s_waitcnt vmcnt(21) lgkmcnt(4)\n\t"
      "v_mfma_f32_16x16x32_bf16 v[24:27], a[224:227], a[0:3], v[24:27]\n\t"
      "ds_read_b64_tr_b16 a[236:237], v9 offset:0\n\t"
      "v_mfma_f32_16x16x32_bf16 v[28:31], a[224:227], a[4:7], v[28:31]\n\t"
      "ds_read_b64_tr_b16 a[238:239], v9 offset:2048\n\t"
      "s_add_u32 m0, s62, 41984\n\t"
      "v_mfma_f32_16x16x32_bf16 v[32:35], a[224:227], a[8:11], v[32:35]\n\t"
      "global_load_lds_dwordx4 v5, s[42:43]\n\t"
      "v_mfma_f32_16x16x32_bf16 v[36:39], a[224:227], a[12:15], v[36:39]\n\t"
      "s_sub_u32 s42, s42, 1572864\n\t"
      "s_subb_u32 s43, s43, 0\n\t"
      "s_waitcnt lgkmcnt(4)\n\t"
      "v_mfma_f32_16x16x32_bf16 v[40:43], a[228:231], a[0:3], v[40:43]\n\t"
      "ds_read_b64_tr_b16 a[224:225], v6 offset:4096\n\t"
      "v_mfma_f32_16x16x32_bf16 v[44:47], a[228:231], a[4:7], v[44:47]\n\t"
      "ds_read_b64_tr_b16 a[226:227], v6 offset:6144\n\t"
      "s_add_u32 m0, s62, 49152\n\t"
      "v_mfma_f32_16x16x32_bf16 v[48:51], a[228:231], a[8:11], v[48:51]\n\t"
      "global_load_lds_dwordx4 v2, s[40:41]\n\t"
      "v_mfma_f32_16x16x32_bf16 v[52:55], a[228:231], a[12:15], v[52:55]\n\t"
      "s_waitcnt lgkmcnt(4)\n\t"
      "v_mfma_f32_16x16x32_bf16 v[56:59], a[232:235], a[0:3], v[56:59]\n\t"
      "ds_read_b64_tr_b16 a[228:229], v7 offset:4096\n\t"
      "v_mfma_f32_16x16x32_bf16 v[60:63], a[232:235], a[4:7], v[60:63]\n\t"
      "ds_read_b64_tr_b16 a[230:231], v7 offset:6144\n\t"
      "s_add_u32 m0, s62, 50176\n\t"
      "v_mfma_f32_16x16x32_bf16 v[64:67], a[232:235], a[8:11], v[64:67]\n\t"
      "global_load_lds_dwordx4 v3, s[40:41]\n\t"
      "v_mfma_f32_16x16x32_bf16 v[68:71], a[232:235], a[12:15], v[68:71]\n\t"
      "s_add_u32 s40, s40, 393216\n\t"
      "s_addc_u32 s41, s41, 0\n\t"
      "s_waitcnt lgkmcnt(4)\n\t"
      "v_mfma_f32_16x16x32_bf16 v[72:75], a[236:239], a[0:3], v[72:75]\n\t"
      "ds_read_b64_tr_b16 a[232:233], v8 offset:4096\n\t"
      "v_mfma_f32_16x16x32_bf16 v[76:79], a[236:239], a[4:7], v[76:79]\n\t"
      "ds_read_b64_tr_b16 a[234:235], v8 offset:6144\n\t"
      "v_mfma_f32_16x16x32_bf16 v[80:83], a[236:239], a[8:11], v[80:83]\n\t"
      "global_load_dwordx4 a[68:71], v1, s[36:37] offset:1024 sc1\n\t"
      "v_mfma_f32_16x16x32_bf16 v[84:87], a[236:239], a[12:15], v[84:87]\n\t"
      "s_waitcnt vmcnt(20) lgkmcnt(4)\n\t"
      "v_mfma_f32_16x16x32_bf16 v[24:27], a[224:227], a[16:19], v[24:27]\n\t"
      "ds_read_b64_tr_b16 a[236:237], v9 offset:4096\n\t"
      "v_mfma_f32_16x16x32_bf16 v[28:31], a[224:227], a[20:23], v[28:31]\n\t"
      "ds_read_b64_tr_b16 a[238:239], v9 offset:6144\n\t"
      "s_add_u32 m0, s62, 57344\n\t"
      "v_mfma_f32_16x16x32_bf16 v[32:35], a[224:227], a[24:27], v[32:35]\n\t"
      "global_load_lds_dwordx4 v2, s[40:41]\n\t"
      "v_mfma_f32_16x16x32_bf16 v[36:39], a[224:227], a[28:31], v[36:39]\n\t"
      "s_waitcnt lgkmcnt(4)\n\t"
      "v_mfma_f32_16x16x32_bf16 v[40:43], a[228:231], a[16:19], v[40:43]\n\t"
      "ds_read_b64_tr_b16 a[224:225], v6 offset:8192\n\t"
      "v_mfma_f32_16x16x32_bf16 v[44:47], a[228:231], a[20:23], v[44:47]\n\t"
      "ds_read_b64_tr_b16 a[226:227], v6 offset:10240\n\t"
      "v_mfma_f32_16x16x32_bf16 v[48:51], a[228:231], a[24:27], v[48:51]\n\t"
      "global_load_dwordx4 a[72:75], v1, s[36:37] offset:2048 sc1\n\t"
      "v_mfma_f32_16x16x32_bf16 v[52:55], a[228:231], a[28:31], v[52:55]\n\t"
      "s_waitcnt lgkmcnt(4)\n\t"
      "v_mfma_f32_16x16x32_bf16 v[56:59], a[232:235], a[16:19], v[56:59]\n\t"
      "ds_read_b64_tr_b16 a[228:229], v7 offset:8192\n\t"
      "v_mfma_f32_16x16x32_bf16 v[60:63], a[232:235], a[20:23], v[60:63]\n\t"
      "ds_read_b64_tr_b16 a[230:231], v7 offset:10240\n\t"
      "v_mfma_f32_16x16x32_bf16 v[64:67], a[232:235], a[24:27], v[64:67]\n\t"
      "global_load_dwordx4 a[76:79], v1, s[36:37] offset:3072 sc1\n\t"
      "v_mfma_f32_16x16x32_bf16 v[68:71], a[232:235], a[28:31], v[68:71]\n\t"
      "s_add_u32 s36, s36, 4096\n\t"
      "s_addc_u32 s37, s37, 0\n\t"
      "s_waitcnt lgkmcnt(4)\n\t"
      "v_mfma_f32_16x16x32_bf16 v[72:75], a[236:239], a[16:19], v[72:75]\n\t"
      "ds_read_b64_tr_b16 a[232:233], v8 offset:8192\n\t"
      "v_mfma_f32_16x16x32_bf16 v[76:79], a[236:239], a[20:23], v[76:79]\n\t"
      "ds_read_b64_tr_b16 a[234:235], v8 offset:10240\n\t"
      "v_mfma_f32_16x16x32_bf16 v[80:83], a[236:239], a[24:27], v[80:83]\n\t"
      "global_load_dwordx4 a[80:83], v1, s[36:37] offset:0 sc1\n\t"
      "v_mfma_f32_16x16x32_bf16 v[84:87], a[236:239], a[28:31], v[84:87]\n\t"
      "s_waitcnt lgkmcnt(4)\n\t"
      "v_mfma_f32_16x16x32_bf16 v[88:91], a[224:227], a[160:163], v[88:91]\n\t"
      "ds_read_b64_tr_b16 a[236:237], v9 offset:8192\n\t"
      "v_mfma_f32_16x16x32_bf16 v[92:95], a[224:227], a[164:167], v[92:95]\n\t"
      "ds_read_b64_tr_b16 a[238:239], v9 offset:10240\n\t"
      "s_add_u32 m0, s62, 58368\n\t"
      "v_mfma_f32_16x16x32_bf16 v[96:99], a[224:227], a[168:171], v[96:99]\n\t"
      "global_load_lds_dwordx4 v3, s[40:41]\n\t"
      "v_mfma_f32_16x16x32_bf16 v[100:103], a[224:227], a[172:175], v[100:103]\n\t"
      "s_add_u32 s40, s40, 262144\n\t"
      "s_addc_u32 s41, s41, 0\n\t"
      "s_waitcnt lgkmcnt(4)\n\t"
      "v_mfma_f32_16x16x32_bf16 v[104:107], a[228:231], a[160:163], v[104:107]\n\t"
      "ds_read_b64_tr_b16 a[224:225], v6 offset:12288\n\t"
      "v_mfma_f32_16x16x32_bf16 v[108:111], a[228:231], a[164:167], v[108:111]\n\t"
      "ds_read_b64_tr_b16 a[226:227], v6 offset:14336\n\t"
      "v_mfma_f32_16x16x32_bf16 v[112:115], a[228:231], a[168:171], v[112:115]\n\t"
      "global_load_dwordx4 a[84:87], v1, s[36:37] offset:1024 sc1\n\t"
      "v_mfma_f32_16x16x32_bf16 v[116:119], a[228:231], a[172:175], v[116:119]\n\t"
      "s_waitcnt lgkmcnt(4)\n\t"
      "v_mfma_f32_16x16x32_bf16 v[120:123], a[232:235], a[160:163], v[120:123]\n\t"
      "ds_read_b64_tr_b16 a[228:229], v7 offset:12288\n\t"
      "v_mfma_f32_16x16x32_bf16 v[124:127], a[232:235], a[164:167], v[124:127]\n\t"
      "ds_read_b64_tr_b16 a[230:231], v7 offset:14336\n\t"
      "v_mfma_f32_16x16x32_bf16 v[128:131], a[232:235], a[168:171], v[128:131]\n\t"
      "global_load_dwordx4 a[88:91], v1, s[36:37] offset:2048 sc1\n\t"
      "v_mfma_f32_16x16x32_bf16 v[132:135], a[232:235], a[172:175], v[132:135]\n\t"
      "s_waitcnt lgkmcnt(4)\n\t"
      "v_mfma_f32_16x16x32_bf16 v[136:139], a[236:239], a[160:163], v[136:139]\n\t"
      "ds_read_b64_tr_b16 a[232:233], v8 offset:12288\n\t"
      "v_mfma_f32_16x16x32_bf16 v[140:143], a[236:239], a[164:167], v[140:143]\n\t"
      "ds_read_b64_tr_b16 a[234:235], v8 offset:14336\n\t"
      "v_mfma_f32_16x16x32_bf16 v[144:147], a[236:239], a[168:171], v[144:147]\n\t"
      "global_load_dwordx4 a[92:95], v1, s[36:37] offset:3072 sc1\n\t"
      "v_mfma_f32_16x16x32_bf16 v[148:151], a[236:239], a[172:175], v[148:151]\n\t"
      "s_add_u32 s36, s36, 12288\n\t"
      "s_addc_u32 s37, s37, 0\n\t"
      "s_waitcnt lgkmcnt(4)\n\t"
      "v_mfma_f32_16x16x32_bf16 v[88:91], a[224:227], a[176:179], v[88:91]\n\t"
      "ds_read_b64_tr_b16 a[236:237], v9 offset:12288\n\t"
      "v_mfma_f32_16x16x32_bf16 v[92:95], a[224:227], a[180:183], v[92:95]\n\t"
      "ds_read_b64_tr_b16 a[238:239], v9 offset:14336\n\t"
      "v_mfma_f32_16x16x32_bf16 v[96:99], a[224:227], a[184:187], v[96:99]\n\t"
      "global_load_dwordx2 a[96:97], v16, s[36:37] offset:0 sc1\n\t"
      "v_mfma_f32_16x16x32_bf16 v[100:103], a[224:227], a[188:191], v[100:103]\n\t"
      "s_waitcnt lgkmcnt(4)\n\t"
      "v_mfma_f32_16x16x32_bf16 v[104:107], a[228:231], a[176:179], v[104:107]\n\t"
      "ds_read_b64_tr_b16 a[224:225], v6 offset:16384\n\t"
      "v_mfma_f32_16x16x32_bf16 v[108:111], a[228:231], a[180:183], v[108:111]\n\t"
      "ds_read_b64_tr_b16 a[226:227], v6 offset:18432\n\t"
      "v_mfma_f32_16x16x32_bf16 v[112:115], a[228:231], a[184:187], v[112:115]\n\t"
      "global_load_dwordx2 a[98:99], v16, s[36:37] offset:2048 sc1\n\t"
      "v_mfma_f32_16x16x32_bf16 v[116:119], a[228:231], a[188:191], v[116:119]\n\t"
      "s_waitcnt lgkmcnt(4)\n\t"
      "v_mfma_f32_16x16x32_bf16 v[120:123], a[232:235], a[176:179], v[120:123]\n\t"
      "ds_read_b64_tr_b16 a[228:229], v7 offset:16384\n\t"
      "v_mfma_f32_16x16x32_bf16 v[124:127], a[232:235], a[180:183], v[124:127]\n\t"
      "ds_read_b64_tr_b16 a[230:231], v7 offset:18432\n\t"
      "v_mfma_f32_16x16x32_bf16 v[128:131], a[232:235], a[184:187], v[128:131]\n\t"
      "global_load_dwordx2 a[100:101], v16, s[36:37] offset:512 sc1\n\t"
      "v_mfma_f32_16x16x32_bf16 v[132:135], a[232:235], a[188:191], v[132:135]\n\t"
      "s_waitcnt lgkmcnt(4)\n\t"
      "v_mfma_f32_16x16x32_bf16 v[136:139], a[236:239], a[176:179], v[136:139]\n\t"
      "ds_read_b64_tr_b16 a[232:233], v8 offset:16384\n\t"
      "v_mfma_f32_16x16x32_bf16 v[140:143], a[236:239], a[180:183], v[140:143]\n\t"
      "ds_read_b64_tr_b16 a[234:235], v8 offset:18432\n\t"
      "v_mfma_f32_16x16x32_bf16 v[144:147], a[236:239], a[184:187], v[144:147]\n\t"
      "global_load_dwordx2 a[102:103], v16, s[36:37] offset:2560 sc1\n\t"
      "v_mfma_f32_16x16x32_bf16 v[148:151], a[236:239], a[188:191], v[148:151]\n\t"
      "s_waitcnt vmcnt(28) lgkmcnt(4)\n\t"
      "v_mfma_f32_16x16x32_bf16 v[24:27], a[224:227], a[32:35], v[24:27]\n\t"
      "ds_read_b64_tr_b16 a[236:237], v9 offset:16384\n\t"
      "v_mfma_f32_16x16x32_bf16 v[28:31], a[224:227], a[36:39], v[28:31]\n\t"
      "ds_read_b64_tr_b16 a[238:239], v9 offset:18432\n\t"
      "v_mfma_f32_16x16x32_bf16 v[32:35], a[224:227], a[40:43], v[32:35]\n\t"
      "global_load_dwordx2 a[104:105], v16, s[36:37] offset:1024 sc1\n\t"
      "v_mfma_f32_16x16x32_bf16 v[36:39], a[224:227], a[44:47], v[36:39]\n\t"
      "s_waitcnt lgkmcnt(4)\n\t"
      "v_mfma_f32_16x16x32_bf16 v[40:43], a[228:231], a[32:35], v[40:43]\n\t"
      "ds_read_b64_tr_b16 a[224:225], v6 offset:20480\n\t"
      "v_mfma_f32_16x16x32_bf16 v[44:47], a[228:231], a[36:39], v[44:47]\n\t"
      "ds_read_b64_tr_b16 a[226:227], v6 offset:22528\n\t"
      "v_mfma_f32_16x16x32_bf16 v[48:51], a[228:231], a[40:43], v[48:51]\n\t"
      "global_load_dwordx2 a[106:107], v16, s[36:37] offset:3072 sc1\n\t"
      "v_mfma_f32_16x16x32_bf16 v[52:55], a[228:231], a[44:47], v[52:55]\n\t"
      "s_waitcnt lgkmcnt(4)\n\t"
      "v_mfma_f32_16x16x32_bf16 v[56:59], a[232:235], a[32:35], v[56:59]\n\t"
      "ds_read_b64_tr_b16 a[228:229], v7 offset:20480\n\t"
      "v_mfma_f32_16x16x32_bf16 v[60:63], a[232:235], a[36:39], v[60:63]\n\t"
      "ds_read_b64_tr_b16 a[230:231], v7 offset:22528\n\t"
      "v_mfma_f32_16x16x32_bf16 v[64:67], a[232:235], a[40:43], v[64:67]\n\t"
      "global_load_dwordx2 a[108:109], v16, s[36:37] offset:1536 sc1\n\t"
      "v_mfma_f32_16x16x32_bf16 v[68:71], a[232:235], a[44:47], v[68:71]\n\t"
      "s_waitcnt lgkmcnt(4)\n\t"
      "v_mfma_f32_16x16x32_bf16 v[72:75], a[236:239], a[32:35], v[72:75]\n\t"
      "ds_read_b64_tr_b16 a[232:233], v8 offset:20480\n\t"
      "v_mfma_f32_16x16x32_bf16 v[76:79], a[236:239], a[36:39], v[76:79]\n\t"
      "ds_read_b64_tr_b16 a[234:235], v8 offset:22528\n\t"
      "v_mfma_f32_16x16x32_bf16 v[80:83], a[236:239], a[40:43], v[80:83]\n\t"
      "global_load_dwordx2 a[110:111], v16, s[36:37] offset:3584 sc1\n\t"
      "v_mfma_f32_16x16x32_bf16 v[84:87], a[236:239], a[44:47], v[84:87]\n\t"
      "s_add_u32 s36, s36, 4096\n\t"
      "s_addc_u32 s37, s37, 0\n\t"
      "s_waitcnt vmcnt(24) lgkmcnt(4)\n\t"
      "v_mfma_f32_16x16x32_bf16 v[24:27], a[224:227], a[48:51], v[24:27]\n\t"
      "ds_read_b64_tr_b16 a[236:237], v9 offset:20480\n\t"
      "v_mfma_f32_16x16x32_bf16 v[28:31], a[224:227], a[52:55], v[28:31]\n\t"
      "ds_read_b64_tr_b16 a[238:239], v9 offset:22528\n\t"
      "v_mfma_f32_16x16x32_bf16 v[32:35], a[224:227], a[56:59], v[32:35]\n\t"
      "global_load_dwordx4 a[112:115], v1, s[36:37] offset:0 sc1\n\t"
      "v_mfma_f32_16x16x32_bf16 v[36:39], a[224:227], a[60:63], v[36:39]\n\t"
      "s_waitcnt lgkmcnt(4)\n\t"
      "v_mfma_f32_16x16x32_bf16 v[40:43], a[228:231], a[48:51], v[40:43]\n\t"
      "ds_read_b64_tr_b16 a[224:225], v6 offset:24576\n\t"
      "v_mfma_f32_16x16x32_bf16 v[44:47], a[228:231], a[52:55], v[44:47]\n\t"
      "ds_read_b64_tr_b16 a[226:227], v6 offset:26624\n\t"
      "v_mfma_f32_16x16x32_bf16 v[48:51], a[228:231], a[56:59], v[48:51]\n\t"
      "global_load_dwordx4 a[116:119], v1, s[36:37] offset:1024 sc1\n\t"
      "v_mfma_f32_16x16x32_bf16 v[52:55], a[228:231], a[60:63], v[52:55]\n\t"
      "s_waitcnt lgkmcnt(4)\n\t"
      "v_mfma_f32_16x16x32_bf16 v[56:59], a[232:235], a[48:51], v[56:59]\n\t"
      "ds_read_b64_tr_b16 a[228:229], v7 offset:24576\n\t"
      "v_mfma_f32_16x16x32_bf16 v[60:63], a[232:235], a[52:55], v[60:63]\n\t"
      "ds_read_b64_tr_b16 a[230:231], v7 offset:26624\n\t"
      "v_mfma_f32_16x16x32_bf16 v[64:67], a[232:235], a[56:59], v[64:67]\n\t"
      "global_load_dwordx4 a[120:123], v1, s[36:37] offset:2048 sc1\n\t"
      "v_mfma_f32_16x16x32_bf16 v[68:71], a[232:235], a[60:63], v[68:71]\n\t"
      "s_waitcnt lgkmcnt(4)\n\t"
      "v_mfma_f32_16x16x32_bf16 v[72:75], a[236:239], a[48:51], v[72:75]\n\t"
      "ds_read_b64_tr_b16 a[232:233], v8 offset:24576\n\t"
      "v_mfma_f32_16x16x32_bf16 v[76:79], a[236:239], a[52:55], v[76:79]\n\t"
      "ds_read_b64_tr_b16 a[234:235], v8 offset:26624\n\t"
      "v_mfma_f32_16x16x32_bf16 v[80:83], a[236:239], a[56:59], v[80:83]\n\t"
      "global_load_dwordx4 a[124:127], v1, s[36:37] offset:3072 sc1\n\t"
      "v_mfma_f32_16x16x32_bf16 v[84:87], a[236:239], a[60:63], v[84:87]\n\t"
      "s_add_u32 s36, s36, 20480\n\t"
      "s_addc_u32 s37, s37, 0\n\t"
      "s_waitcnt lgkmcnt(4)\n\t"
      "v_mfma_f32_16x16x32_bf16 v[88:91], a[224:227], a[192:195], v[88:91]\n\t"
      "ds_read_b64_tr_b16 a[236:237], v9 offset:24576\n\t"
      "v_mfma_f32_16x16x32_bf16 v[92:95], a[224:227], a[196:199], v[92:95]\n\t"
      "ds_read_b64_tr_b16 a[238:239], v9 offset:26624\n\t"
      "v_mfma_f32_16x16x32_bf16 v[96:99], a[224:227], a[200:203], v[96:99]\n\t"
      "global_load_dwordx4 a[128:131], v1, s[36:37] offset:0 sc1\n\t"
      "v_mfma_f32_16x16x32_bf16 v[100:103], a[224:227], a[204:207], v[100:103]\n\t"
      "s_waitcnt lgkmcnt(4)\n\t"
      "v_mfma_f32_16x16x32_bf16 v[104:107], a[228:231], a[192:195], v[104:107]\n\t"
      "ds_read_b64_tr_b16 a[224:225], v6 offset:28672\n\t"
      "v_mfma_f32_16x16x32_bf16 v[108:111], a[228:231], a[196:199], v[108:111]\n\t"
      "ds_read_b64_tr_b16 a[226:227], v6 offset:30720\n\t"
      "v_mfma_f32_16x16x32_bf16 v[112:115], a[228:231], a[200:203], v[112:115]\n\t"
      "global_load_dwordx4 a[132:135], v1, s[36:37] offset:1024 sc1\n\t"
      "v_mfma_f32_16x16x32_bf16 v[116:119], a[228:231], a[204:207], v[116:119]\n\t"
      "s_waitcnt lgkmcnt(4)\n\t"
      "v_mfma_f32_16x16x32_bf16 v[120:123], a[232:235], a[192:195], v[120:123]\n\t"
      "ds_read_b64_tr_b16 a[228:229], v7 offset:28672\n\t"
      "v_mfma_f32_16x16x32_bf16 v[124:127], a[232:235], a[196:199], v[124:127]\n\t"
      "ds_read_b64_tr_b16 a[230:231], v7 offset:30720\n\t"
      "v_mfma_f32_16x16x32_bf16 v[128:131], a[232:235], a[200:203], v[128:131]\n\t"
      "global_load_dwordx4 a[136:139], v1, s[36:37] offset:2048 sc1\n\t"
      "v_mfma_f32_16x16x32_bf16 v[132:135], a[232:235], a[204:207], v[132:135]\n\t"
      "s_waitcnt lgkmcnt(4)\n\t"
      "v_mfma_f32_16x16x32_bf16 v[136:139], a[236:239], a[192:195], v[136:139]\n\t"
      "ds_read_b64_tr_b16 a[232:233], v8 offset:28672\n\t"
      "v_mfma_f32_16x16x32_bf16 v[140:143], a[236:239], a[196:199], v[140:143]\n\t"
      "ds_read_b64_tr_b16 a[234:235], v8 offset:30720\n\t"
      "v_mfma_f32_16x16x32_bf16 v[144:147], a[236:239], a[200:203], v[144:147]\n\t"
      "global_load_dwordx4 a[140:143], v1, s[36:37] offset:3072 sc1\n\t"
      "v_mfma_f32_16x16x32_bf16 v[148:151], a[236:239], a[204:207], v[148:151]\n\t"
      "s_add_u32 s36, s36, 4096\n\t"
      "s_addc_u32 s37, s37, 0\n\t"
      "s_waitcnt lgkmcnt(4)\n\t"
      "v_mfma_f32_16x16x32_bf16 v[88:91], a[224:227], a[208:211], v[88:91]\n\t"
      "ds_read_b64_tr_b16 a[236:237], v9 offset:28672\n\t"
      "v_mfma_f32_16x16x32_bf16 v[92:95], a[224:227], a[212:215], v[92:95]\n\t"
      "ds_read_b64_tr_b16 a[238:239], v9 offset:30720\n\t"
      "v_mfma_f32_16x16x32_bf16 v[96:99], a[224:227], a[216:219], v[96:99]\n\t"
      "global_load_dwordx2 a[144:145], v16, s[36:37] offset:0 sc1\n\t"
      "v_mfma_f32_16x16x32_bf16 v[100:103], a[224:227], a[220:223], v[100:103]\n\t"
      "s_waitcnt vmcnt(20) lgkmcnt(4)\n\t"
      "s_barrier\n\t"
      "v_mfma_f32_16x16x32_bf16 v[104:107], a[228:231], a[208:211], v[104:107]\n\t"
      "ds_read_b64_tr_b16 a[224:225], v6 offset:32768\n\t"
      "v_mfma_f32_16x16x32_bf16 v[108:111], a[228:231], a[212:215], v[108:111]\n\t"
      "ds_read_b64_tr_b16 a[226:227], v6 offset:34816\n\t"
      "s_add_u32 m0, s62, 65536\n\t"
      "v_mfma_f32_16x16x32_bf16 v[112:115], a[228:231], a[216:219], v[112:115]\n\t"
      "global_load_lds_dwordx4 v2, s[40:41]\n\t"
      "v_mfma_f32_16x16x32_bf16 v[116:119], a[228:231], a[220:223], v[116:119]\n\t"
      "s_waitcnt lgkmcnt(4)\n\t"
      "v_mfma_f32_16x16x32_bf16 v[120:123], a[232:235], a[208:211], v[120:123]\n\t"
      "ds_read_b64_tr_b16 a[228:229], v7 offset:32768\n\t"
      "v_mfma_f32_16x16x32_bf16 v[124:127], a[232:235], a[212:215], v[124:127]\n\t"
      "ds_read_b64_tr_b16 a[230:231], v7 offset:34816\n\t"
      "s_add_u32 m0, s62, 66560\n\t"
      "v_mfma_f32_16x16x32_bf16 v[128:131], a[232:235], a[216:219], v[128:131]\n\t"
      "global_load_lds_dwordx4 v3, s[40:41]\n\t"
      "v_mfma_f32_16x16x32_bf16 v[132:135], a[232:235], a[220:223], v[132:135]\n\t"
      "s_add_u32 s40, s40, 262144\n\t"
      "s_addc_u32 s41, s41, 0\n\t"
      "s_waitcnt lgkmcnt(4)\n\t"
      "v_mfma_f32_16x16x32_bf16 v[136:139], a[236:239], a[208:211], v[136:139]\n\t"
      "ds_read_b64_tr_b16 a[232:233], v8 offset:32768\n\t"
      "v_mfma_f32_16x16x32_bf16 v[140:143], a[236:239], a[212:215], v[140:143]\n\t"
      "ds_read_b64_tr_b16 a[234:235], v8 offset:34816\n\t"
      "s_add_u32 m0, s62, 73728\n\t"
      "v_mfma_f32_16x16x32_bf16 v[144:147], a[236:239], a[216:219], v[144:147]\n\t"
      "global_load_lds_dwordx4 v4, s[42:43]\n\t"
      "v_mfma_f32_16x16x32_bf16 v[148:151], a[236:239], a[220:223], v[148:151]\n\t"
      "s_waitcnt vmcnt(25) lgkmcnt(4)\n\t"
      "v_mfma_f32_16x16x32_bf16 v[24:27], a[224:227], a[64:67], v[24:27]\n\t"
      "ds_read_b64_tr_b16 a[236:237], v9 offset:32768\n\t"
      "v_mfma_f32_16x16x32_bf16 v[28:31], a[224:227], a[68:71], v[28:31]\n\t"
      "ds_read_b64_tr_b16 a[238:239], v9 offset:34816\n\t"
      "s_add_u32 m0, s62, 74752\n\t"
      "v_mfma_f32_16x16x32_bf16 v[32:35], a[224:227], a[72:75], v[32:35]\n\t"
      "global_load_lds_dwordx4 v5, s[42:43]\n\t"
      "v_mfma_f32_16x16x32_bf16 v[36:39], a[224:227], a[76:79], v[36:39]\n\t"
      "s_add_u32 s42, s42, 262144\n\t"
      "s_addc_u32 s43, s43, 0\n\t"
      "s_waitcnt lgkmcnt(4)\n\t"
      "v_mfma_f32_16x16x32_bf16 v[40:43], a[228:231], a[64:67], v[40:43]\n\t"
      "ds_read_b64_tr_b16 a[224:225], v6 offset:36864\n\t"
      "v_mfma_f32_16x16x32_bf16 v[44:47], a[228:231], a[68:71], v[44:47]\n\t"
      "ds_read_b64_tr_b16 a[226:227], v6 offset:38912\n\t"
      "s_add_u32 m0, s62, 81920\n\t"
      "v_mfma_f32_16x16x32_bf16 v[48:51], a[228:231], a[72:75], v[48:51]\n\t"
      "global_load_lds_dwordx4 v2, s[40:41]\n\t"
      "v_mfma_f32_16x16x32_bf16 v[52:55], a[228:231], a[76:79], v[52:55]\n\t"
      "s_waitcnt lgkmcnt(4)\n\t"
      "v_mfma_f32_16x16x32_bf16 v[56:59], a[232:235], a[64:67], v[56:59]\n\t"
      "ds_read_b64_tr_b16 a[228:229], v7 offset:36864\n\t"
      "v_mfma_f32_16x16x32_bf16 v[60:63], a[232:235], a[68:71], v[60:63]\n\t"
      "ds_read_b64_tr_b16 a[230:231], v7 offset:38912\n\t"
      "v_mfma_f32_16x16x32_bf16 v[64:67], a[232:235], a[72:75], v[64:67]\n\t"
      "global_load_dwordx2 a[146:147], v16, s[36:37] offset:2048 sc1\n\t"
      "v_mfma_f32_16x16x32_bf16 v[68:71], a[232:235], a[76:79], v[68:71]\n\t"
      "s_waitcnt lgkmcnt(4)\n\t"
      "v_mfma_f32_16x16x32_bf16 v[72:75], a[236:239], a[64:67], v[72:75]\n\t"
      "ds_read_b64_tr_b16 a[232:233], v8 offset:36864\n\t"
      "v_mfma_f32_16x16x32_bf16 v[76:79], a[236:239], a[68:71], v[76:79]\n\t"
      "ds_read_b64_tr_b16 a[234:235], v8 offset:38912\n\t"
      "v_mfma_f32_16x16x32_bf16 v[80:83], a[236:239], a[72:75], v[80:83]\n\t"
      "global_load_dwordx2 a[148:149], v16, s[36:37] offset:512 sc1\n\t"
      "v_mfma_f32_16x16x32_bf16 v[84:87], a[236:239], a[76:79], v[84:87]\n\t"
      "s_waitcnt vmcnt(24) lgkmcnt(4)\n\t"
      "v_mfma_f32_16x16x32_bf16 v[24:27], a[224:227], a[80:83], v[24:27]\n\t"
      "ds_read_b64_tr_b16 a[236:237], v9 offset:36864\n\t"
      "v_mfma_f32_16x16x32_bf16 v[28:31], a[224:227], a[84:87], v[28:31]\n\t"
      "ds_read_b64_tr_b16 a[238:239], v9 offset:38912\n\t"
      "s_add_u32 m0, s62, 82944\n\t"
      "v_mfma_f32_16x16x32_bf16 v[32:35], a[224:227], a[88:91], v[32:35]\n\t"
      "global_load_lds_dwordx4 v3, s[40:41]\n\t"
      "v_mfma_f32_16x16x32_bf16 v[36:39], a[224:227], a[92:95], v[36:39]\n\t"
      "s_add_u32 s40, s40, 262144\n\t"
      "s_addc_u32 s41, s41, 0\n\t"
      "s_waitcnt lgkmcnt(4)\n\t"
      "v_mfma_f32_16x16x32_bf16 v[40:43], a[228:231], a[80:83], v[40:43]\n\t"
      "ds_read_b64_tr_b16 a[224:225], v6 offset:40960\n\t"
      "v_mfma_f32_16x16x32_bf16 v[44:47], a[228:231], a[84:87], v[44:47]\n\t"
      "ds_read_b64_tr_b16 a[226:227], v6 offset:43008\n\t"
      "v_mfma_f32_16x16x32_bf16 v[48:51], a[228:231], a[88:91], v[48:51]\n\t"
      "global_load_dwordx2 a[150:151], v16, s[36:37] offset:2560 sc1\n\t"
      "v_mfma_f32_16x16x32_bf16 v[52:55], a[228:231], a[92:95], v[52:55]\n\t"
      "s_waitcnt lgkmcnt(4)\n\t"
      "v_mfma_f32_16x16x32_bf16 v[56:59], a[232:235], a[80:83], v[56:59]\n\t"
      "ds_read_b64_tr_b16 a[228:229], v7 offset:40960\n\t"
      "v_mfma_f32_16x16x32_bf16 v[60:63], a[232:235], a[84:87], v[60:63]\n\t"
      "ds_read_b64_tr_b16 a[230:231], v7 offset:43008\n\t"
      "v_mfma_f32_16x16x32_bf16 v[64:67], a[232:235], a[88:91], v[64:67]\n\t"
      "global_load_dwordx2 a[152:153], v16, s[36:37] offset:1024 sc1\n\t"
      "v_mfma_f32_16x16x32_bf16 v[68:71], a[232:235], a[92:95], v[68:71]\n\t"
      "s_waitcnt lgkmcnt(4)\n\t"
      "v_mfma_f32_16x16x32_bf16 v[72:75], a[236:239], a[80:83], v[72:75]\n\t"
      "ds_read_b64_tr_b16 a[232:233], v8 offset:40960\n\t"
      "v_mfma_f32_16x16x32_bf16 v[76:79], a[236:239], a[84:87], v[76:79]\n\t"
      "ds_read_b64_tr_b16 a[234:235], v8 offset:43008\n\t"
      "v_mfma_f32_16x16x32_bf16 v[80:83], a[236:239], a[88:91], v[80:83]\n\t"
      "global_load_dwordx2 a[154:155], v16, s[36:37] offset:3072 sc1\n\t"
      "v_mfma_f32_16x16x32_bf16 v[84:87], a[236:239], a[92:95], v[84:87]\n\t"
      "s_waitcnt lgkmcnt(4)\n\t"
      "v_mfma_f32_16x16x32_bf16 v[88:91], a[224:227], a[0:3], v[88:91]\n\t"
      "ds_read_b64_tr_b16 a[236:237], v9 offset:40960\n\t"
      "v_mfma_f32_16x16x32_bf16 v[92:95], a[224:227], a[4:7], v[92:95]\n\t"
      "ds_read_b64_tr_b16 a[238:239], v9 offset:43008\n\t"
      "s_add_u32 m0, s62, 90112\n\t"
      "v_mfma_f32_16x16x32_bf16 v[96:99], a[224:227], a[8:11], v[96:99]\n\t"
      "global_load_lds_dwordx4 v4, s[42:43]\n\t"
      "v_mfma_f32_16x16x32_bf16 v[100:103], a[224:227], a[12:15], v[100:103]\n\t"
      "s_waitcnt lgkmcnt(4)\n\t"
      "v_mfma_f32_16x16x32_bf16 v[104:107], a[228:231], a[0:3], v[104:107]\n\t"
      "ds_read_b64_tr_b16 a[224:225], v6 offset:45056\n\t"
      "v_mfma_f32_16x16x32_bf16 v[108:111], a[228:231], a[4:7], v[108:111]\n\t"
      "ds_read_b64_tr_b16 a[226:227], v6 offset:47104\n\t"
      "v_mfma_f32_16x16x32_bf16 v[112:115], a[228:231], a[8:11], v[112:115]\n\t"
      "global_load_dwordx2 a[156:157], v16, s[36:37] offset:1536 sc1\n\t"
      "v_mfma_f32_16x16x32_bf16 v[116:119], a[228:231], a[12:15], v[116:119]\n\t"
      "s_waitcnt lgkmcnt(4)\n\t"
      "v_mfma_f32_16x16x32_bf16 v[120:123], a[232:235], a[0:3], v[120:123]\n\t"
      "ds_read_b64_tr_b16 a[228:229], v7 offset:45056\n\t"
      "v_mfma_f32_16x16x32_bf16 v[124:127], a[232:235], a[4:7], v[124:127]\n\t"
      "ds_read_b64_tr_b16 a[230:231], v7 offset:47104\n\t"
      "v_mfma_f32_16x16x32_bf16 v[128:131], a[232:235], a[8:11], v[128:131]\n\t"
      "global_load_dwordx2 a[158:159], v16, s[36:37] offset:3584 sc1\n\t"
      "v_mfma_f32_16x16x32_bf16 v[132:135], a[232:235], a[12:15], v[132:135]\n\t"
      "s_add_u32 s36, s36, 12288\n\t"
      "s_addc_u32 s37, s37, 0\n\t"
      "s_waitcnt lgkmcnt(4)\n\t"
      "v_mfma_f32_16x16x32_bf16 v[136:139], a[236:239], a[0:3], v[136:139]\n\t"
      "ds_read_b64_tr_b16 a[232:233], v8 offset:45056\n\t"
      "v_mfma_f32_16x16x32_bf16 v[140:143], a[236:239], a[4:7], v[140:143]\n\t"
      "ds_read_b64_tr_b16 a[234:235], v8 offset:47104\n\t"
      "v_mfma_f32_16x16x32_bf16 v[144:147], a[236:239], a[8:11], v[144:147]\n\t"
      "global_load_dwordx4 a[160:163], v1, s[36:37] offset:0 sc1\n\t"
      "v_mfma_f32_16x16x32_bf16 v[148:151], a[236:239], a[12:15], v[148:151]\n\t"
      "s_waitcnt lgkmcnt(4)\n\t"
      "v_mfma_f32_16x16x32_bf16 v[88:91], a[224:227], a[16:19], v[88:91]\n\t"
      "ds_read_b64_tr_b16 a[236:237], v9 offset:45056\n\t"
      "v_mfma_f32_16x16x32_bf16 v[92:95], a[224:227], a[20:23], v[92:95]\n\t"
      "ds_read_b64_tr_b16 a[238:239], v9 offset:47104\n\t"
      "v_mfma_f32_16x16x32_bf16 v[96:99], a[224:227], a[24:27], v[96:99]\n\t"
      "global_load_dwordx4 a[164:167], v1, s[36:37] offset:1024 sc1\n\t"
      "v_mfma_f32_16x16x32_bf16 v[100:103], a[224:227], a[28:31], v[100:103]\n\t"
      "s_waitcnt lgkmcnt(4)\n\t"
      "v_mfma_f32_16x16x32_bf16 v[104:107], a[228:231], a[16:19], v[104:107]\n\t"
      "ds_read_b64_tr_b16 a[224:225], v6 offset:49152\n\t"
      "v_mfma_f32_16x16x32_bf16 v[108:111], a[228:231], a[20:23], v[108:111]\n\t"
      "ds_read_b64_tr_b16 a[226:227], v6 offset:51200\n\t"
      "v_mfma_f32_16x16x32_bf16 v[112:115], a[228:231], a[24:27], v[112:115]\n\t"
      "global_load_dwordx4 a[168:171], v1, s[36:37] offset:2048 sc1\n\t"
      "v_mfma_f32_16x16x32_bf16 v[116:119], a[228:231], a[28:31], v[116:119]\n\t"
      "s_waitcnt lgkmcnt(4)\n\t"
      "v_mfma_f32_16x16x32_bf16 v[120:123], a[232:235], a[16:19], v[120:123]\n\t"
      "ds_read_b64_tr_b16 a[228:229], v7 offset:49152\n\t"
      "v_mfma_f32_16x16x32_bf16 v[124:127], a[232:235], a[20:23], v[124:127]\n\t"
      "ds_read_b64_tr_b16 a[230:231], v7 offset:51200\n\t"
      "v_mfma_f32_16x16x32_bf16 v[128:131], a[232:235], a[24:27], v[128:131]\n\t"
      "global_load_dwordx4 a[172:175], v1, s[36:37] offset:3072 sc1\n\t"
      "v_mfma_f32_16x16x32_bf16 v[132:135], a[232:235], a[28:31], v[132:135]\n\t"
      "s_add_u32 s36, s36, 4096\n\t"
      "s_addc_u32 s37, s37, 0\n\t"
      "s_waitcnt lgkmcnt(4)\n\t"
      "v_mfma_f32_16x16x32_bf16 v[136:139], a[236:239], a[16:19], v[136:139]\n\t"
      "ds_read_b64_tr_b16 a[232:233], v8 offset:49152\n\t"
      "v_mfma_f32_16x16x32_bf16 v[140:143], a[236:239], a[20:23], v[140:143]\n\t"
      "ds_read_b64_tr_b16 a[234:235], v8 offset:51200\n\t"
      "v_mfma_f32_16x16x32_bf16 v[144:147], a[236:239], a[24:27], v[144:147]\n\t"
      "global_load_dwordx4 a[176:179], v1, s[36:37] offset:0 sc1\n\t"
      "v_mfma_f32_16x16x32_bf16 v[148:151], a[236:239], a[28:31], v[148:151]\n\t"
      "s_waitcnt vmcnt(28) lgkmcnt(4)\n\t"
      "v_mfma_f32_16x16x32_bf16 v[24:27], a[224:227], a[96:99], v[24:27]\n\t"
      "ds_read_b64_tr_b16 a[236:237], v9 offset:49152\n\t"
      "v_mfma_f32_16x16x32_bf16 v[28:31], a[224:227], a[100:103], v[28:31]\n\t"
      "ds_read_b64_tr_b16 a[238:239], v9 offset:51200\n\t"
      "s_add_u32 m0, s62, 91136\n\t"
      "v_mfma_f32_16x16x32_bf16 v[32:35], a[224:227], a[104:107], v[32:35]\n\t"
      "global_load_lds_dwordx4 v5, s[42:43]\n\t"
      "v_mfma_f32_16x16x32_bf16 v[36:39], a[224:227], a[108:111], v[36:39]\n\t"
      "s_add_u32 s42, s42, 262144\n\t"
      "s_addc_u32 s43, s43, 0\n\t"
      "s_waitcnt lgkmcnt(4)\n\t"
      "v_mfma_f32_16x16x32_bf16 v[40:43], a[228:231], a[96:99], v[40:43]\n\t"
      "ds_read_b64_tr_b16 a[224:225], v6 offset:53248\n\t"
      "v_mfma_f32_16x16x32_bf16 v[44:47], a[228:231], a[100:103], v[44:47]\n\t"
      "ds_read_b64_tr_b16 a[226:227], v6 offset:55296\n\t"
      "v_mfma_f32_16x16x32_bf16 v[48:51], a[228:231], a[104:107], v[48:51]\n\t"
      "global_load_dwordx4 a[180:183], v1, s[36:37] offset:1024 sc1\n\t"
      "v_mfma_f32_16x16x32_bf16 v[52:55], a[228:231], a[108:111], v[52:55]\n\t"
      "s_waitcnt lgkmcnt(4)\n\t"
      "v_mfma_f32_16x16x32_bf16 v[56:59], a[232:235], a[96:99], v[56:59]\n\t"
      "ds_read_b64_tr_b16 a[228:229], v7 offset:53248\n\t"
      "v_mfma_f32_16x16x32_bf16 v[60:63], a[232:235], a[100:103], v[60:63]\n\t"
      "ds_read_b64_tr_b16 a[230:231], v7 offset:55296\n\t"
      "v_mfma_f32_16x16x32_bf16 v[64:67], a[232:235], a[104:107], v[64:67]\n\t"
      "global_load_dwordx4 a[184:187], v1, s[36:37] offset:2048 sc1\n\t"
      "v_mfma_f32_16x16x32_bf16 v[68:71], a[232:235], a[108:111], v[68:71]\n\t"
      "s_waitcnt lgkmcnt(4)\n\t"
      "v_mfma_f32_16x16x32_bf16 v[72:75], a[236:239], a[96:99], v[72:75]\n\t"
      "ds_read_b64_tr_b16 a[232:233], v8 offset:53248\n\t"
      "s_add_u32 s83, s83, 524288\n\t"
      "v_mfma_f32_16x16x32_bf16 v[76:79], a[236:239], a[100:103], v[76:79]\n\t"
      "ds_read_b64_tr_b16 a[234:235], v8 offset:55296\n\t"
      "v_mfma_f32_16x16x32_bf16 v[80:83], a[236:239], a[104:107], v[80:83]\n\t"
      "global_load_dwordx4 a[188:191], v1, s[36:37] offset:3072 sc1\n\t"
      "v_mfma_f32_16x16x32_bf16 v[84:87], a[236:239], a[108:111], v[84:87]\n\t"
      "s_add_u32 s36, s36, 12288\n\t"
      "s_addc_u32 s37, s37, 0\n\t"
      "s_waitcnt vmcnt(28) lgkmcnt(4)\n\t"
      "v_mfma_f32_16x16x32_bf16 v[24:27], a[224:227], a[112:115], v[24:27]\n\t"
      "ds_read_b64_tr_b16 a[236:237], v9 offset:53248\n\t"
      "v_mfma_f32_16x16x32_bf16 v[28:31], a[224:227], a[116:119], v[28:31]\n\t"
      "ds_read_b64_tr_b16 a[238:239], v9 offset:55296\n\t"
      "v_mfma_f32_16x16x32_bf16 v[32:35], a[224:227], a[120:123], v[32:35]\n\t"
      "global_load_dwordx2 a[192:193], v16, s[36:37] offset:0 sc1\n\t"
      "v_mfma_f32_16x16x32_bf16 v[36:39], a[224:227], a[124:127], v[36:39]\n\t"
      "buffer_load_dwordx2 v[14:15], v20, s[44:47], s83 offen sc1\n\t"
      "s_waitcnt lgkmcnt(4)\n\t"
      "v_mfma_f32_16x16x32_bf16 v[40:43], a[228:231], a[112:115], v[40:43]\n\t"
      "ds_read_b64_tr_b16 a[224:225], v6 offset:57344\n\t"
      "v_mfma_f32_16x16x32_bf16 v[44:47], a[228:231], a[116:119], v[44:47]\n\t"
      "ds_read_b64_tr_b16 a[226:227], v6 offset:59392\n\t"
      "v_mfma_f32_16x16x32_bf16 v[48:51], a[228:231], a[120:123], v[48:51]\n\t"
      "global_load_dwordx2 a[194:195], v16, s[36:37] offset:2048 sc1\n\t"
      "v_mfma_f32_16x16x32_bf16 v[52:55], a[228:231], a[124:127], v[52:55]\n\t"
      "s_waitcnt lgkmcnt(4)\n\t"
      "v_mfma_f32_16x16x32_bf16 v[56:59], a[232:235], a[112:115], v[56:59]\n\t"
      "ds_read_b64_tr_b16 a[228:229], v7 offset:57344\n\t"
      "v_mfma_f32_16x16x32_bf16 v[60:63], a[232:235], a[116:119], v[60:63]\n\t"
      "ds_read_b64_tr_b16 a[230:231], v7 offset:59392\n\t"
      "v_mfma_f32_16x16x32_bf16 v[64:67], a[232:235], a[120:123], v[64:67]\n\t"
      "global_load_dwordx2 a[196:197], v16, s[36:37] offset:512 sc1\n\t"
      "v_mfma_f32_16x16x32_bf16 v[68:71], a[232:235], a[124:127], v[68:71]\n\t"
      "s_waitcnt lgkmcnt(4)\n\t"
      "v_mfma_f32_16x16x32_bf16 v[72:75], a[236:239], a[112:115], v[72:75]\n\t"
      "ds_read_b64_tr_b16 a[232:233], v8 offset:57344\n\t"
      "v_mfma_f32_16x16x32_bf16 v[76:79], a[236:239], a[116:119], v[76:79]\n\t"
      "ds_read_b64_tr_b16 a[234:235], v8 offset:59392\n\t"
      "v_mfma_f32_16x16x32_bf16 v[80:83], a[236:239], a[120:123], v[80:83]\n\t"
      "global_load_dwordx2 a[198:199], v16, s[36:37] offset:2560 sc1\n\t"
      "v_mfma_f32_16x16x32_bf16 v[84:87], a[236:239], a[124:127], v[84:87]\n\t"
      "s_waitcnt vmcnt(29) lgkmcnt(4)\n\t"
      "v_mfma_f32_16x16x32_bf16 v[24:27], a[224:227], a[128:131], v[24:27]\n\t"
      "ds_read_b64_tr_b16 a[236:237], v9 offset:57344\n\t"
      "v_mfma_f32_16x16x32_bf16 v[28:31], a[224:227], a[132:135], v[28:31]\n\t"
      "ds_read_b64_tr_b16 a[238:239], v9 offset:59392\n\t"
      "v_mfma_f32_16x16x32_bf16 v[32:35], a[224:227], a[136:139], v[32:35]\n\t"
      "global_load_dwordx2 a[200:201], v16, s[36:37] offset:1024 sc1\n\t"
      "v_mfma_f32_16x16x32_bf16 v[36:39], a[224:227], a[140:143], v[36:39]\n\t"
      "s_waitcnt lgkmcnt(4)\n\t"
      "v_mfma_f32_16x16x32_bf16 v[40:43], a[228:231], a[128:131], v[40:43]\n\t"
      "ds_read_b64_tr_b16 a[224:225], v6 offset:61440\n\t"
      "v_mfma_f32_16x16x32_bf16 v[44:47], a[228:231], a[132:135], v[44:47]\n\t"
      "ds_read_b64_tr_b16 a[226:227], v6 offset:63488\n\t"
      "v_mfma_f32_16x16x32_bf16 v[48:51], a[228:231], a[136:139], v[48:51]\n\t"
      "global_load_dwordx2 a[202:203], v16, s[36:37] offset:3072 sc1\n\t"
      "v_mfma_f32_16x16x32_bf16 v[52:55], a[228:231], a[140:143], v[52:55]\n\t"
      "s_waitcnt lgkmcnt(4)\n\t"
      "v_mfma_f32_16x16x32_bf16 v[56:59], a[232:235], a[128:131], v[56:59]\n\t"
      "ds_read_b64_tr_b16 a[228:229], v7 offset:61440\n\t"
      "v_mfma_f32_16x16x32_bf16 v[60:63], a[232:235], a[132:135], v[60:63]\n\t"
      "ds_read_b64_tr_b16 a[230:231], v7 offset:63488\n\t"
      "v_mfma_f32_16x16x32_bf16 v[64:67], a[232:235], a[136:139], v[64:67]\n\t"
      "global_load_dwordx2 a[204:205], v16, s[36:37] offset:1536 sc1\n\t"
      "v_mfma_f32_16x16x32_bf16 v[68:71], a[232:235], a[140:143], v[68:71]\n\t"
      "s_waitcnt lgkmcnt(4)\n\t"
      "v_mfma_f32_16x16x32_bf16 v[72:75], a[236:239], a[128:131], v[72:75]\n\t"
      "ds_read_b64_tr_b16 a[232:233], v8 offset:61440\n\t"
      "v_mfma_f32_16x16x32_bf16 v[76:79], a[236:239], a[132:135], v[76:79]\n\t"
      "ds_read_b64_tr_b16 a[234:235], v8 offset:63488\n\t"
      "v_mfma_f32_16x16x32_bf16 v[80:83], a[236:239], a[136:139], v[80:83]\n\t"
      "global_load_dwordx2 a[206:207], v16, s[36:37] offset:3584 sc1\n\t"
      "v_mfma_f32_16x16x32_bf16 v[84:87], a[236:239], a[140:143], v[84:87]\n\t"
      "s_add_u32 s36, s36, 4096\n\t"
      "s_addc_u32 s37, s37, 0\n\t"
      "s_waitcnt vmcnt(18) lgkmcnt(4)\n\t"
      "v_mfma_f32_16x16x32_bf16 v[24:27], a[224:227], a[144:147], v[24:27]\n\t"
      "ds_read_b64_tr_b16 a[236:237], v9 offset:61440\n\t"
      "v_mfma_f32_16x16x32_bf16 v[28:31], a[224:227], a[148:151], v[28:31]\n\t"
      "ds_read_b64_tr_b16 a[238:239], v9 offset:63488\n\t"
      "v_mfma_f32_16x16x32_bf16 v[32:35], a[224:227], a[152:155], v[32:35]\n\t"
      "global_load_dwordx4 a[208:211], v1, s[36:37] offset:0 sc1\n\t"
      "v_mfma_f32_16x16x32_bf16 v[36:39], a[224:227], a[156:159], v[36:39]\n\t"
      "s_waitcnt vmcnt(13) lgkmcnt(4)\n\t"
      "s_barrier\n\t"
      "v_mfma_f32_16x16x32_bf16 v[40:43], a[228:231], a[144:147], v[40:43]\n\t"
      "ds_read_b64_tr_b16 a[224:225], v152 offset:0\n\t"
      "s_add_u32 s40, %8, s63\n\t"
      "s_addc_u32 s41, %9, 0\n\t"
      "v_mfma_f32_16x16x32_bf16 v[44:47], a[228:231], a[148:151], v[44:47]\n\t"
      "ds_read_b64_tr_b16 a[226:227], v152 offset:2048\n\t"
      "s_add_u32 m0, s62, 0\n\t"
      "v_mfma_f32_16x16x32_bf16 v[48:51], a[228:231], a[152:155], v[48:51]\n\t"
      "global_load_lds_dwordx4 v2, s[40:41]\n\t"
      "v_mfma_f32_16x16x32_bf16 v[52:55], a[228:231], a[156:159], v[52:55]\n\t"
      "s_waitcnt lgkmcnt(4)\n\t"
      "v_mfma_f32_16x16x32_bf16 v[56:59], a[232:235], a[144:147], v[56:59]\n\t"
      "ds_read_b64_tr_b16 a[228:229], v153 offset:0\n\t"
      "v_mfma_f32_16x16x32_bf16 v[60:63], a[232:235], a[148:151], v[60:63]\n\t"
      "ds_read_b64_tr_b16 a[230:231], v153 offset:2048\n\t"
      "s_add_u32 m0, s62, 1024\n\t"
      "v_mfma_f32_16x16x32_bf16 v[64:67], a[232:235], a[152:155], v[64:67]\n\t"
      "global_load_lds_dwordx4 v3, s[40:41]\n\t"
      "v_mfma_f32_16x16x32_bf16 v[68:71], a[232:235], a[156:159], v[68:71]\n\t"
      "s_add_u32 s40, s40, 131072\n\t"
      "s_addc_u32 s41, s41, 0\n\t"
      "s_waitcnt lgkmcnt(4)\n\t"
      "v_mfma_f32_16x16x32_bf16 v[72:75], a[236:239], a[144:147], v[72:75]\n\t"
      "ds_read_b64_tr_b16 a[232:233], v154 offset:0\n\t"
      "v_mfma_f32_16x16x32_bf16 v[76:79], a[236:239], a[148:151], v[76:79]\n\t"
      "ds_read_b64_tr_b16 a[234:235], v154 offset:2048\n\t"
      "v_mfma_f32_16x16x32_bf16 v[80:83], a[236:239], a[152:155], v[80:83]\n\t"
      "global_load_dwordx4 a[212:215], v1, s[36:37] offset:1024 sc1\n\t"
      "v_mfma_f32_16x16x32_bf16 v[84:87], a[236:239], a[156:159], v[84:87]\n\t"
      "s_waitcnt vmcnt(18) lgkmcnt(4)\n\t"
      "v_mfma_f32_16x16x32_bf16 v[24:27], a[224:227], a[160:163], v[24:27]\n\t"
      "ds_read_b64_tr_b16 a[236:237], v155 offset:0\n\t"
      "v_mfma_f32_16x16x32_bf16 v[28:31], a[224:227], a[164:167], v[28:31]\n\t"
      "ds_read_b64_tr_b16 a[238:239], v155 offset:2048\n\t"
      "s_add_u32 m0, s62, 8192\n\t"
      "v_mfma_f32_16x16x32_bf16 v[32:35], a[224:227], a[168:171], v[32:35]\n\t"
      "global_load_lds_dwordx4 v4, s[42:43]\n\t"
      "v_mfma_f32_16x16x32_bf16 v[36:39], a[224:227], a[172:175], v[36:39]\n\t"
      "s_waitcnt lgkmcnt(4)\n\t"
      "v_mfma_f32_16x16x32_bf16 v[40:43], a[228:231], a[160:163], v[40:43]\n\t"
      "ds_read_b64_tr_b16 a[224:225], v152 offset:4096\n\t"
      "v_mfma_f32_16x16x32_bf16 v[44:47], a[228:231], a[164:167], v[44:47]\n\t"
      "ds_read_b64_tr_b16 a[226:227], v152 offset:6144\n\t"
      "v_mfma_f32_16x16x32_bf16 v[48:51], a[228:231], a[168:171], v[48:51]\n\t"
      "global_load_dwordx4 a[216:219], v1, s[36:37] offset:2048 sc1\n\t"
      "v_mfma_f32_16x16x32_bf16 v[52:55], a[228:231], a[172:175], v[52:55]\n\t"
      "s_waitcnt lgkmcnt(4)\n\t"
      "v_mfma_f32_16x16x32_bf16 v[56:59], a[232:235], a[160:163], v[56:59]\n\t"
      "ds_read_b64_tr_b16 a[228:229], v153 offset:4096\n\t"
      "v_mfma_f32_16x16x32_bf16 v[60:63], a[232:235], a[164:167], v[60:63]\n\t"
      "ds_read_b64_tr_b16 a[230:231], v153 offset:6144\n\t"
      "v_mfma_f32_16x16x32_bf16 v[64:67], a[232:235], a[168:171], v[64:67]\n\t"
      "global_load_dwordx4 a[220:223], v1, s[36:37] offset:3072 sc1\n\t"
      "v_mfma_f32_16x16x32_bf16 v[68:71], a[232:235], a[172:175], v[68:71]\n\t"
      "s_add_u32 s36, s36, 12288\n\t"
      "s_addc_u32 s37, s37, 0\n\t"
      "s_waitcnt lgkmcnt(4)\n\t"
      "v_mfma_f32_16x16x32_bf16 v[72:75], a[236:239], a[160:163], v[72:75]\n\t"
      "ds_read_b64_tr_b16 a[232:233], v154 offset:4096\n\t"
      "v_mfma_f32_16x16x32_bf16 v[76:79], a[236:239], a[164:167], v[76:79]\n\t"
      "ds_read_b64_tr_b16 a[234:235], v154 offset:6144\n\t"
      "s_add_u32 m0, s62, 9216\n\t"
      "v_mfma_f32_16x16x32_bf16 v[80:83], a[236:239], a[168:171], v[80:83]\n\t"
      "global_load_lds_dwordx4 v5, s[42:43]\n\t"
      "v_mfma_f32_16x16x32_bf16 v[84:87], a[236:239], a[172:175], v[84:87]\n\t"
      "s_add_u32 s42, s42, 393216\n\t"
      "s_addc_u32 s43, s43, 0\n\t"
      "s_waitcnt vmcnt(17) lgkmcnt(4)\n\t"
      "v_mfma_f32_16x16x32_bf16 v[24:27], a[224:227], a[176:179], v[24:27]\n\t"
      "ds_read_b64_tr_b16 a[236:237], v155 offset:4096\n\t"
      "v_mfma_f32_16x16x32_bf16 v[28:31], a[224:227], a[180:183], v[28:31]\n\t"
      "ds_read_b64_tr_b16 a[238:239], v155 offset:6144\n\t"
      "s_add_u32 m0, s62, 16384\n\t"
      "v_mfma_f32_16x16x32_bf16 v[32:35], a[224:227], a[184:187], v[32:35]\n\t"
      "global_load_lds_dwordx4 v2, s[40:41]\n\t"
      "v_mfma_f32_16x16x32_bf16 v[36:39], a[224:227], a[188:191], v[36:39]\n\t"
      "s_waitcnt lgkmcnt(4)\n\t"
      "v_mfma_f32_16x16x32_bf16 v[40:43], a[228:231], a[176:179], v[40:43]\n\t"
      "ds_read_b64_tr_b16 a[224:225], v152 offset:8192\n\t"
      "v_mfma_f32_16x16x32_bf16 v[44:47], a[228:231], a[180:183], v[44:47]\n\t"
      "ds_read_b64_tr_b16 a[226:227], v152 offset:10240\n\t"
      "s_add_u32 m0, s62, 17408\n\t"
      "v_mfma_f32_16x16x32_bf16 v[48:51], a[228:231], a[184:187], v[48:51]\n\t"
      "global_load_lds_dwordx4 v3, s[40:41]\n\t"
      "v_mfma_f32_16x16x32_bf16 v[52:55], a[228:231], a[188:191], v[52:55]\n\t"
      "s_add_u32 s40, s40, 131072\n\t"
      "s_addc_u32 s41, s41, 0\n\t"
      "s_waitcnt lgkmcnt(4)\n\t"
      "s_waitcnt vmcnt(17)\n\t"
      "v_cmp_eq_u64 vcc, s[58:59], v[14:15]\n\t"
      "s_nop 1\n\t"
      "s_cmp_eq_u64 vcc, -1\n\t"
      "s_cbranch_scc1 .Lkm_a28_ok_%=\n\t"
      "s_mov_b32 s67, 0\n\t"
      ".Lkm_a28_spin_%=:\n\t"
      "s_sleep 1\n\t"
      "buffer_load_dwordx2 v[14:15], v20, s[44:47], s83 offen sc1\n\t"
      "s_add_u32 s67, s67, 1\n\t"
      "s_cmp_ge_u32 s67, 262144\n\t"
      "s_cbranch_scc1 .Lkm_a28_ok_%=\n\t"
      "s_waitcnt vmcnt(0)\n\t"
      "v_cmp_eq_u64 vcc, s[58:59], v[14:15]\n\t"
      "s_nop 1\n\t"
      "s_cmp_eq_u64 vcc, -1\n\t"
      "s_cbranch_scc0 .Lkm_a28_spin_%=\n\t"
      ".Lkm_a28_ok_%=:\n\t"
      "v_mfma_f32_16x16x32_bf16 v[56:59], a[232:235], a[176:179], v[56:59]\n\t"
      "ds_read_b64_tr_b16 a[228:229], v153 offset:8192\n\t"
      "v_mfma_f32_16x16x32_bf16 v[60:63], a[232:235], a[180:183], v[60:63]\n\t"
      "ds_read_b64_tr_b16 a[230:231], v153 offset:10240\n\t"
      "v_mfma_f32_16x16x32_bf16 v[64:67], a[232:235], a[184:187], v[64:67]\n\t"
      "global_load_dwordx4 a[0:3], v1, s[36:37] offset:0 sc1\n\t"
      "v_mfma_f32_16x16x32_bf16 v[68:71], a[232:235], a[188:191], v[68:71]\n\t"
      "s_waitcnt lgkmcnt(4)\n\t"
      "v_mfma_f32_16x16x32_bf16 v[72:75], a[236:239], a[176:179], v[72:75]\n\t"
      "ds_read_b64_tr_b16 a[232:233], v154 offset:8192\n\t"
      "v_mfma_f32_16x16x32_bf16 v[76:79], a[236:239], a[180:183], v[76:79]\n\t"
      "ds_read_b64_tr_b16 a[234:235], v154 offset:10240\n\t"
      "v_mfma_f32_16x16x32_bf16 v[80:83], a[236:239], a[184:187], v[80:83]\n\t"
      "global_load_dwordx4 a[4:7], v1, s[36:37] offset:1024 sc1\n\t"
      "v_mfma_f32_16x16x32_bf16 v[84:87], a[236:239], a[188:191], v[84:87]\n\t"
      "s_waitcnt lgkmcnt(4)\n\t"
      "v_mfma_f32_16x16x32_bf16 v[88:91], a[224:227], a[32:35], v[88:91]\n\t"
      "ds_read_b64_tr_b16 a[236:237], v155 offset:8192\n\t"
      "v_mfma_f32_16x16x32_bf16 v[92:95], a[224:227], a[36:39], v[92:95]\n\t"
      "ds_read_b64_tr_b16 a[238:239], v155 offset:10240\n\t"
      "s_add_u32 m0, s62, 24576\n\t"
      "v_mfma_f32_16x16x32_bf16 v[96:99], a[224:227], a[40:43], v[96:99]\n\t"
      "global_load_lds_dwordx4 v4, s[42:43]\n\t"
      "v_mfma_f32_16x16x32_bf16 v[100:103], a[224:227], a[44:47], v[100:103]\n\t"
      "s_waitcnt lgkmcnt(4)\n\t"
      "v_mfma_f32_16x16x32_bf16 v[104:107], a[228:231], a[32:35], v[104:107]\n\t"
      "ds_read_b64_tr_b16 a[224:225], v152 offset:12288\n\t"
      "v_mfma_f32_16x16x32_bf16 v[108:111], a[228:231], a[36:39], v[108:111]\n\t"
      "ds_read_b64_tr_b16 a[226:227], v152 offset:14336\n\t"
      "v_mfma_f32_16x16x32_bf16 v[112:115], a[228:231], a[40:43], v[112:115]\n\t"
      "global_load_dwordx4 a[8:11], v1, s[36:37] offset:2048 sc1\n\t"
      "v_mfma_f32_16x16x32_bf16 v[116:119], a[228:231], a[44:47], v[116:119]\n\t"
      "s_waitcnt lgkmcnt(4)\n\t"
      "v_mfma_f32_16x16x32_bf16 v[120:123], a[232:235], a[32:35], v[120:123]\n\t"
      "ds_read_b64_tr_b16 a[228:229], v153 offset:12288\n\t"
      "v_mfma_f32_16x16x32_bf16 v[124:127], a[232:235], a[36:39], v[124:127]\n\t"
      "ds_read_b64_tr_b16 a[230:231], v153 offset:14336\n\t"
      "v_mfma_f32_16x16x32_bf16 v[128:131], a[232:235], a[40:43], v[128:131]\n\t"
      "global_load_dwordx4 a[12:15], v1, s[36:37] offset:3072 sc1\n\t"
      "v_mfma_f32_16x16x32_bf16 v[132:135], a[232:235], a[44:47], v[132:135]\n\t"
      "s_add_u32 s36, s36, 4096\n\t"
      "s_addc_u32 s37, s37, 0\n\t"
      "s_waitcnt lgkmcnt(4)\n\t"
      "v_mfma_f32_16x16x32_bf16 v[136:139], a[236:239], a[32:35], v[136:139]\n\t"
      "ds_read_b64_tr_b16 a[232:233], v154 offset:12288\n\t"
      "v_mfma_f32_16x16x32_bf16 v[140:143], a[236:239], a[36:39], v[140:143]\n\t"
      "ds_read_b64_tr_b16 a[234:235], v154 offset:14336\n\t"
      "v_mfma_f32_16x16x32_bf16 v[144:147], a[236:239], a[40:43], v[144:147]\n\t"
      "global_load_dwordx4 a[16:19], v1, s[36:37] offset:0 sc1\n\t"
      "v_mfma_f32_16x16x32_bf16 v[148:151], a[236:239], a[44:47], v[148:151]\n\t"
      "s_waitcnt lgkmcnt(4)\n\t"
      "v_mfma_f32_16x16x32_bf16 v[88:91], a[224:227], a[48:51], v[88:91]\n\t"
      "ds_read_b64_tr_b16 a[236:237], v155 offset:12288\n\t"
      "v_mfma_f32_16x16x32_bf16 v[92:95], a[224:227], a[52:55], v[92:95]\n\t"
      "ds_read_b64_tr_b16 a[238:239], v155 offset:14336\n\t"
      "s_add_u32 m0, s62, 25600\n\t"
      "v_mfma_f32_16x16x32_bf16 v[96:99], a[224:227], a[56:59], v[96:99]\n\t"
      "global_load_lds_dwordx4 v5, s[42:43]\n\t"
      "v_mfma_f32_16x16x32_bf16 v[100:103], a[224:227], a[60:63], v[100:103]\n\t"
      "s_add_u32 s42, s42, 262144\n\t"
      "s_addc_u32 s43, s43, 0\n\t"
      "s_waitcnt lgkmcnt(4)\n\t"
      "v_mfma_f32_16x16x32_bf16 v[104:107], a[228:231], a[48:51], v[104:107]\n\t"
      "ds_read_b64_tr_b16 a[224:225], v152 offset:16384\n\t"
      "v_mfma_f32_16x16x32_bf16 v[108:111], a[228:231], a[52:55], v[108:111]\n\t"
      "ds_read_b64_tr_b16 a[226:227], v152 offset:18432\n\t"
      "v_mfma_f32_16x16x32_bf16 v[112:115], a[228:231], a[56:59], v[112:115]\n\t"
      "global_load_dwordx4 a[20:23], v1, s[36:37] offset:1024 sc1\n\t"
      "v_mfma_f32_16x16x32_bf16 v[116:119], a[228:231], a[60:63], v[116:119]\n\t"
      "s_waitcnt lgkmcnt(4)\n\t"
      "v_mfma_f32_16x16x32_bf16 v[120:123], a[232:235], a[48:51], v[120:123]\n\t"
      "ds_read_b64_tr_b16 a[228:229], v153 offset:16384\n\t"
      "v_mfma_f32_16x16x32_bf16 v[124:127], a[232:235], a[52:55], v[124:127]\n\t"
      "ds_read_b64_tr_b16 a[230:231], v153 offset:18432\n\t"
      "v_mfma_f32_16x16x32_bf16 v[128:131], a[232:235], a[56:59], v[128:131]\n\t"
      "global_load_dwordx4 a[24:27], v1, s[36:37] offset:2048 sc1\n\t"
      "v_mfma_f32_16x16x32_bf16 v[132:135], a[232:235], a[60:63], v[132:135]\n\t"
      "s_waitcnt lgkmcnt(4)\n\t"
      "v_mfma_f32_16x16x32_bf16 v[136:139], a[236:239], a[48:51], v[136:139]\n\t"
      "ds_read_b64_tr_b16 a[232:233], v154 offset:16384\n\t"
      "v_mfma_f32_16x16x32_bf16 v[140:143], a[236:239], a[52:55], v[140:143]\n\t"
      "ds_read_b64_tr_b16 a[234:235], v154 offset:18432\n\t"
      "v_mfma_f32_16x16x32_bf16 v[144:147], a[236:239], a[56:59], v[144:147]\n\t"
      "global_load_dwordx4 a[28:31], v1, s[36:37] offset:3072 sc1\n\t"
      "v_mfma_f32_16x16x32_bf16 v[148:151], a[236:239], a[60:63], v[148:151]\n\t"
      "s_add_u32 s36, s36, 4096\n\t"
      "s_addc_u32 s37, s37, 0\n\t"
      "s_waitcnt vmcnt(20) lgkmcnt(4)\n\t"
      "v_mfma_f32_16x16x32_bf16 v[24:27], a[224:227], a[192:195], v[24:27]\n\t"
      "ds_read_b64_tr_b16 a[236:237], v155 offset:16384\n\t"
      "v_mfma_f32_16x16x32_bf16 v[28:31], a[224:227], a[196:199], v[28:31]\n\t"
      "ds_read_b64_tr_b16 a[238:239], v155 offset:18432\n\t"
      "v_mfma_f32_16x16x32_bf16 v[32:35], a[224:227], a[200:203], v[32:35]\n\t"
      "global_load_dwordx4 a[32:35], v1, s[36:37] offset:0 sc1\n\t"
      "v_mfma_f32_16x16x32_bf16 v[36:39], a[224:227], a[204:207], v[36:39]\n\t"
      "s_waitcnt lgkmcnt(4)\n\t"
      "v_mfma_f32_16x16x32_bf16 v[40:43], a[228:231], a[192:195], v[40:43]\n\t"
      "ds_read_b64_tr_b16 a[224:225], v152 offset:20480\n\t"
      "v_mfma_f32_16x16x32_bf16 v[44:47], a[228:231], a[196:199], v[44:47]\n\t"
      "ds_read_b64_tr_b16 a[226:227], v152 offset:22528\n\t"
      "v_mfma_f32_16x16x32_bf16 v[48:51], a[228:231], a[200:203], v[48:51]\n\t"
      "global_load_dwordx4 a[36:39], v1, s[36:37] offset:1024 sc1\n\t"
      "v_mfma_f32_16x16x32_bf16 v[52:55], a[228:231], a[204:207], v[52:55]\n\t"
      "s_waitcnt lgkmcnt(4)\n\t"
      "v_mfma_f32_16x16x32_bf16 v[56:59], a[232:235], a[192:195], v[56:59]\n\t"
      "ds_read_b64_tr_b16 a[228:229], v153 offset:20480\n\t"
      "v_mfma_f32_16x16x32_bf16 v[60:63], a[232:235], a[196:199], v[60:63]\n\t"
      "ds_read_b64_tr_b16 a[230:231], v153 offset:22528\n\t"
      "v_mfma_f32_16x16x32_bf16 v[64:67], a[232:235], a[200:203], v[64:67]\n\t"
      "global_load_dwordx4 a[40:43], v1, s[36:37] offset:2048 sc1\n\t"
      "v_mfma_f32_16x16x32_bf16 v[68:71], a[232:235], a[204:207], v[68:71]\n\t"
      "s_waitcnt lgkmcnt(4)\n\t"
      "v_mfma_f32_16x16x32_bf16 v[72:75], a[236:239], a[192:195], v[72:75]\n\t"
      "ds_read_b64_tr_b16 a[232:233], v154 offset:20480\n\t"
      "v_mfma_f32_16x16x32_bf16 v[76:79], a[236:239], a[196:199], v[76:79]\n\t"
      "ds_read_b64_tr_b16 a[234:235], v154 offset:22528\n\t"
      "v_mfma_f32_16x16x32_bf16 v[80:83], a[236:239], a[200:203], v[80:83]\n\t"
      "global_load_dwordx4 a[44:47], v1, s[36:37] offset:3072 sc1\n\t"
      "v_mfma_f32_16x16x32_bf16 v[84:87], a[236:239], a[204:207], v[84:87]\n\t"
      "s_add_u32 s36, s36, 4096\n\t"
      "s_addc_u32 s37, s37, 0\n\t"
      "s_waitcnt vmcnt(17) lgkmcnt(4)\n\t"
      "v_mfma_f32_16x16x32_bf16 v[24:27], a[224:227], a[208:211], v[24:27]\n\t"
      "ds_read_b64_tr_b16 a[236:237], v155 offset:20480\n\t"
      "v_mfma_f32_16x16x32_bf16 v[28:31], a[224:227], a[212:215], v[28:31]\n\t"
      "ds_read_b64_tr_b16 a[238:239], v155 offset:22528\n\t"
      "v_mfma_f32_16x16x32_bf16 v[32:35], a[224:227], a[216:219], v[32:35]\n\t"
      "global_load_dwordx4 a[48:51], v1, s[36:37] offset:0 sc1\n\t"
      "v_mfma_f32_16x16x32_bf16 v[36:39], a[224:227], a[220:223], v[36:39]\n\t"
      "s_waitcnt lgkmcnt(4)\n\t"
      "v_mfma_f32_16x16x32_bf16 v[40:43], a[228:231], a[208:211], v[40:43]\n\t"
      "ds_read_b64_tr_b16 a[224:225], v152 offset:24576\n\t"
      "v_mfma_f32_16x16x32_bf16 v[44:47], a[228:231], a[212:215], v[44:47]\n\t"
      "ds_read_b64_tr_b16 a[226:227], v152 offset:26624\n\t"
      "v_mfma_f32_16x16x32_bf16 v[48:51], a[228:231], a[216:219], v[48:51]\n\t"
      "global_load_dwordx4 a[52:55], v1, s[36:37] offset:1024 sc1\n\t"
      "v_mfma_f32_16x16x32_bf16 v[52:55], a[228:231], a[220:223], v[52:55]\n\t"
      "s_waitcnt lgkmcnt(4)\n\t"
      "v_mfma_f32_16x16x32_bf16 v[56:59], a[232:235], a[208:211], v[56:59]\n\t"
      "ds_read_b64_tr_b16 a[228:229], v153 offset:24576\n\t"
      "v_mfma_f32_16x16x32_bf16 v[60:63], a[232:235], a[212:215], v[60:63]\n\t"
      "ds_read_b64_tr_b16 a[230:231], v153 offset:26624\n\t"
      "v_mfma_f32_16x16x32_bf16 v[64:67], a[232:235], a[216:219], v[64:67]\n\t"
      "global_load_dwordx4 a[56:59], v1, s[36:37] offset:2048 sc1\n\t"
      "v_mfma_f32_16x16x32_bf16 v[68:71], a[232:235], a[220:223], v[68:71]\n\t"
      "s_waitcnt lgkmcnt(4)\n\t"
      "v_mfma_f32_16x16x32_bf16 v[72:75], a[236:239], a[208:211], v[72:75]\n\t"
      "ds_read_b64_tr_b16 a[232:233], v154 offset:24576\n\t"
      "v_mfma_f32_16x16x32_bf16 v[76:79], a[236:239], a[212:215], v[76:79]\n\t"
      "ds_read_b64_tr_b16 a[234:235], v154 offset:26624\n\t"
      "v_mfma_f32_16x16x32_bf16 v[80:83], a[236:239], a[216:219], v[80:83]\n\t"
      "global_load_dwordx4 a[60:63], v1, s[36:37] offset:3072 sc1\n\t"
      "v_mfma_f32_16x16x32_bf16 v[84:87], a[236:239], a[220:223], v[84:87]\n\t"
      "s_add_u32 s36, s36, 4096\n\t"
      "s_addc_u32 s37, s37, 0\n\t"
      "s_waitcnt lgkmcnt(4)\n\t"
      "v_mfma_f32_16x16x32_bf16 v[88:91], a[224:227], a[64:67], v[88:91]\n\t"
      "ds_read_b64_tr_b16 a[236:237], v155 offset:24576\n\t"
      "v_mfma_f32_16x16x32_bf16 v[92:95], a[224:227], a[68:71], v[92:95]\n\t"
      "ds_read_b64_tr_b16 a[238:239], v155 offset:26624\n\t"
      "v_mfma_f32_16x16x32_bf16 v[96:99], a[224:227], a[72:75], v[96:99]\n\t"
      "v_mfma_f32_16x16x32_bf16 v[100:103], a[224:227], a[76:79], v[100:103]\n\t"
      "s_waitcnt lgkmcnt(4)\n\t"
      "v_mfma_f32_16x16x32_bf16 v[104:107], a[228:231], a[64:67], v[104:107]\n\t"
      "ds_read_b64_tr_b16 a[224:225], v152 offset:28672\n\t"
      "v_mfma_f32_16x16x32_bf16 v[108:111], a[228:231], a[68:71], v[108:111]\n\t"
      "ds_read_b64_tr_b16 a[226:227], v152 offset:30720\n\t"
      "v_mfma_f32_16x16x32_bf16 v[112:115], a[228:231], a[72:75], v[112:115]\n\t"
      "v_mfma_f32_16x16x32_bf16 v[116:119], a[228:231], a[76:79], v[116:119]\n\t"
      "s_waitcnt lgkmcnt(4)\n\t"
      "v_mfma_f32_16x16x32_bf16 v[120:123], a[232:235], a[64:67], v[120:123]\n\t"
      "ds_read_b64_tr_b16 a[228:229], v153 offset:28672\n\t"
      "v_mfma_f32_16x16x32_bf16 v[124:127], a[232:235], a[68:71], v[124:127]\n\t"
      "ds_read_b64_tr_b16 a[230:231], v153 offset:30720\n\t"
      "v_mfma_f32_16x16x32_bf16 v[128:131], a[232:235], a[72:75], v[128:131]\n\t"
      "v_mfma_f32_16x16x32_bf16 v[132:135], a[232:235], a[76:79], v[132:135]\n\t"
      "s_waitcnt lgkmcnt(4)\n\t"
      "v_mfma_f32_16x16x32_bf16 v[136:139], a[236:239], a[64:67], v[136:139]\n\t"
      "ds_read_b64_tr_b16 a[232:233], v154 offset:28672\n\t"
      "v_mfma_f32_16x16x32_bf16 v[140:143], a[236:239], a[68:71], v[140:143]\n\t"
      "ds_read_b64_tr_b16 a[234:235], v154 offset:30720\n\t"
      "v_mfma_f32_16x16x32_bf16 v[144:147], a[236:239], a[72:75], v[144:147]\n\t"
      "v_mfma_f32_16x16x32_bf16 v[148:151], a[236:239], a[76:79], v[148:151]\n\t"
      "s_waitcnt lgkmcnt(4)\n\t"
      "v_mfma_f32_16x16x32_bf16 v[88:91], a[224:227], a[80:83], v[88:91]\n\t"
      "ds_read_b64_tr_b16 a[236:237], v155 offset:28672\n\t"
      "v_mfma_f32_16x16x32_bf16 v[92:95], a[224:227], a[84:87], v[92:95]\n\t"
      "ds_read_b64_tr_b16 a[238:239], v155 offset:30720\n\t"
      "v_mfma_f32_16x16x32_bf16 v[96:99], a[224:227], a[88:91], v[96:99]\n\t"
      "global_load_dwordx4 a[64:67], v1, s[36:37] offset:0 sc1\n\t"
      "v_mfma_f32_16x16x32_bf16 v[100:103], a[224:227], a[92:95], v[100:103]\n\t"
      "s_waitcnt vmcnt(12) lgkmcnt(4)\n\t"
      "s_barrier\n\t"
      "v_mfma_f32_16x16x32_bf16 v[104:107], a[228:231], a[80:83], v[104:107]\n\t"
      "ds_read_b64_tr_b16 a[224:225], v6 offset:0\n\t"
      "v_mfma_f32_16x16x32_bf16 v[108:111], a[228:231], a[84:87], v[108:111]\n\t"
      "ds_read_b64_tr_b16 a[226:227], v6 offset:2048\n\t"
      "s_add_u32 m0, s62, 32768\n\t"
      "v_mfma_f32_16x16x32_bf16 v[112:115], a[228:231], a[88:91], v[112:115]\n\t"
      "global_load_lds_dwordx4 v2, s[40:41]\n\t"
      "v_mfma_f32_16x16x32_bf16 v[116:119], a[228:231], a[92:95], v[116:119]\n\t"
      "s_waitcnt lgkmcnt(4)\n\t"
      "v_mfma_f32_16x16x32_bf16 v[120:123], a[232:235], a[80:83], v[120:123]\n\t"
      "ds_read_b64_tr_b16 a[228:229], v7 offset:0\n\t"
      "v_mfma_f32_16x16x32_bf16 v[124:127], a[232:235], a[84:87], v[124:127]\n\t"
      "ds_read_b64_tr_b16 a[230:231], v7 offset:2048\n\t"
      "s_add_u32 m0, s62, 33792\n\t"
      "v_mfma_f32_16x16x32_bf16 v[128:131], a[232:235], a[88:91], v[128:131]\n\t"
      "global_load_lds_dwordx4 v3, s[40:41]\n\t"
      "v_mfma_f32_16x16x32_bf16 v[132:135], a[232:235], a[92:95], v[132:135]\n\t"
      "s_add_u32 s40, s40, 131072\n\t"
      "s_addc_u32 s41, s41, 0\n\t"
      "s_waitcnt lgkmcnt(4)\n\t"
      "v_mfma_f32_16x16x32_bf16 v[136:139], a[236:239], a[80:83], v[136:139]\n\t"
      "ds_read_b64_tr_b16 a[232:233], v8 offset:0\n\t"
      "v_mfma_f32_16x16x32_bf16 v[140:143], a[236:239], a[84:87], v[140:143]\n\t"
      "ds_read_b64_tr_b16 a[234:235], v8 offset:2048\n\t"
      "s_add_u32 m0, s62, 40960\n\t"
      "v_mfma_f32_16x16x32_bf16 v[144:147], a[236:239], a[88:91], v[144:147]\n\t"
      "global_load_lds_dwordx4 v4, s[42:43]\n\t"
      "v_mfma_f32_16x16x32_bf16 v[148:151], a[236:239], a[92:95], v[148:151]\n\t"
      "s_waitcnt vmcnt(17) lgkmcnt(4)\n\t"
      "v_mfma_f32_16x16x32_bf16 v[24:27], a[224:227], a[0:3], v[24:27]\n\t"
      "ds_read_b64_tr_b16 a[236:237], v9 offset:0\n\t"
      "v_mfma_f32_16x16x32_bf16 v[28:31], a[224:227], a[4:7], v[28:31]\n\t"
      "ds_read_b64_tr_b16 a[238:239], v9 offset:2048\n\t"
      "s_add_u32 m0, s62, 41984\n\t"
      "v_mfma_f32_16x16x32_bf16 v[32:35], a[224:227], a[8:11], v[32:35]\n\t"
      "global_load_lds_dwordx4 v5, s[42:43]\n\t"
      "v_mfma_f32_16x16x32_bf16 v[36:39], a[224:227], a[12:15], v[36:39]\n\t"
      "s_add_u32 s42, s42, 262144\n\t"
      "s_addc_u32 s43, s43, 0\n\t"
      "s_waitcnt lgkmcnt(4)\n\t"
      "v_mfma_f32_16x16x32_bf16 v[40:43], a[228:231], a[0:3], v[40:43]\n\t"
      "ds_read_b64_tr_b16 a[224:225], v6 offset:4096\n\t"
      "v_mfma_f32_16x16x32_bf16 v[44:47], a[228:231], a[4:7], v[44:47]\n\t"
      "ds_read_b64_tr_b16 a[226:227], v6 offset:6144\n\t"
      "s_add_u32 m0, s62, 49152\n\t"
      "v_mfma_f32_16x16x32_bf16 v[48:51], a[228:231], a[8:11], v[48:51]\n\t"
      "global_load_lds_dwordx4 v2, s[40:41]\n\t"
      "v_mfma_f32_16x16x32_bf16 v[52:55], a[228:231], a[12:15], v[52:55]\n\t"
      "s_waitcnt lgkmcnt(4)\n\t"
      "v_mfma_f32_16x16x32_bf16 v[56:59], a[232:235], a[0:3], v[56:59]\n\t"
      "ds_read_b64_tr_b16 a[228:229], v7 offset:4096\n\t"
      "v_mfma_f32_16x16x32_bf16 v[60:63], a[232:235], a[4:7], v[60:63]\n\t"
      "ds_read_b64_tr_b16 a[230:231], v7 offset:6144\n\t"
      "s_add_u32 m0, s62, 50176\n\t"
      "v_mfma_f32_16x16x32_bf16 v[64:67], a[232:235], a[8:11], v[64:67]\n\t"
      "global_load_lds_dwordx4 v3, s[40:41]\n\t"
      "v_mfma_f32_16x16x32_bf16 v[68:71], a[232:235], a[12:15], v[68:71]\n\t"
      "s_waitcnt lgkmcnt(4)\n\t"
      "v_mfma_f32_16x16x32_bf16 v[72:75], a[236:239], a[0:3], v[72:75]\n\t"
      "ds_read_b64_tr_b16 a[232:233], v8 offset:4096\n\t"
      "v_mfma_f32_16x16x32_bf16 v[76:79], a[236:239], a[4:7], v[76:79]\n\t"
      "ds_read_b64_tr_b16 a[234:235], v8 offset:6144\n\t"
      "v_mfma_f32_16x16x32_bf16 v[80:83], a[236:239], a[8:11], v[80:83]\n\t"
      "global_load_dwordx4 a[68:71], v1, s[36:37] offset:1024 sc1\n\t"
      "v_mfma_f32_16x16x32_bf16 v[84:87], a[236:239], a[12:15], v[84:87]\n\t"
      "s_waitcnt vmcnt(16) lgkmcnt(4)\n\t"
      "v_mfma_f32_16x16x32_bf16 v[24:27], a[224:227], a[16:19], v[24:27]\n\t"
      "ds_read_b64_tr_b16 a[236:237], v9 offset:4096\n\t"
      "v_mfma_f32_16x16x32_bf16 v[28:31], a[224:227], a[20:23], v[28:31]\n\t"
      "ds_read_b64_tr_b16 a[238:239], v9 offset:6144\n\t"
      "s_add_u32 m0, s62, 57344\n\t"
      "v_mfma_f32_16x16x32_bf16 v[32:35], a[224:227], a[24:27], v[32:35]\n\t"
      "global_load_lds_dwordx4 v4, s[42:43]\n\t"
      "v_mfma_f32_16x16x32_bf16 v[36:39], a[224:227], a[28:31], v[36:39]\n\t"
      "s_waitcnt lgkmcnt(4)\n\t"
      "v_mfma_f32_16x16x32_bf16 v[40:43], a[228:231], a[16:19], v[40:43]\n\t"
      "ds_read_b64_tr_b16 a[224:225], v6 offset:8192\n\t"
      "v_mfma_f32_16x16x32_bf16 v[44:47], a[228:231], a[20:23], v[44:47]\n\t"
      "ds_read_b64_tr_b16 a[226:227], v6 offset:10240\n\t"
      "v_mfma_f32_16x16x32_bf16 v[48:51], a[228:231], a[24:27], v[48:51]\n\t"
      "global_load_dwordx4 a[72:75], v1, s[36:37] offset:2048 sc1\n\t"
      "v_mfma_f32_16x16x32_bf16 v[52:55], a[228:231], a[28:31], v[52:55]\n\t"
      "s_waitcnt lgkmcnt(4)\n\t"
      "v_mfma_f32_16x16x32_bf16 v[56:59], a[232:235], a[16:19], v[56:59]\n\t"
      "ds_read_b64_tr_b16 a[228:229], v7 offset:8192\n\t"
      "v_mfma_f32_16x16x32_bf16 v[60:63], a[232:235], a[20:23], v[60:63]\n\t"
      "ds_read_b64_tr_b16 a[230:231], v7 offset:10240\n\t"
      "v_mfma_f32_16x16x32_bf16 v[64:67], a[232:235], a[24:27], v[64:67]\n\t"
      "global_load_dwordx4 a[76:79], v1, s[36:37] offset:3072 sc1\n\t"
      "v_mfma_f32_16x16x32_bf16 v[68:71], a[232:235], a[28:31], v[68:71]\n\t"
      "s_add_u32 s36, s36, 4096\n\t"
      "s_addc_u32 s37, s37, 0\n\t"
      "s_waitcnt lgkmcnt(4)\n\t"
      "v_mfma_f32_16x16x32_bf16 v[72:75], a[236:239], a[16:19], v[72:75]\n\t"
      "ds_read_b64_tr_b16 a[232:233], v8 offset:8192\n\t"
      "v_mfma_f32_16x16x32_bf16 v[76:79], a[236:239], a[20:23], v[76:79]\n\t"
      "ds_read_b64_tr_b16 a[234:235], v8 offset:10240\n\t"
      "v_mfma_f32_16x16x32_bf16 v[80:83], a[236:239], a[24:27], v[80:83]\n\t"
      "global_load_dwordx4 a[80:83], v1, s[36:37] offset:0 sc1\n\t"
      "v_mfma_f32_16x16x32_bf16 v[84:87], a[236:239], a[28:31], v[84:87]\n\t"
      "s_waitcnt lgkmcnt(4)\n\t"
      "v_mfma_f32_16x16x32_bf16 v[88:91], a[224:227], a[96:99], v[88:91]\n\t"
      "ds_read_b64_tr_b16 a[236:237], v9 offset:8192\n\t"
      "v_mfma_f32_16x16x32_bf16 v[92:95], a[224:227], a[100:103], v[92:95]\n\t"
      "ds_read_b64_tr_b16 a[238:239], v9 offset:10240\n\t"
      "s_add_u32 m0, s62, 58368\n\t"
      "v_mfma_f32_16x16x32_bf16 v[96:99], a[224:227], a[104:107], v[96:99]\n\t"
      "global_load_lds_dwordx4 v5, s[42:43]\n\t"
      "v_mfma_f32_16x16x32_bf16 v[100:103], a[224:227], a[108:111], v[100:103]\n\t"
      "s_add_u32 s42, s42, 262144\n\t"
      "s_addc_u32 s43, s43, 0\n\t"
      "s_waitcnt lgkmcnt(4)\n\t"
      "v_mfma_f32_16x16x32_bf16 v[104:107], a[228:231], a[96:99], v[104:107]\n\t"
      "ds_read_b64_tr_b16 a[224:225], v6 offset:12288\n\t"
      "v_mfma_f32_16x16x32_bf16 v[108:111], a[228:231], a[100:103], v[108:111]\n\t"
      "ds_read_b64_tr_b16 a[226:227], v6 offset:14336\n\t"
      "v_mfma_f32_16x16x32_bf16 v[112:115], a[228:231], a[104:107], v[112:115]\n\t"
      "global_load_dwordx4 a[84:87], v1, s[36:37] offset:1024 sc1\n\t"
      "v_mfma_f32_16x16x32_bf16 v[116:119], a[228:231], a[108:111], v[116:119]\n\t"
      "s_waitcnt lgkmcnt(4)\n\t"
      "v_mfma_f32_16x16x32_bf16 v[120:123], a[232:235], a[96:99], v[120:123]\n\t"
      "ds_read_b64_tr_b16 a[228:229], v7 offset:12288\n\t"
      "v_mfma_f32_16x16x32_bf16 v[124:127], a[232:235], a[100:103], v[124:127]\n\t"
      "ds_read_b64_tr_b16 a[230:231], v7 offset:14336\n\t"
      "v_mfma_f32_16x16x32_bf16 v[128:131], a[232:235], a[104:107], v[128:131]\n\t"
      "global_load_dwordx4 a[88:91], v1, s[36:37] offset:2048 sc1\n\t"
      "v_mfma_f32_16x16x32_bf16 v[132:135], a[232:235], a[108:111], v[132:135]\n\t"
      "s_waitcnt lgkmcnt(4)\n\t"
      "v_mfma_f32_16x16x32_bf16 v[136:139], a[236:239], a[96:99], v[136:139]\n\t"
      "ds_read_b64_tr_b16 a[232:233], v8 offset:12288\n\t"
      "v_mfma_f32_16x16x32_bf16 v[140:143], a[236:239], a[100:103], v[140:143]\n\t"
      "ds_read_b64_tr_b16 a[234:235], v8 offset:14336\n\t"
      "v_mfma_f32_16x16x32_bf16 v[144:147], a[236:239], a[104:107], v[144:147]\n\t"
      "global_load_dwordx4 a[92:95], v1, s[36:37] offset:3072 sc1\n\t"
      "v_mfma_f32_16x16x32_bf16 v[148:151], a[236:239], a[108:111], v[148:151]\n\t"
      "s_add_u32 s36, s36, 4096\n\t"
      "s_addc_u32 s37, s37, 0\n\t"
      "s_waitcnt lgkmcnt(4)\n\t"
      "v_mfma_f32_16x16x32_bf16 v[88:91], a[224:227], a[112:115], v[88:91]\n\t"
      "ds_read_b64_tr_b16 a[236:237], v9 offset:12288\n\t"
      "v_mfma_f32_16x16x32_bf16 v[92:95], a[224:227], a[116:119], v[92:95]\n\t"
      "ds_read_b64_tr_b16 a[238:239], v9 offset:14336\n\t"
      "v_mfma_f32_16x16x32_bf16 v[96:99], a[224:227], a[120:123], v[96:99]\n\t"
      "global_load_dwordx4 a[96:99], v1, s[36:37] offset:0 sc1\n\t"
      "v_mfma_f32_16x16x32_bf16 v[100:103], a[224:227], a[124:127], v[100:103]\n\t"
      "s_waitcnt lgkmcnt(4)\n\t"
      "v_mfma_f32_16x16x32_bf16 v[104:107], a[228:231], a[112:115], v[104:107]\n\t"
      "ds_read_b64_tr_b16 a[224:225], v6 offset:16384\n\t"
      "v_mfma_f32_16x16x32_bf16 v[108:111], a[228:231], a[116:119], v[108:111]\n\t"
      "ds_read_b64_tr_b16 a[226:227], v6 offset:18432\n\t"
      "v_mfma_f32_16x16x32_bf16 v[112:115], a[228:231], a[120:123], v[112:115]\n\t"
      "global_load_dwordx4 a[100:103], v1, s[36:37] offset:1024 sc1\n\t"
      "v_mfma_f32_16x16x32_bf16 v[116:119], a[228:231], a[124:127], v[116:119]\n\t"
      "s_waitcnt lgkmcnt(4)\n\t"
      "v_mfma_f32_16x16x32_bf16 v[120:123], a[232:235], a[112:115], v[120:123]\n\t"
      "ds_read_b64_tr_b16 a[228:229], v7 offset:16384\n\t"
      "v_mfma_f32_16x16x32_bf16 v[124:127], a[232:235], a[116:119], v[124:127]\n\t"
      "ds_read_b64_tr_b16 a[230:231], v7 offset:18432\n\t"
      "v_mfma_f32_16x16x32_bf16 v[128:131], a[232:235], a[120:123], v[128:131]\n\t"
      "global_load_dwordx4 a[104:107], v1, s[36:37] offset:2048 sc1\n\t"
      "v_mfma_f32_16x16x32_bf16 v[132:135], a[232:235], a[124:127], v[132:135]\n\t"
      "s_waitcnt lgkmcnt(4)\n\t"
      "v_mfma_f32_16x16x32_bf16 v[136:139], a[236:239], a[112:115], v[136:139]\n\t"
      "ds_read_b64_tr_b16 a[232:233], v8 offset:16384\n\t"
      "v_mfma_f32_16x16x32_bf16 v[140:143], a[236:239], a[116:119], v[140:143]\n\t"
      "ds_read_b64_tr_b16 a[234:235], v8 offset:18432\n\t"
      "v_mfma_f32_16x16x32_bf16 v[144:147], a[236:239], a[120:123], v[144:147]\n\t"
      "global_load_dwordx4 a[108:111], v1, s[36:37] offset:3072 sc1\n\t"
      "v_mfma_f32_16x16x32_bf16 v[148:151], a[236:239], a[124:127], v[148:151]\n\t"
      "s_add_u32 s36, s36, 4096\n\t"
      "s_addc_u32 s37, s37, 0\n\t"
      "s_waitcnt vmcnt(24) lgkmcnt(4)\n\t"
      "v_mfma_f32_16x16x32_bf16 v[24:27], a[224:227], a[32:35], v[24:27]\n\t"
      "ds_read_b64_tr_b16 a[236:237], v9 offset:16384\n\t"
      "v_mfma_f32_16x16x32_bf16 v[28:31], a[224:227], a[36:39], v[28:31]\n\t"
      "ds_read_b64_tr_b16 a[238:239], v9 offset:18432\n\t"
      "v_mfma_f32_16x16x32_bf16 v[32:35], a[224:227], a[40:43], v[32:35]\n\t"
      "global_load_dwordx4 a[112:115], v1, s[36:37] offset:0 sc1\n\t"
      "v_mfma_f32_16x16x32_bf16 v[36:39], a[224:227], a[44:47], v[36:39]\n\t"
      "s_waitcnt lgkmcnt(4)\n\t"
      "v_mfma_f32_16x16x32_bf16 v[40:43], a[228:231], a[32:35], v[40:43]\n\t"
      "ds_read_b64_tr_b16 a[224:225], v6 offset:20480\n\t"
      "v_mfma_f32_16x16x32_bf16 v[44:47], a[228:231], a[36:39], v[44:47]\n\t"
      "ds_read_b64_tr_b16 a[226:227], v6 offset:22528\n\t"
      "v_mfma_f32_16x16x32_bf16 v[48:51], a[228:231], a[40:43], v[48:51]\n\t"
      "global_load_dwordx4 a[116:119], v1, s[36:37] offset:1024 sc1\n\t"
      "v_mfma_f32_16x16x32_bf16 v[52:55], a[228:231], a[44:47], v[52:55]\n\t"
      "s_waitcnt lgkmcnt(4)\n\t"
      "v_mfma_f32_16x16x32_bf16 v[56:59], a[232:235], a[32:35], v[56:59]\n\t"
      "ds_read_b64_tr_b16 a[228:229], v7 offset:20480\n\t"
      "v_mfma_f32_16x16x32_bf16 v[60:63], a[232:235], a[36:39], v[60:63]\n\t"
      "ds_read_b64_tr_b16 a[230:231], v7 offset:22528\n\t"
      "v_mfma_f32_16x16x32_bf16 v[64:67], a[232:235], a[40:43], v[64:67]\n\t"
      "global_load_dwordx4 a[120:123], v1, s[36:37] offset:2048 sc1\n\t"
      "v_mfma_f32_16x16x32_bf16 v[68:71], a[232:235], a[44:47], v[68:71]\n\t"
      "s_waitcnt lgkmcnt(4)\n\t"
      "v_mfma_f32_16x16x32_bf16 v[72:75], a[236:239], a[32:35], v[72:75]\n\t"
      "ds_read_b64_tr_b16 a[232:233], v8 offset:20480\n\t"
      "v_mfma_f32_16x16x32_bf16 v[76:79], a[236:239], a[36:39], v[76:79]\n\t"
      "ds_read_b64_tr_b16 a[234:235], v8 offset:22528\n\t"
      "v_mfma_f32_16x16x32_bf16 v[80:83], a[236:239], a[40:43], v[80:83]\n\t"
      "global_load_dwordx4 a[124:127], v1, s[36:37] offset:3072 sc1\n\t"
      "v_mfma_f32_16x16x32_bf16 v[84:87], a[236:239], a[44:47], v[84:87]\n\t"
      "s_add_u32 s36, s36, 0\n\t"
      "s_addc_u32 s37, s37, 0\n\t"
      "s_waitcnt vmcnt(24) lgkmcnt(4)\n\t"
      "v_mfma_f32_16x16x32_bf16 v[24:27], a[224:227], a[48:51], v[24:27]\n\t"
      "ds_read_b64_tr_b16 a[236:237], v9 offset:20480\n\t"
      "v_mfma_f32_16x16x32_bf16 v[28:31], a[224:227], a[52:55], v[28:31]\n\t"
      "ds_read_b64_tr_b16 a[238:239], v9 offset:22528\n\t"
      "s_add_u32 m0, s61, 0\n\t"
      "v_mfma_f32_16x16x32_bf16 v[32:35], a[224:227], a[56:59], v[32:35]\n\t"
      "buffer_load_dwordx4 v246, s[48:51], s52 offen lds\n\t"
      "v_mfma_f32_16x16x32_bf16 v[36:39], a[224:227], a[60:63], v[36:39]\n\t"
      "s_waitcnt lgkmcnt(4)\n\t"
      "v_mfma_f32_16x16x32_bf16 v[40:43], a[228:231], a[48:51], v[40:43]\n\t"
      "ds_read_b64_tr_b16 a[224:225], v6 offset:24576\n\t"
      "v_mfma_f32_16x16x32_bf16 v[44:47], a[228:231], a[52:55], v[44:47]\n\t"
      "ds_read_b64_tr_b16 a[226:227], v6 offset:26624\n\t"
      "v_mfma_f32_16x16x32_bf16 v[48:51], a[228:231], a[56:59], v[48:51]\n\t"
      "v_mfma_f32_16x16x32_bf16 v[52:55], a[228:231], a[60:63], v[52:55]\n\t"
      "s_waitcnt lgkmcnt(4)\n\t"
      "v_mfma_f32_16x16x32_bf16 v[56:59], a[232:235], a[48:51], v[56:59]\n\t"
      "ds_read_b64_tr_b16 a[228:229], v7 offset:24576\n\t"
      "v_mfma_f32_16x16x32_bf16 v[60:63], a[232:235], a[52:55], v[60:63]\n\t"
      "ds_read_b64_tr_b16 a[230:231], v7 offset:26624\n\t"
      "v_mfma_f32_16x16x32_bf16 v[64:67], a[232:235], a[56:59], v[64:67]\n\t"
      "v_mfma_f32_16x16x32_bf16 v[68:71], a[232:235], a[60:63], v[68:71]\n\t"
      "s_waitcnt lgkmcnt(4)\n\t"
      "v_mfma_f32_16x16x32_bf16 v[72:75], a[236:239], a[48:51], v[72:75]\n\t"
      "ds_read_b64_tr_b16 a[232:233], v8 offset:24576\n\t"
      "v_mfma_f32_16x16x32_bf16 v[76:79], a[236:239], a[52:55], v[76:79]\n\t"
      "ds_read_b64_tr_b16 a[234:235], v8 offset:26624\n\t"
      "s_add_u32 m0, s61, 1024\n\t"
      "v_mfma_f32_16x16x32_bf16 v[80:83], a[236:239], a[56:59], v[80:83]\n\t"
      "buffer_load_dwordx4 v246, s[48:51], s53 offen lds\n\t"
      "v_mfma_f32_16x16x32_bf16 v[84:87], a[236:239], a[60:63], v[84:87]\n\t"
      "s_waitcnt lgkmcnt(4)\n\t"
      "v_mfma_f32_16x16x32_bf16 v[88:91], a[224:227], a[128:131], v[88:91]\n\t"
      "ds_read_b64_tr_b16 a[236:237], v9 offset:24576\n\t"
      "v_mfma_f32_16x16x32_bf16 v[92:95], a[224:227], a[132:135], v[92:95]\n\t"
      "ds_read_b64_tr_b16 a[238:239], v9 offset:26624\n\t"
      "v_mfma_f32_16x16x32_bf16 v[96:99], a[224:227], a[136:139], v[96:99]\n\t"
      "v_mfma_f32_16x16x32_bf16 v[100:103], a[224:227], a[140:143], v[100:103]\n\t"
      "s_waitcnt lgkmcnt(4)\n\t"
      "v_mfma_f32_16x16x32_bf16 v[104:107], a[228:231], a[128:131], v[104:107]\n\t"
      "ds_read_b64_tr_b16 a[224:225], v6 offset:28672\n\t"
      "v_mfma_f32_16x16x32_bf16 v[108:111], a[228:231], a[132:135], v[108:111]\n\t"
      "ds_read_b64_tr_b16 a[226:227], v6 offset:30720\n\t"
      "v_mfma_f32_16x16x32_bf16 v[112:115], a[228:231], a[136:139], v[112:115]\n\t"
      "v_mfma_f32_16x16x32_bf16 v[116:119], a[228:231], a[140:143], v[116:119]\n\t"
      "s_waitcnt lgkmcnt(4)\n\t"
      "v_mfma_f32_16x16x32_bf16 v[120:123], a[232:235], a[128:131], v[120:123]\n\t"
      "ds_read_b64_tr_b16 a[228:229], v7 offset:28672\n\t"
      "v_mfma_f32_16x16x32_bf16 v[124:127], a[232:235], a[132:135], v[124:127]\n\t"
      "ds_read_b64_tr_b16 a[230:231], v7 offset:30720\n\t"
      "s_add_u32 m0, s61, 2048\n\t"
      "v_mfma_f32_16x16x32_bf16 v[128:131], a[232:235], a[136:139], v[128:131]\n\t"
      "buffer_load_dwordx4 v246, s[48:51], s54 offen lds\n\t"
      "v_mfma_f32_16x16x32_bf16 v[132:135], a[232:235], a[140:143], v[132:135]\n\t"
      "s_waitcnt lgkmcnt(4)\n\t"
      "v_mfma_f32_16x16x32_bf16 v[136:139], a[236:239], a[128:131], v[136:139]\n\t"
      "ds_read_b64_tr_b16 a[232:233], v8 offset:28672\n\t"
      "v_mfma_f32_16x16x32_bf16 v[140:143], a[236:239], a[132:135], v[140:143]\n\t"
      "ds_read_b64_tr_b16 a[234:235], v8 offset:30720\n\t"
      "v_mfma_f32_16x16x32_bf16 v[144:147], a[236:239], a[136:139], v[144:147]\n\t"
      "v_mfma_f32_16x16x32_bf16 v[148:151], a[236:239], a[140:143], v[148:151]\n\t"
      "s_waitcnt lgkmcnt(4)\n\t"
      "v_mfma_f32_16x16x32_bf16 v[88:91], a[224:227], a[144:147], v[88:91]\n\t"
      "ds_read_b64_tr_b16 a[236:237], v9 offset:28672\n\t"
      "v_mfma_f32_16x16x32_bf16 v[92:95], a[224:227], a[148:151], v[92:95]\n\t"
      "ds_read_b64_tr_b16 a[238:239], v9 offset:30720\n\t"
      "v_mfma_f32_16x16x32_bf16 v[96:99], a[224:227], a[152:155], v[96:99]\n\t"
      "v_mfma_f32_16x16x32_bf16 v[100:103], a[224:227], a[156:159], v[100:103]\n\t"
      "s_waitcnt vmcnt(14) lgkmcnt(4)\n\t"
      "s_barrier\n\t"
      "v_mfma_f32_16x16x32_bf16 v[104:107], a[228:231], a[144:147], v[104:107]\n\t"
      "ds_read_b64_tr_b16 a[224:225], v6 offset:32768\n\t"
      "s_add_u32 s42, %8, s63\n\t"
      "s_addc_u32 s43, %9, 0\n\t"
      "v_mfma_f32_16x16x32_bf16 v[108:111], a[228:231], a[148:151], v[108:111]\n\t"
      "ds_read_b64_tr_b16 a[226:227], v6 offset:34816\n\t"
      "s_add_u32 m0, s62, 65536\n\t"
      "v_mfma_f32_16x16x32_bf16 v[112:115], a[228:231], a[152:155], v[112:115]\n\t"
      "global_load_lds_dwordx4 v4, s[42:43]\n\t"
      "v_mfma_f32_16x16x32_bf16 v[116:119], a[228:231], a[156:159], v[116:119]\n\t"
      "s_waitcnt lgkmcnt(4)\n\t"
      "v_mfma_f32_16x16x32_bf16 v[120:123], a[232:235], a[144:147], v[120:123]\n\t"
      "ds_read_b64_tr_b16 a[228:229], v7 offset:32768\n\t"
      "v_mfma_f32_16x16x32_bf16 v[124:127], a[232:235], a[148:151], v[124:127]\n\t"
      "ds_read_b64_tr_b16 a[230:231], v7 offset:34816\n\t"
      "s_add_u32 m0, s62, 66560\n\t"
      "v_mfma_f32_16x16x32_bf16 v[128:131], a[232:235], a[152:155], v[128:131]\n\t"
      "global_load_lds_dwordx4 v5, s[42:43]\n\t"
      "v_mfma_f32_16x16x32_bf16 v[132:135], a[232:235], a[156:159], v[132:135]\n\t"
      "s_add_u32 s42, s42, 131072\n\t"
      "s_addc_u32 s43, s43, 0\n\t"
      "s_waitcnt lgkmcnt(4)\n\t"
      "v_mfma_f32_16x16x32_bf16 v[136:139], a[236:239], a[144:147], v[136:139]\n\t"
      "ds_read_b64_tr_b16 a[232:233], v8 offset:32768\n\t"
      "v_mfma_f32_16x16x32_bf16 v[140:143], a[236:239], a[148:151], v[140:143]\n\t"
      "ds_read_b64_tr_b16 a[234:235], v8 offset:34816\n\t"
      "s_add_u32 m0, s62, 73728\n\t"
      "v_mfma_f32_16x16x32_bf16 v[144:147], a[236:239], a[152:155], v[144:147]\n\t"
      "global_load_lds_dwordx4 v4, s[42:43]\n\t"
      "v_mfma_f32_16x16x32_bf16 v[148:151], a[236:239], a[156:159], v[148:151]\n\t"
      "s_waitcnt vmcnt(19) lgkmcnt(4)\n\t"
      "v_mfma_f32_16x16x32_bf16 v[24:27], a[224:227], a[64:67], v[24:27]\n\t"
      "ds_read_b64_tr_b16 a[236:237], v9 offset:32768\n\t"
      "v_mfma_f32_16x16x32_bf16 v[28:31], a[224:227], a[68:71], v[28:31]\n\t"
      "ds_read_b64_tr_b16 a[238:239], v9 offset:34816\n\t"
      "s_add_u32 m0, s62, 74752\n\t"
      "v_mfma_f32_16x16x32_bf16 v[32:35], a[224:227], a[72:75], v[32:35]\n\t"
      "global_load_lds_dwordx4 v5, s[42:43]\n\t"
      "v_mfma_f32_16x16x32_bf16 v[36:39], a[224:227], a[76:79], v[36:39]\n\t"
      "s_add_u32 s42, s42, 131072\n\t"
      "s_addc_u32 s43, s43, 0\n\t"
      "s_waitcnt lgkmcnt(4)\n\t"
      "v_mfma_f32_16x16x32_bf16 v[40:43], a[228:231], a[64:67], v[40:43]\n\t"
      "ds_read_b64_tr_b16 a[224:225], v6 offset:36864\n\t"
      "v_mfma_f32_16x16x32_bf16 v[44:47], a[228:231], a[68:71], v[44:47]\n\t"
      "ds_read_b64_tr_b16 a[226:227], v6 offset:38912\n\t"
      "s_add_u32 m0, s62, 81920\n\t"
      "v_mfma_f32_16x16x32_bf16 v[48:51], a[228:231], a[72:75], v[48:51]\n\t"
      "global_load_lds_dwordx4 v4, s[42:43]\n\t"
      "v_mfma_f32_16x16x32_bf16 v[52:55], a[228:231], a[76:79], v[52:55]\n\t"
      "s_waitcnt lgkmcnt(4)\n\t"
      "v_mfma_f32_16x16x32_bf16 v[56:59], a[232:235], a[64:67], v[56:59]\n\t"
      "ds_read_b64_tr_b16 a[228:229], v7 offset:36864\n\t"
      "v_mfma_f32_16x16x32_bf16 v[60:63], a[232:235], a[68:71], v[60:63]\n\t"
      "ds_read_b64_tr_b16 a[230:231], v7 offset:38912\n\t"
      "s_add_u32 m0, s62, 82944\n\t"
      "v_mfma_f32_16x16x32_bf16 v[64:67], a[232:235], a[72:75], v[64:67]\n\t"
      "global_load_lds_dwordx4 v5, s[42:43]\n\t"
      "v_mfma_f32_16x16x32_bf16 v[68:71], a[232:235], a[76:79], v[68:71]\n\t"
      "s_add_u32 s42, s42, 131072\n\t"
      "s_addc_u32 s43, s43, 0\n\t"
      "s_waitcnt lgkmcnt(4)\n\t"
      "v_mfma_f32_16x16x32_bf16 v[72:75], a[236:239], a[64:67], v[72:75]\n\t"
      "ds_read_b64_tr_b16 a[232:233], v8 offset:36864\n\t"
      "v_mfma_f32_16x16x32_bf16 v[76:79], a[236:239], a[68:71], v[76:79]\n\t"
      "ds_read_b64_tr_b16 a[234:235], v8 offset:38912\n\t"
      "s_add_u32 m0, s62, 90112\n\t"
      "v_mfma_f32_16x16x32_bf16 v[80:83], a[236:239], a[72:75], v[80:83]\n\t"
      "global_load_lds_dwordx4 v4, s[42:43]\n\t"
      "v_mfma_f32_16x16x32_bf16 v[84:87], a[236:239], a[76:79], v[84:87]\n\t"
      "s_waitcnt vmcnt(18) lgkmcnt(4)\n\t"
      "v_mfma_f32_16x16x32_bf16 v[24:27], a[224:227], a[80:83], v[24:27]\n\t"
      "ds_read_b64_tr_b16 a[236:237], v9 offset:36864\n\t"
      "v_mfma_f32_16x16x32_bf16 v[28:31], a[224:227], a[84:87], v[28:31]\n\t"
      "ds_read_b64_tr_b16 a[238:239], v9 offset:38912\n\t"
      "s_add_u32 m0, s62, 91136\n\t"
      "v_mfma_f32_16x16x32_bf16 v[32:35], a[224:227], a[88:91], v[32:35]\n\t"
      "global_load_lds_dwordx4 v5, s[42:43]\n\t"
      "v_mfma_f32_16x16x32_bf16 v[36:39], a[224:227], a[92:95], v[36:39]\n\t"
      "s_waitcnt lgkmcnt(4)\n\t"
      "v_mfma_f32_16x16x32_bf16 v[40:43], a[228:231], a[80:83], v[40:43]\n\t"
      "ds_read_b64_tr_b16 a[224:225], v6 offset:40960\n\t"
      "v_mfma_f32_16x16x32_bf16 v[44:47], a[228:231], a[84:87], v[44:47]\n\t"
      "ds_read_b64_tr_b16 a[226:227], v6 offset:43008\n\t"
      "v_mfma_f32_16x16x32_bf16 v[48:51], a[228:231], a[88:91], v[48:51]\n\t"
      "v_mfma_f32_16x16x32_bf16 v[52:55], a[228:231], a[92:95], v[52:55]\n\t"
      "s_waitcnt lgkmcnt(4)\n\t"
      "v_mfma_f32_16x16x32_bf16 v[56:59], a[232:235], a[80:83], v[56:59]\n\t"
      "ds_read_b64_tr_b16 a[228:229], v7 offset:40960\n\t"
      "v_mfma_f32_16x16x32_bf16 v[60:63], a[232:235], a[84:87], v[60:63]\n\t"
      "ds_read_b64_tr_b16 a[230:231], v7 offset:43008\n\t"
      "s_add_u32 m0, s61, 3072\n\t"
      "v_mfma_f32_16x16x32_bf16 v[64:67], a[232:235], a[88:91], v[64:67]\n\t"
      "buffer_load_dwordx4 v246, s[48:51], s55 offen lds\n\t"
      "v_mfma_f32_16x16x32_bf16 v[68:71], a[232:235], a[92:95], v[68:71]\n\t"
      "s_waitcnt lgkmcnt(4)\n\t"
      "v_mfma_f32_16x16x32_bf16 v[72:75], a[236:239], a[80:83], v[72:75]\n\t"
      "ds_read_b64_tr_b16 a[232:233], v8 offset:40960\n\t"
      "v_mfma_f32_16x16x32_bf16 v[76:79], a[236:239], a[84:87], v[76:79]\n\t"
      "ds_read_b64_tr_b16 a[234:235], v8 offset:43008\n\t"
      "v_mfma_f32_16x16x32_bf16 v[80:83], a[236:239], a[88:91], v[80:83]\n\t"
      "v_mfma_f32_16x16x32_bf16 v[84:87], a[236:239], a[92:95], v[84:87]\n\t"
      "s_waitcnt lgkmcnt(4)\n\t"
      "v_mfma_f32_16x16x32_bf16 v[88:91], a[224:227], a[160:163], v[88:91]\n\t"
      "ds_read_b64_tr_b16 a[236:237], v9 offset:40960\n\t"
      "v_mfma_f32_16x16x32_bf16 v[92:95], a[224:227], a[164:167], v[92:95]\n\t"
      "ds_read_b64_tr_b16 a[238:239], v9 offset:43008\n\t"
      "v_mfma_f32_16x16x32_bf16 v[96:99], a[224:227], a[168:171], v[96:99]\n\t"
      "v_mfma_f32_16x16x32_bf16 v[100:103], a[224:227], a[172:175], v[100:103]\n\t"
      "s_waitcnt lgkmcnt(4)\n\t"
      "v_mfma_f32_16x16x32_bf16 v[104:107], a[228:231], a[160:163], v[104:107]\n\t"
      "ds_read_b64_tr_b16 a[224:225], v6 offset:45056\n\t"
      "v_mfma_f32_16x16x32_bf16 v[108:111], a[228:231], a[164:167], v[108:111]\n\t"
      "ds_read_b64_tr_b16 a[226:227], v6 offset:47104\n\t"
      "v_mfma_f32_16x16x32_bf16 v[112:115], a[228:231], a[168:171], v[112:115]\n\t"
      "v_mfma_f32_16x16x32_bf16 v[116:119], a[228:231], a[172:175], v[116:119]\n\t"
      "s_waitcnt lgkmcnt(4)\n\t"
      "v_mfma_f32_16x16x32_bf16 v[120:123], a[232:235], a[160:163], v[120:123]\n\t"
      "ds_read_b64_tr_b16 a[228:229], v7 offset:45056\n\t"
      "v_mfma_f32_16x16x32_bf16 v[124:127], a[232:235], a[164:167], v[124:127]\n\t"
      "ds_read_b64_tr_b16 a[230:231], v7 offset:47104\n\t"
      "v_mfma_f32_16x16x32_bf16 v[128:131], a[232:235], a[168:171], v[128:131]\n\t"
      "v_mfma_f32_16x16x32_bf16 v[132:135], a[232:235], a[172:175], v[132:135]\n\t"
      "s_waitcnt lgkmcnt(4)\n\t"
      "v_mfma_f32_16x16x32_bf16 v[136:139], a[236:239], a[160:163], v[136:139]\n\t"
      "ds_read_b64_tr_b16 a[232:233], v8 offset:45056\n\t"
      "v_mfma_f32_16x16x32_bf16 v[140:143], a[236:239], a[164:167], v[140:143]\n\t"
      "ds_read_b64_tr_b16 a[234:235], v8 offset:47104\n\t"
      "v_mfma_f32_16x16x32_bf16 v[144:147], a[236:239], a[168:171], v[144:147]\n\t"
      "v_mfma_f32_16x16x32_bf16 v[148:151], a[236:239], a[172:175], v[148:151]\n\t"
      "s_waitcnt lgkmcnt(4)\n\t"
      "v_mfma_f32_16x16x32_bf16 v[88:91], a[224:227], a[176:179], v[88:91]\n\t"
      "ds_read_b64_tr_b16 a[236:237], v9 offset:45056\n\t"
      "v_mfma_f32_16x16x32_bf16 v[92:95], a[224:227], a[180:183], v[92:95]\n\t"
      "ds_read_b64_tr_b16 a[238:239], v9 offset:47104\n\t"
      "v_mfma_f32_16x16x32_bf16 v[96:99], a[224:227], a[184:187], v[96:99]\n\t"
      "v_mfma_f32_16x16x32_bf16 v[100:103], a[224:227], a[188:191], v[100:103]\n\t"
      "s_waitcnt lgkmcnt(4)\n\t"
      "v_mfma_f32_16x16x32_bf16 v[104:107], a[228:231], a[176:179], v[104:107]\n\t"
      "ds_read_b64_tr_b16 a[224:225], v6 offset:49152\n\t"
      "v_mfma_f32_16x16x32_bf16 v[108:111], a[228:231], a[180:183], v[108:111]\n\t"
      "ds_read_b64_tr_b16 a[226:227], v6 offset:51200\n\t"
      "v_mfma_f32_16x16x32_bf16 v[112:115], a[228:231], a[184:187], v[112:115]\n\t"
      "v_mfma_f32_16x16x32_bf16 v[116:119], a[228:231], a[188:191], v[116:119]\n\t"
      "s_waitcnt lgkmcnt(4)\n\t"
      "v_mfma_f32_16x16x32_bf16 v[120:123], a[232:235], a[176:179], v[120:123]\n\t"
      "ds_read_b64_tr_b16 a[228:229], v6 offset:53248\n\t"
      "v_mfma_f32_16x16x32_bf16 v[124:127], a[232:235], a[180:183], v[124:127]\n\t"
      "ds_read_b64_tr_b16 a[230:231], v6 offset:55296\n\t"
      "v_mfma_f32_16x16x32_bf16 v[128:131], a[232:235], a[184:187], v[128:131]\n\t"
      "v_mfma_f32_16x16x32_bf16 v[132:135], a[232:235], a[188:191], v[132:135]\n\t"
      "s_waitcnt lgkmcnt(4)\n\t"
      "v_mfma_f32_16x16x32_bf16 v[136:139], a[236:239], a[176:179], v[136:139]\n\t"
      "ds_read_b64_tr_b16 a[232:233], v7 offset:49152\n\t"
      "v_mfma_f32_16x16x32_bf16 v[140:143], a[236:239], a[180:183], v[140:143]\n\t"
      "ds_read_b64_tr_b16 a[234:235], v7 offset:51200\n\t"
      "v_mfma_f32_16x16x32_bf16 v[144:147], a[236:239], a[184:187], v[144:147]\n\t"
      "v_mfma_f32_16x16x32_bf16 v[148:151], a[236:239], a[188:191], v[148:151]\n\t"
      "s_waitcnt vmcnt(16) lgkmcnt(4)\n\t"
      "v_mfma_f32_16x16x32_bf16 v[24:27], a[224:227], a[96:99], v[24:27]\n\t"
      "ds_read_b64_tr_b16 a[236:237], v7 offset:53248\n\t"
      "v_mfma_f32_16x16x32_bf16 v[28:31], a[224:227], a[100:103], v[28:31]\n\t"
      "ds_read_b64_tr_b16 a[238:239], v7 offset:55296\n\t"
      "v_mfma_f32_16x16x32_bf16 v[32:35], a[224:227], a[104:107], v[32:35]\n\t"
      "v_mfma_f32_16x16x32_bf16 v[36:39], a[224:227], a[108:111], v[36:39]\n\t"
      "s_waitcnt vmcnt(12) lgkmcnt(4)\n\t"
      "v_mfma_f32_16x16x32_bf16 v[24:27], a[228:231], a[112:115], v[24:27]\n\t"
      "ds_read_b64_tr_b16 a[224:225], v8 offset:49152\n\t"
      "v_mfma_f32_16x16x32_bf16 v[28:31], a[228:231], a[116:119], v[28:31]\n\t"
      "ds_read_b64_tr_b16 a[226:227], v8 offset:51200\n\t"
      "v_mfma_f32_16x16x32_bf16 v[32:35], a[228:231], a[120:123], v[32:35]\n\t"
      "v_mfma_f32_16x16x32_bf16 v[36:39], a[228:231], a[124:127], v[36:39]\n\t"
      "s_waitcnt lgkmcnt(4)\n\t"
      "v_mfma_f32_16x16x32_bf16 v[40:43], a[232:235], a[96:99], v[40:43]\n\t"
      "ds_read_b64_tr_b16 a[228:229], v8 offset:53248\n\t"
      "v_mfma_f32_16x16x32_bf16 v[44:47], a[232:235], a[100:103], v[44:47]\n\t"
      "ds_read_b64_tr_b16 a[230:231], v8 offset:55296\n\t"
      "v_mfma_f32_16x16x32_bf16 v[48:51], a[232:235], a[104:107], v[48:51]\n\t"
      "v_mfma_f32_16x16x32_bf16 v[52:55], a[232:235], a[108:111], v[52:55]\n\t"
      "s_waitcnt lgkmcnt(4)\n\t"
      "v_mfma_f32_16x16x32_bf16 v[40:43], a[236:239], a[112:115], v[40:43]\n\t"
      "ds_read_b64_tr_b16 a[232:233], v9 offset:49152\n\t"
      "v_mfma_f32_16x16x32_bf16 v[44:47], a[236:239], a[116:119], v[44:47]\n\t"
      "ds_read_b64_tr_b16 a[234:235], v9 offset:51200\n\t"
      "v_mfma_f32_16x16x32_bf16 v[48:51], a[236:239], a[120:123], v[48:51]\n\t"
      "v_mfma_f32_16x16x32_bf16 v[52:55], a[236:239], a[124:127], v[52:55]\n\t"
      "s_waitcnt lgkmcnt(4)\n\t"
      "v_mfma_f32_16x16x32_bf16 v[56:59], a[224:227], a[96:99], v[56:59]\n\t"
      "ds_read_b64_tr_b16 a[236:237], v9 offset:53248\n\t"
      "v_mfma_f32_16x16x32_bf16 v[60:63], a[224:227], a[100:103], v[60:63]\n\t"
      "ds_read_b64_tr_b16 a[238:239], v9 offset:55296\n\t"
      "v_mfma_f32_16x16x32_bf16 v[64:67], a[224:227], a[104:107], v[64:67]\n\t"
      "v_mfma_f32_16x16x32_bf16 v[68:71], a[224:227], a[108:111], v[68:71]\n\t"
      "s_waitcnt lgkmcnt(4)\n\t"
      "v_mfma_f32_16x16x32_bf16 v[56:59], a[228:231], a[112:115], v[56:59]\n\t"
      "ds_read_b64_tr_b16 a[224:225], v6 offset:57344\n\t"
      "v_mfma_f32_16x16x32_bf16 v[60:63], a[228:231], a[116:119], v[60:63]\n\t"
      "ds_read_b64_tr_b16 a[226:227], v6 offset:59392\n\t"
      "v_mfma_f32_16x16x32_bf16 v[64:67], a[228:231], a[120:123], v[64:67]\n\t"
      "v_cvt_pk_bf16_f32 v24, v24, v25\n\t"
      "v_mfma_f32_16x16x32_bf16 v[68:71], a[228:231], a[124:127], v[68:71]\n\t"
      "s_waitcnt lgkmcnt(4)\n\t"
      "v_mfma_f32_16x16x32_bf16 v[72:75], a[232:235], a[96:99], v[72:75]\n\t"
      "ds_read_b64_tr_b16 a[228:229], v7 offset:57344\n\t"
      "v_mfma_f32_16x16x32_bf16 v[76:79], a[232:235], a[100:103], v[76:79]\n\t"
      "ds_read_b64_tr_b16 a[230:231], v7 offset:59392\n\t"
      "v_mfma_f32_16x16x32_bf16 v[80:83], a[232:235], a[104:107], v[80:83]\n\t"
      "v_cvt_pk_bf16_f32 v25, v26, v27\n\t"
      "v_mfma_f32_16x16x32_bf16 v[84:87], a[232:235], a[108:111], v[84:87]\n\t"
      "v_cvt_pk_bf16_f32 v26, v40, v41\n\t"
      "s_waitcnt lgkmcnt(4)\n\t"
      "v_mfma_f32_16x16x32_bf16 v[72:75], a[236:239], a[112:115], v[72:75]\n\t"
      "ds_read_b64_tr_b16 a[232:233], v8 offset:57344\n\t"
      "v_mfma_f32_16x16x32_bf16 v[76:79], a[236:239], a[116:119], v[76:79]\n\t"
      "ds_read_b64_tr_b16 a[234:235], v8 offset:59392\n\t"
      "v_mfma_f32_16x16x32_bf16 v[80:83], a[236:239], a[120:123], v[80:83]\n\t"
      "v_cvt_pk_bf16_f32 v27, v42, v43\n\t"
      "v_mfma_f32_16x16x32_bf16 v[84:87], a[236:239], a[124:127], v[84:87]\n\t"
      "s_waitcnt lgkmcnt(4)\n\t"
      "v_mfma_f32_16x16x32_bf16 v[88:91], a[224:227], a[192:195], v[88:91]\n\t"
      "ds_read_b64_tr_b16 a[236:237], v9 offset:57344\n\t"
      "v_mfma_f32_16x16x32_bf16 v[92:95], a[224:227], a[196:199], v[92:95]\n\t"
      "ds_read_b64_tr_b16 a[238:239], v9 offset:59392\n\t"
      "v_mfma_f32_16x16x32_bf16 v[96:99], a[224:227], a[200:203], v[96:99]\n\t"
      "buffer_store_dwordx4 v[24:27], v1, s[44:47], s68 offen offset:0 sc1\n\t"
      "v_mfma_f32_16x16x32_bf16 v[100:103], a[224:227], a[204:207], v[100:103]\n\t"
      "v_cvt_pk_bf16_f32 v28, v28, v29\n\t"
      "s_waitcnt lgkmcnt(4)\n\t"
      "v_mfma_f32_16x16x32_bf16 v[104:107], a[228:231], a[192:195], v[104:107]\n\t"
      "ds_read_b64_tr_b16 a[224:225], v6 offset:61440\n\t"
      "v_mfma_f32_16x16x32_bf16 v[108:111], a[228:231], a[196:199], v[108:111]\n\t"
      "ds_read_b64_tr_b16 a[226:227], v6 offset:63488\n\t"
      "v_mfma_f32_16x16x32_bf16 v[112:115], a[228:231], a[200:203], v[112:115]\n\t"
      "v_cvt_pk_bf16_f32 v29, v30, v31\n\t"
      "v_mfma_f32_16x16x32_bf16 v[116:119], a[228:231], a[204:207], v[116:119]\n\t"
      "v_cvt_pk_bf16_f32 v30, v44, v45\n\t"
      "s_waitcnt lgkmcnt(4)\n\t"
      "v_mfma_f32_16x16x32_bf16 v[120:123], a[232:235], a[192:195], v[120:123]\n\t"
      "ds_read_b64_tr_b16 a[228:229], v7 offset:61440\n\t"
      "v_mfma_f32_16x16x32_bf16 v[124:127], a[232:235], a[196:199], v[124:127]\n\t"
      "ds_read_b64_tr_b16 a[230:231], v7 offset:63488\n\t"
      "v_mfma_f32_16x16x32_bf16 v[128:131], a[232:235], a[200:203], v[128:131]\n\t"
      "v_cvt_pk_bf16_f32 v31, v46, v47\n\t"
      "v_mfma_f32_16x16x32_bf16 v[132:135], a[232:235], a[204:207], v[132:135]\n\t"
      "buffer_store_dwordx4 v[28:31], v1, s[44:47], s68 offen offset:1024 sc1\n\t"
      "s_waitcnt lgkmcnt(4)\n\t"
      "v_mfma_f32_16x16x32_bf16 v[136:139], a[236:239], a[192:195], v[136:139]\n\t"
      "ds_read_b64_tr_b16 a[232:233], v8 offset:61440\n\t"
      "v_mfma_f32_16x16x32_bf16 v[140:143], a[236:239], a[196:199], v[140:143]\n\t"
      "ds_read_b64_tr_b16 a[234:235], v8 offset:63488\n\t"
      "v_mfma_f32_16x16x32_bf16 v[144:147], a[236:239], a[200:203], v[144:147]\n\t"
      "v_cvt_pk_bf16_f32 v32, v32, v33\n\t"
      "v_mfma_f32_16x16x32_bf16 v[148:151], a[236:239], a[204:207], v[148:151]\n\t"
      "s_waitcnt lgkmcnt(4)\n\t"
      "v_mfma_f32_16x16x32_bf16 v[88:91], a[224:227], a[208:211], v[88:91]\n\t"
      "ds_read_b64_tr_b16 a[236:237], v9 offset:61440\n\t"
      "v_mfma_f32_16x16x32_bf16 v[92:95], a[224:227], a[212:215], v[92:95]\n\t"
      "ds_read_b64_tr_b16 a[238:239], v9 offset:63488\n\t"
      "v_mfma_f32_16x16x32_bf16 v[96:99], a[224:227], a[216:219], v[96:99]\n\t"
      "v_cvt_pk_bf16_f32 v33, v34, v35\n\t"
      "v_mfma_f32_16x16x32_bf16 v[100:103], a[224:227], a[220:223], v[100:103]\n\t"
      "v_cvt_pk_bf16_f32 v34, v48, v49\n\t"
      "s_waitcnt vmcnt(3) lgkmcnt(4)\n\t"
      "s_barrier\n\t"
      "v_mfma_f32_16x16x32_bf16 v[104:107], a[228:231], a[208:211], v[104:107]\n\t"
      "ds_read_b64_tr_b16 a[224:225], v152 offset:0\n\t"
      "v_mfma_f32_16x16x32_bf16 v[108:111], a[228:231], a[212:215], v[108:111]\n\t"
      "ds_read_b64_tr_b16 a[226:227], v152 offset:2048\n\t"
      "v_mfma_f32_16x16x32_bf16 v[112:115], a[228:231], a[216:219], v[112:115]\n\t"
      "v_cvt_pk_bf16_f32 v35, v50, v51\n\t"
      "v_mfma_f32_16x16x32_bf16 v[116:119], a[228:231], a[220:223], v[116:119]\n\t"
      "buffer_store_dwordx4 v[32:35], v1, s[44:47], s68 offen offset:2048 sc1\n\t"
      "s_waitcnt lgkmcnt(4)\n\t"
      "v_mfma_f32_16x16x32_bf16 v[120:123], a[232:235], a[208:211], v[120:123]\n\t"
      "ds_read_b64_tr_b16 a[228:229], v153 offset:0\n\t"
      "v_mfma_f32_16x16x32_bf16 v[124:127], a[232:235], a[212:215], v[124:127]\n\t"
      "ds_read_b64_tr_b16 a[230:231], v153 offset:2048\n\t"
      "v_mfma_f32_16x16x32_bf16 v[128:131], a[232:235], a[216:219], v[128:131]\n\t"
      "v_cvt_pk_bf16_f32 v36, v36, v37\n\t"
      "v_mfma_f32_16x16x32_bf16 v[132:135], a[232:235], a[220:223], v[132:135]\n\t"
      "v_cvt_pk_bf16_f32 v37, v38, v39\n\t"
      "s_waitcnt lgkmcnt(4)\n\t"
      "v_mfma_f32_16x16x32_bf16 v[136:139], a[236:239], a[208:211], v[136:139]\n\t"
      "ds_read_b64_tr_b16 a[232:233], v152 offset:4096\n\t"
      "v_mfma_f32_16x16x32_bf16 v[140:143], a[236:239], a[212:215], v[140:143]\n\t"
      "ds_read_b64_tr_b16 a[234:235], v152 offset:6144\n\t"
      "v_mfma_f32_16x16x32_bf16 v[144:147], a[236:239], a[216:219], v[144:147]\n\t"
      "v_cvt_pk_bf16_f32 v38, v52, v53\n\t"
      "v_mfma_f32_16x16x32_bf16 v[148:151], a[236:239], a[220:223], v[148:151]\n\t"
      "s_waitcnt lgkmcnt(4)\n\t"
      "v_mfma_f32_16x16x32_bf16 v[88:91], a[224:227], a[0:3], v[88:91]\n\t"
      "ds_read_b64_tr_b16 a[236:237], v153 offset:4096\n\t"
      "v_mfma_f32_16x16x32_bf16 v[92:95], a[224:227], a[4:7], v[92:95]\n\t"
      "ds_read_b64_tr_b16 a[238:239], v153 offset:6144\n\t"
      "v_mfma_f32_16x16x32_bf16 v[96:99], a[224:227], a[8:11], v[96:99]\n\t"
      "v_cvt_pk_bf16_f32 v39, v54, v55\n\t"
      "v_mfma_f32_16x16x32_bf16 v[100:103], a[224:227], a[12:15], v[100:103]\n\t"
      "buffer_store_dwordx4 v[36:39], v1, s[44:47], s68 offen offset:3072 sc1\n\t"
      "s_waitcnt lgkmcnt(4)\n\t"
      "v_mfma_f32_16x16x32_bf16 v[104:107], a[228:231], a[0:3], v[104:107]\n\t"
      "ds_read_b64_tr_b16 a[224:225], v152 offset:8192\n\t"
      "v_mfma_f32_16x16x32_bf16 v[108:111], a[228:231], a[4:7], v[108:111]\n\t"
      "ds_read_b64_tr_b16 a[226:227], v152 offset:10240\n\t"
      "v_mfma_f32_16x16x32_bf16 v[112:115], a[228:231], a[8:11], v[112:115]\n\t"
      "v_cvt_pk_bf16_f32 v56, v56, v57\n\t"
      "v_mfma_f32_16x16x32_bf16 v[116:119], a[228:231], a[12:15], v[116:119]\n\t"
      "v_cvt_pk_bf16_f32 v57, v58, v59\n\t"
      "s_waitcnt lgkmcnt(4)\n\t"
      "v_mfma_f32_16x16x32_bf16 v[88:91], a[232:235], a[16:19], v[88:91]\n\t"
      "ds_read_b64_tr_b16 a[228:229], v153 offset:8192\n\t"
      "v_mfma_f32_16x16x32_bf16 v[92:95], a[232:235], a[20:23], v[92:95]\n\t"
      "ds_read_b64_tr_b16 a[230:231], v153 offset:10240\n\t"
      "v_mfma_f32_16x16x32_bf16 v[96:99], a[232:235], a[24:27], v[96:99]\n\t"
      "v_cvt_pk_bf16_f32 v58, v72, v73\n\t"
      "v_mfma_f32_16x16x32_bf16 v[100:103], a[232:235], a[28:31], v[100:103]\n\t"
      "s_waitcnt lgkmcnt(4)\n\t"
      "v_mfma_f32_16x16x32_bf16 v[104:107], a[236:239], a[16:19], v[104:107]\n\t"
      "ds_read_b64_tr_b16 a[232:233], v152 offset:12288\n\t"
      "v_mfma_f32_16x16x32_bf16 v[108:111], a[236:239], a[20:23], v[108:111]\n\t"
      "ds_read_b64_tr_b16 a[234:235], v152 offset:14336\n\t"
      "v_mfma_f32_16x16x32_bf16 v[112:115], a[236:239], a[24:27], v[112:115]\n\t"
      "v_cvt_pk_bf16_f32 v59, v74, v75\n\t"
      "v_mfma_f32_16x16x32_bf16 v[116:119], a[236:239], a[28:31], v[116:119]\n\t"
      "buffer_store_dwordx4 v[56:59], v1, s[44:47], s69 offen offset:0 sc1\n\t"
      "s_waitcnt lgkmcnt(4)\n\t"
      "v_mfma_f32_16x16x32_bf16 v[88:91], a[224:227], a[32:35], v[88:91]\n\t"
      "ds_read_b64_tr_b16 a[236:237], v153 offset:12288\n\t"
      "v_mfma_f32_16x16x32_bf16 v[92:95], a[224:227], a[36:39], v[92:95]\n\t"
      "ds_read_b64_tr_b16 a[238:239], v153 offset:14336\n\t"
      "v_mfma_f32_16x16x32_bf16 v[96:99], a[224:227], a[40:43], v[96:99]\n\t"
      "v_cvt_pk_bf16_f32 v60, v60, v61\n\t"
      "v_mfma_f32_16x16x32_bf16 v[100:103], a[224:227], a[44:47], v[100:103]\n\t"
      "v_cvt_pk_bf16_f32 v61, v62, v63\n\t"
      "s_waitcnt lgkmcnt(4)\n\t"
      "v_mfma_f32_16x16x32_bf16 v[104:107], a[228:231], a[32:35], v[104:107]\n\t"
      "ds_read_b64_tr_b16 a[224:225], v152 offset:16384\n\t"
      "v_mfma_f32_16x16x32_bf16 v[108:111], a[228:231], a[36:39], v[108:111]\n\t"
      "ds_read_b64_tr_b16 a[226:227], v152 offset:18432\n\t"
      "v_mfma_f32_16x16x32_bf16 v[112:115], a[228:231], a[40:43], v[112:115]\n\t"
      "v_cvt_pk_bf16_f32 v62, v76, v77\n\t"
      "v_mfma_f32_16x16x32_bf16 v[116:119], a[228:231], a[44:47], v[116:119]\n\t"
      "s_waitcnt lgkmcnt(4)\n\t"
      "v_mfma_f32_16x16x32_bf16 v[88:91], a[232:235], a[48:51], v[88:91]\n\t"
      "ds_read_b64_tr_b16 a[228:229], v153 offset:16384\n\t"
      "v_mfma_f32_16x16x32_bf16 v[92:95], a[232:235], a[52:55], v[92:95]\n\t"
      "ds_read_b64_tr_b16 a[230:231], v153 offset:18432\n\t"
      "v_mfma_f32_16x16x32_bf16 v[96:99], a[232:235], a[56:59], v[96:99]\n\t"
      "v_cvt_pk_bf16_f32 v63, v78, v79\n\t"
      "v_mfma_f32_16x16x32_bf16 v[100:103], a[232:235], a[60:63], v[100:103]\n\t"
      "buffer_store_dwordx4 v[60:63], v1, s[44:47], s69 offen offset:1024 sc1\n\t"
      "s_waitcnt lgkmcnt(4)\n\t"
      "v_mfma_f32_16x16x32_bf16 v[104:107], a[236:239], a[48:51], v[104:107]\n\t"
      "ds_read_b64_tr_b16 a[232:233], v152 offset:20480\n\t"
      "v_mfma_f32_16x16x32_bf16 v[108:111], a[236:239], a[52:55], v[108:111]\n\t"
      "ds_read_b64_tr_b16 a[234:235], v152 offset:22528\n\t"
      "v_mfma_f32_16x16x32_bf16 v[112:115], a[236:239], a[56:59], v[112:115]\n\t"
      "v_cvt_pk_bf16_f32 v64, v64, v65\n\t"
      "v_mfma_f32_16x16x32_bf16 v[116:119], a[236:239], a[60:63], v[116:119]\n\t"
      "v_cvt_pk_bf16_f32 v65, v66, v67\n\t"
      "s_waitcnt lgkmcnt(4)\n\t"
      "v_mfma_f32_16x16x32_bf16 v[88:91], a[224:227], a[64:67], v[88:91]\n\t"
      "ds_read_b64_tr_b16 a[236:237], v153 offset:20480\n\t"
      "v_mfma_f32_16x16x32_bf16 v[92:95], a[224:227], a[68:71], v[92:95]\n\t"
      "ds_read_b64_tr_b16 a[238:239], v153 offset:22528\n\t"
      "v_mfma_f32_16x16x32_bf16 v[96:99], a[224:227], a[72:75], v[96:99]\n\t"
      "v_cvt_pk_bf16_f32 v66, v80, v81\n\t"
      "v_mfma_f32_16x16x32_bf16 v[100:103], a[224:227], a[76:79], v[100:103]\n\t"
      "s_waitcnt lgkmcnt(4)\n\t"
      "v_mfma_f32_16x16x32_bf16 v[104:107], a[228:231], a[64:67], v[104:107]\n\t"
      "ds_read_b64_tr_b16 a[224:225], v152 offset:24576\n\t"
      "v_mfma_f32_16x16x32_bf16 v[108:111], a[228:231], a[68:71], v[108:111]\n\t"
      "ds_read_b64_tr_b16 a[226:227], v152 offset:26624\n\t"
      "v_mfma_f32_16x16x32_bf16 v[112:115], a[228:231], a[72:75], v[112:115]\n\t"
      "v_cvt_pk_bf16_f32 v67, v82, v83\n\t"
      "v_mfma_f32_16x16x32_bf16 v[116:119], a[228:231], a[76:79], v[116:119]\n\t"
      "buffer_store_dwordx4 v[64:67], v1, s[44:47], s69 offen offset:2048 sc1\n\t"
      "s_waitcnt lgkmcnt(4)\n\t"
      "v_mfma_f32_16x16x32_bf16 v[88:91], a[232:235], a[80:83], v[88:91]\n\t"
      "ds_read_b64_tr_b16 a[228:229], v153 offset:24576\n\t"
      "v_mfma_f32_16x16x32_bf16 v[92:95], a[232:235], a[84:87], v[92:95]\n\t"
      "ds_read_b64_tr_b16 a[230:231], v153 offset:26624\n\t"
      "v_mfma_f32_16x16x32_bf16 v[96:99], a[232:235], a[88:91], v[96:99]\n\t"
      "v_cvt_pk_bf16_f32 v68, v68, v69\n\t"
      "v_mfma_f32_16x16x32_bf16 v[100:103], a[232:235], a[92:95], v[100:103]\n\t"
      "v_cvt_pk_bf16_f32 v69, v70, v71\n\t"
      "s_waitcnt lgkmcnt(4)\n\t"
      "v_mfma_f32_16x16x32_bf16 v[104:107], a[236:239], a[80:83], v[104:107]\n\t"
      "ds_read_b64_tr_b16 a[232:233], v152 offset:28672\n\t"
      "v_mfma_f32_16x16x32_bf16 v[108:111], a[236:239], a[84:87], v[108:111]\n\t"
      "ds_read_b64_tr_b16 a[234:235], v152 offset:30720\n\t"
      "v_mfma_f32_16x16x32_bf16 v[112:115], a[236:239], a[88:91], v[112:115]\n\t"
      "v_cvt_pk_bf16_f32 v70, v84, v85\n\t"
      "v_mfma_f32_16x16x32_bf16 v[116:119], a[236:239], a[92:95], v[116:119]\n\t"
      "s_waitcnt lgkmcnt(4)\n\t"
      "v_mfma_f32_16x16x32_bf16 v[88:91], a[224:227], a[96:99], v[88:91]\n\t"
      "ds_read_b64_tr_b16 a[236:237], v153 offset:28672\n\t"
      "v_mfma_f32_16x16x32_bf16 v[92:95], a[224:227], a[100:103], v[92:95]\n\t"
      "ds_read_b64_tr_b16 a[238:239], v153 offset:30720\n\t"
      "v_mfma_f32_16x16x32_bf16 v[96:99], a[224:227], a[104:107], v[96:99]\n\t"
      "v_cvt_pk_bf16_f32 v71, v86, v87\n\t"
      "v_mfma_f32_16x16x32_bf16 v[100:103], a[224:227], a[108:111], v[100:103]\n\t"
      "buffer_store_dwordx4 v[68:71], v1, s[44:47], s69 offen offset:3072 sc1\n\t"
      "s_waitcnt vmcnt(4) lgkmcnt(4)\n\t"
      "s_mov_b64 exec, 0xff\n\t"
      "buffer_store_dwordx4 a[252:255], v1, s[44:47], s71 offen sc1\n\t"
      "s_mov_b64 exec, -1\n\t"
      "v_mfma_f32_16x16x32_bf16 v[104:107], a[228:231], a[96:99], v[104:107]\n\t"
      "ds_read_b64_tr_b16 a[224:225], v154 offset:0\n\t"
      "v_mfma_f32_16x16x32_bf16 v[108:111], a[228:231], a[100:103], v[108:111]\n\t"
      "ds_read_b64_tr_b16 a[226:227], v154 offset:2048\n\t"
      "v_mfma_f32_16x16x32_bf16 v[112:115], a[228:231], a[104:107], v[112:115]\n\t"
      "v_mfma_f32_16x16x32_bf16 v[116:119], a[228:231], a[108:111], v[116:119]\n\t"
      "s_waitcnt lgkmcnt(4)\n\t"
      "v_mfma_f32_16x16x32_bf16 v[88:91], a[232:235], a[112:115], v[88:91]\n\t"
      "ds_read_b64_tr_b16 a[228:229], v155 offset:0\n\t"
      "v_mfma_f32_16x16x32_bf16 v[92:95], a[232:235], a[116:119], v[92:95]\n\t"
      "ds_read_b64_tr_b16 a[230:231], v155 offset:2048\n\t"
      "v_mfma_f32_16x16x32_bf16 v[96:99], a[232:235], a[120:123], v[96:99]\n\t"
      "v_mfma_f32_16x16x32_bf16 v[100:103], a[232:235], a[124:127], v[100:103]\n\t"
      "s_waitcnt lgkmcnt(4)\n\t"
      "v_mfma_f32_16x16x32_bf16 v[104:107], a[236:239], a[112:115], v[104:107]\n\t"
      "ds_read_b64_tr_b16 a[232:233], v154 offset:4096\n\t"
      "v_mfma_f32_16x16x32_bf16 v[108:111], a[236:239], a[116:119], v[108:111]\n\t"
      "ds_read_b64_tr_b16 a[234:235], v154 offset:6144\n\t"
      "v_mfma_f32_16x16x32_bf16 v[112:115], a[236:239], a[120:123], v[112:115]\n\t"
      "v_mfma_f32_16x16x32_bf16 v[116:119], a[236:239], a[124:127], v[116:119]\n\t"
      "s_waitcnt lgkmcnt(4)\n\t"
      "v_mfma_f32_16x16x32_bf16 v[120:123], a[224:227], a[0:3], v[120:123]\n\t"
      "ds_read_b64_tr_b16 a[236:237], v155 offset:4096\n\t"
      "v_mfma_f32_16x16x32_bf16 v[124:127], a[224:227], a[4:7], v[124:127]\n\t"
      "ds_read_b64_tr_b16 a[238:239], v155 offset:6144\n\t"
      "v_mfma_f32_16x16x32_bf16 v[128:131], a[224:227], a[8:11], v[128:131]\n\t"
      "v_mfma_f32_16x16x32_bf16 v[132:135], a[224:227], a[12:15], v[132:135]\n\t"
      "s_waitcnt lgkmcnt(4)\n\t"
      "v_mfma_f32_16x16x32_bf16 v[136:139], a[228:231], a[0:3], v[136:139]\n\t"
      "ds_read_b64_tr_b16 a[224:225], v154 offset:8192\n\t"
      "v_mfma_f32_16x16x32_bf16 v[140:143], a[228:231], a[4:7], v[140:143]\n\t"
      "ds_read_b64_tr_b16 a[226:227], v154 offset:10240\n\t"
      "v_mfma_f32_16x16x32_bf16 v[144:147], a[228:231], a[8:11], v[144:147]\n\t"
      "v_cvt_pk_bf16_f32 v88, v88, v89\n\t"
      "v_mfma_f32_16x16x32_bf16 v[148:151], a[228:231], a[12:15], v[148:151]\n\t"
      "s_waitcnt lgkmcnt(4)\n\t"
      "v_mfma_f32_16x16x32_bf16 v[120:123], a[232:235], a[16:19], v[120:123]\n\t"
      "ds_read_b64_tr_b16 a[228:229], v155 offset:8192\n\t"
      "v_mfma_f32_16x16x32_bf16 v[124:127], a[232:235], a[20:23], v[124:127]\n\t"
      "ds_read_b64_tr_b16 a[230:231], v155 offset:10240\n\t"
      "v_mfma_f32_16x16x32_bf16 v[128:131], a[232:235], a[24:27], v[128:131]\n\t"
      "v_cvt_pk_bf16_f32 v89, v90, v91\n\t"
      "v_mfma_f32_16x16x32_bf16 v[132:135], a[232:235], a[28:31], v[132:135]\n\t"
      "v_cvt_pk_bf16_f32 v90, v104, v105\n\t"
      "s_waitcnt lgkmcnt(4)\n\t"
      "v_mfma_f32_16x16x32_bf16 v[136:139], a[236:239], a[16:19], v[136:139]\n\t"
      "ds_read_b64_tr_b16 a[232:233], v154 offset:12288\n\t"
      "v_mfma_f32_16x16x32_bf16 v[140:143], a[236:239], a[20:23], v[140:143]\n\t"
      "ds_read_b64_tr_b16 a[234:235], v154 offset:14336\n\t"
      "v_mfma_f32_16x16x32_bf16 v[144:147], a[236:239], a[24:27], v[144:147]\n\t"
      "v_cvt_pk_bf16_f32 v91, v106, v107\n\t"
      "v_mfma_f32_16x16x32_bf16 v[148:151], a[236:239], a[28:31], v[148:151]\n\t"
      "buffer_store_dwordx4 v[88:91], v1, s[44:47], s70 offen offset:0 sc1\n\t"
      "s_waitcnt lgkmcnt(4)\n\t"
      "v_mfma_f32_16x16x32_bf16 v[120:123], a[224:227], a[32:35], v[120:123]\n\t"
      "ds_read_b64_tr_b16 a[236:237], v155 offset:12288\n\t"
      "v_mfma_f32_16x16x32_bf16 v[124:127], a[224:227], a[36:39], v[124:127]\n\t"
      "ds_read_b64_tr_b16 a[238:239], v155 offset:14336\n\t"
      "v_mfma_f32_16x16x32_bf16 v[128:131], a[224:227], a[40:43], v[128:131]\n\t"
      "v_cvt_pk_bf16_f32 v92, v92, v93\n\t"
      "v_mfma_f32_16x16x32_bf16 v[132:135], a[224:227], a[44:47], v[132:135]\n\t"
      "v_cvt_pk_bf16_f32 v93, v94, v95\n\t"
      "s_waitcnt lgkmcnt(4)\n\t"
      "v_mfma_f32_16x16x32_bf16 v[136:139], a[228:231], a[32:35], v[136:139]\n\t"
      "ds_read_b64_tr_b16 a[224:225], v154 offset:16384\n\t"
      "v_mfma_f32_16x16x32_bf16 v[140:143], a[228:231], a[36:39], v[140:143]\n\t"
      "ds_read_b64_tr_b16 a[226:227], v154 offset:18432\n\t"
      "v_mfma_f32_16x16x32_bf16 v[144:147], a[228:231], a[40:43], v[144:147]\n\t"
      "v_cvt_pk_bf16_f32 v94, v108, v109\n\t"
      "v_mfma_f32_16x16x32_bf16 v[148:151], a[228:231], a[44:47], v[148:151]\n\t"
      "v_cvt_pk_bf16_f32 v95, v110, v111\n\t"
      "s_waitcnt lgkmcnt(4)\n\t"
      "v_mfma_f32_16x16x32_bf16 v[120:123], a[232:235], a[48:51], v[120:123]\n\t"
      "ds_read_b64_tr_b16 a[228:229], v155 offset:16384\n\t"
      "v_mfma_f32_16x16x32_bf16 v[124:127], a[232:235], a[52:55], v[124:127]\n\t"
      "ds_read_b64_tr_b16 a[230:231], v155 offset:18432\n\t"
      "v_mfma_f32_16x16x32_bf16 v[128:131], a[232:235], a[56:59], v[128:131]\n\t"
      "buffer_store_dwordx4 v[92:95], v1, s[44:47], s70 offen offset:1024 sc1\n\t"
      "v_mfma_f32_16x16x32_bf16 v[132:135], a[232:235], a[60:63], v[132:135]\n\t"
      "v_cvt_pk_bf16_f32 v96, v96, v97\n\t"
      "s_waitcnt lgkmcnt(4)\n\t"
      "v_mfma_f32_16x16x32_bf16 v[136:139], a[236:239], a[48:51], v[136:139]\n\t"
      "ds_read_b64_tr_b16 a[232:233], v154 offset:20480\n\t"
      "v_mfma_f32_16x16x32_bf16 v[140:143], a[236:239], a[52:55], v[140:143]\n\t"
      "ds_read_b64_tr_b16 a[234:235], v154 offset:22528\n\t"
      "v_mfma_f32_16x16x32_bf16 v[144:147], a[236:239], a[56:59], v[144:147]\n\t"
      "v_cvt_pk_bf16_f32 v97, v98, v99\n\t"
      "v_mfma_f32_16x16x32_bf16 v[148:151], a[236:239], a[60:63], v[148:151]\n\t"
      "v_cvt_pk_bf16_f32 v98, v112, v113\n\t"
      "s_waitcnt lgkmcnt(4)\n\t"
      "v_mfma_f32_16x16x32_bf16 v[120:123], a[224:227], a[64:67], v[120:123]\n\t"
      "ds_read_b64_tr_b16 a[236:237], v155 offset:20480\n\t"
      "v_mfma_f32_16x16x32_bf16 v[124:127], a[224:227], a[68:71], v[124:127]\n\t"
      "ds_read_b64_tr_b16 a[238:239], v155 offset:22528\n\t"
      "v_mfma_f32_16x16x32_bf16 v[128:131], a[224:227], a[72:75], v[128:131]\n\t"
      "v_cvt_pk_bf16_f32 v99, v114, v115\n\t"
      "v_mfma_f32_16x16x32_bf16 v[132:135], a[224:227], a[76:79], v[132:135]\n\t"
      "buffer_store_dwordx4 v[96:99], v1, s[44:47], s70 offen offset:2048 sc1\n\t"
      "v_cvt_pk_bf16_f32 v100, v100, v101\n\t"
      "v_cvt_pk_bf16_f32 v101, v102, v103\n\t"
      "v_cvt_pk_bf16_f32 v102, v116, v117\n\t"
      "v_cvt_pk_bf16_f32 v103, v118, v119\n\t"
      "buffer_store_dwordx4 v[100:103], v1, s[44:47], s70 offen offset:3072 sc1\n\t"
      "s_waitcnt vmcnt(5) lgkmcnt(4)\n\t"
      "s_mov_b64 exec, 0xff\n\t"
      "buffer_store_dwordx4 a[252:255], v1, s[44:47], s72 offen sc1\n\t"
      "s_mov_b64 exec, -1\n\t"
      "v_mfma_f32_16x16x32_bf16 v[136:139], a[228:231], a[64:67], v[136:139]\n\t"
      "ds_read_b64_tr_b16 a[224:225], v154 offset:24576\n\t"
      "v_mfma_f32_16x16x32_bf16 v[140:143], a[228:231], a[68:71], v[140:143]\n\t"
      "ds_read_b64_tr_b16 a[226:227], v154 offset:26624\n\t"
      "v_mfma_f32_16x16x32_bf16 v[144:147], a[228:231], a[72:75], v[144:147]\n\t"
      "v_mfma_f32_16x16x32_bf16 v[148:151], a[228:231], a[76:79], v[148:151]\n\t"
      "s_waitcnt lgkmcnt(4)\n\t"
      "v_mfma_f32_16x16x32_bf16 v[120:123], a[232:235], a[80:83], v[120:123]\n\t"
      "ds_read_b64_tr_b16 a[228:229], v155 offset:24576\n\t"
      "v_mfma_f32_16x16x32_bf16 v[124:127], a[232:235], a[84:87], v[124:127]\n\t"
      "ds_read_b64_tr_b16 a[230:231], v155 offset:26624\n\t"
      "v_mfma_f32_16x16x32_bf16 v[128:131], a[232:235], a[88:91], v[128:131]\n\t"
      "v_mfma_f32_16x16x32_bf16 v[132:135], a[232:235], a[92:95], v[132:135]\n\t"
      "s_waitcnt lgkmcnt(4)\n\t"
      "v_mfma_f32_16x16x32_bf16 v[136:139], a[236:239], a[80:83], v[136:139]\n\t"
      "ds_read_b64_tr_b16 a[232:233], v154 offset:28672\n\t"
      "v_mfma_f32_16x16x32_bf16 v[140:143], a[236:239], a[84:87], v[140:143]\n\t"
      "ds_read_b64_tr_b16 a[234:235], v154 offset:30720\n\t"
      "v_mfma_f32_16x16x32_bf16 v[144:147], a[236:239], a[88:91], v[144:147]\n\t"
      "v_mfma_f32_16x16x32_bf16 v[148:151], a[236:239], a[92:95], v[148:151]\n\t"
      "s_waitcnt lgkmcnt(4)\n\t"
      "v_mfma_f32_16x16x32_bf16 v[120:123], a[224:227], a[96:99], v[120:123]\n\t"
      "ds_read_b64_tr_b16 a[236:237], v155 offset:28672\n\t"
      "v_mfma_f32_16x16x32_bf16 v[124:127], a[224:227], a[100:103], v[124:127]\n\t"
      "ds_read_b64_tr_b16 a[238:239], v155 offset:30720\n\t"
      "v_mfma_f32_16x16x32_bf16 v[128:131], a[224:227], a[104:107], v[128:131]\n\t"
      "v_mfma_f32_16x16x32_bf16 v[132:135], a[224:227], a[108:111], v[132:135]\n\t"
      "s_waitcnt lgkmcnt(4)\n\t"
      "v_mfma_f32_16x16x32_bf16 v[136:139], a[228:231], a[96:99], v[136:139]\n\t"
      "v_mfma_f32_16x16x32_bf16 v[140:143], a[228:231], a[100:103], v[140:143]\n\t"
      "v_mfma_f32_16x16x32_bf16 v[144:147], a[228:231], a[104:107], v[144:147]\n\t"
      "v_mfma_f32_16x16x32_bf16 v[148:151], a[228:231], a[108:111], v[148:151]\n\t"
      "s_waitcnt lgkmcnt(2)\n\t"
      "v_mfma_f32_16x16x32_bf16 v[120:123], a[232:235], a[112:115], v[120:123]\n\t"
      "v_mfma_f32_16x16x32_bf16 v[124:127], a[232:235], a[116:119], v[124:127]\n\t"
      "v_mfma_f32_16x16x32_bf16 v[128:131], a[232:235], a[120:123], v[128:131]\n\t"
      "v_mfma_f32_16x16x32_bf16 v[132:135], a[232:235], a[124:127], v[132:135]\n\t"
      "s_waitcnt lgkmcnt(0)\n\t"
      "v_mfma_f32_16x16x32_bf16 v[136:139], a[236:239], a[112:115], v[136:139]\n\t"
      "v_mfma_f32_16x16x32_bf16 v[140:143], a[236:239], a[116:119], v[140:143]\n\t"
      "v_mfma_f32_16x16x32_bf16 v[144:147], a[236:239], a[120:123], v[144:147]\n\t"
      "v_mfma_f32_16x16x32_bf16 v[148:151], a[236:239], a[124:127], v[148:151]\n\t"
      "s_waitcnt vmcnt(14)\n\t"
      "ds_read_b64 v[168:169], v247 offset:0\n\t"
      "ds_read_b64 v[170:171], v247 offset:32\n\t"
      "ds_read_b64 v[172:173], v247 offset:1024\n\t"
      "ds_read_b64 v[174:175], v247 offset:1056\n\t"
      "ds_read_b64 v[176:177], v247 offset:2048\n\t"
      "ds_read_b64 v[178:179], v247 offset:2080\n\t"
      "ds_read_b64 v[180:181], v247 offset:3072\n\t"
      "ds_read_b64 v[182:183], v247 offset:3104\n\t"
      "v_and_b32 v21, 128, v20\n\t"
      "v_mov_b32 v20, 256\n\t"
      "v_and_b32 v245, 3, v0\n\t"
      "v_min_u32 v245, 2, v245\n\t"
      "v_lshlrev_b32 v245, 7, v245\n\t"
      "buffer_load_dwordx2 v[22:23], v245, s[44:47], s76 offen sc1\n\t"
      "s_waitcnt vmcnt(2)\n\t"
      "s_mov_b64 exec, 0xff\n\t"
      "buffer_store_dwordx4 a[252:255], v1, s[44:47], s73 offen sc1\n\t"
      "s_mov_b64 exec, -1\n\t"
      "s_waitcnt lgkmcnt(0)\n\t"
      "v_mfma_f32_16x16x32_bf16 v[120:123], v[186:189], v[168:171], v[120:123]\n\t"
      "v_mfma_f32_16x16x32_bf16 v[124:127], v[186:189], v[172:175], v[124:127]\n\t"
      "v_mfma_f32_16x16x32_bf16 v[128:131], v[186:189], v[176:179], v[128:131]\n\t"
      "v_mfma_f32_16x16x32_bf16 v[132:135], v[186:189], v[180:183], v[132:135]\n\t"
      "v_mfma_f32_16x16x32_bf16 v[136:139], v[184:187], v[168:171], v[136:139]\n\t"
      "v_mfma_f32_16x16x32_bf16 v[140:143], v[184:187], v[172:175], v[140:143]\n\t"
      "v_mfma_f32_16x16x32_bf16 v[144:147], v[184:187], v[176:179], v[144:147]\n\t"
      "v_mfma_f32_16x16x32_bf16 v[148:151], v[184:187], v[180:183], v[148:151]\n\t"
      "s_mov_b32 s36, %12\n\t"
      "s_mov_b32 s37, %13\n\t"
      "s_mov_b32 s38, 4194304\n\t"
      "s_mov_b32 s39, 0x20000\n\t"
      "s_waitcnt vmcnt(1)\n\t"
      "v_cmp_eq_u64 vcc, s[58:59], v[22:23]\n\t"
      "s_nop 1\n\t"
      "s_cmp_eq_u64 vcc, -1\n\t"
      "s_cbranch_scc0 .Lkm_slow_%=\n\t"
      "buffer_load_dwordx4 v[40:43], v1, s[44:47], s74 offen offset:0 sc1\n\t"
      "buffer_load_dwordx4 v[44:47], v1, s[44:47], s74 offen offset:1024 sc1\n\t"
      "buffer_load_dwordx4 v[48:51], v1, s[44:47], s74 offen offset:2048 sc1\n\t"
      "buffer_load_dwordx4 v[52:55], v1, s[44:47], s74 offen offset:3072 sc1\n\t"
      "buffer_load_dwordx4 v[72:75], v1, s[44:47], s64 offen offset:0 sc1\n\t"
      "buffer_load_dwordx4 v[76:79], v1, s[44:47], s64 offen offset:1024 sc1\n\t"
      "buffer_load_dwordx4 v[80:83], v1, s[44:47], s64 offen offset:2048 sc1\n\t"
      "buffer_load_dwordx4 v[84:87], v1, s[44:47], s64 offen offset:3072 sc1\n\t"
      "buffer_load_dwordx4 v[104:107], v1, s[44:47], s65 offen offset:0 sc1\n\t"
      "buffer_load_dwordx4 v[108:111], v1, s[44:47], s65 offen offset:1024 sc1\n\t"
      "buffer_load_dwordx4 v[112:115], v1, s[44:47], s65 offen offset:2048 sc1\n\t"
      "buffer_load_dwordx4 v[116:119], v1, s[44:47], s65 offen offset:3072 sc1\n\t"
      "s_waitcnt vmcnt(8)\n\t"
      "v_mfma_f32_16x16x32_bf16 v[120:123], v[186:189], v[40:43], v[120:123]\n\t"
      "v_mfma_f32_16x16x32_bf16 v[124:127], v[186:189], v[44:47], v[124:127]\n\t"
      "v_mfma_f32_16x16x32_bf16 v[128:131], v[186:189], v[48:51], v[128:131]\n\t"
      "v_mfma_f32_16x16x32_bf16 v[132:135], v[186:189], v[52:55], v[132:135]\n\t"
      "v_mfma_f32_16x16x32_bf16 v[136:139], v[184:187], v[40:43], v[136:139]\n\t"
      "v_mfma_f32_16x16x32_bf16 v[140:143], v[184:187], v[44:47], v[140:143]\n\t"
      "v_mfma_f32_16x16x32_bf16 v[144:147], v[184:187], v[48:51], v[144:147]\n\t"
      "v_mfma_f32_16x16x32_bf16 v[148:151], v[184:187], v[52:55], v[148:151]\n\t"
      "s_waitcnt vmcnt(4)\n\t"
      "v_mfma_f32_16x16x32_bf16 v[120:123], v[186:189], v[72:75], v[120:123]\n\t"
      "v_mfma_f32_16x16x32_bf16 v[124:127], v[186:189], v[76:79], v[124:127]\n\t"
      "v_mfma_f32_16x16x32_bf16 v[128:131], v[186:189], v[80:83], v[128:131]\n\t"
      "v_mfma_f32_16x16x32_bf16 v[132:135], v[186:189], v[84:87], v[132:135]\n\t"
      "v_mfma_f32_16x16x32_bf16 v[136:139], v[184:187], v[72:75], v[136:139]\n\t"
      "v_mfma_f32_16x16x32_bf16 v[140:143], v[184:187], v[76:79], v[140:143]\n\t"
      "v_mfma_f32_16x16x32_bf16 v[144:147], v[184:187], v[80:83], v[144:147]\n\t"
      "v_mfma_f32_16x16x32_bf16 v[148:151], v[184:187], v[84:87], v[148:151]\n\t"
      "s_mov_b32 s67, 0\n\t"
      "s_branch .Lkm_have2_%=\n\t"
      ".Lkm_slow_%=:\n\t"
      "s_and_b32 s67, vcc_lo, 3\n\t"
      "s_cmp_eq_u32 s67, 3\n\t"
      "s_cbranch_scc1 .Lkm_p01_ok_%=\n\t"
      "s_mov_b32 s67, 0\n\t"
      ".Lkm_p01_spin_%=:\n\t"
      "s_sleep 1\n\t"
      "buffer_load_dwordx2 v[22:23], v21, s[44:47], s76 offen sc1\n\t"
      "s_add_u32 s67, s67, 1\n\t"
      "s_cmp_ge_u32 s67, 262144\n\t"
      "s_cbranch_scc1 .Lkm_p01_ok_%=\n\t"
      "s_waitcnt vmcnt(0)\n\t"
      "v_cmp_eq_u64 vcc, s[58:59], v[22:23]\n\t"
      "s_nop 1\n\t"
      "s_cmp_eq_u64 vcc, -1\n\t"
      "s_cbranch_scc0 .Lkm_p01_spin_%=\n\t"
      ".Lkm_p01_ok_%=:\n\t"
      "buffer_load_dwordx4 v[40:43], v1, s[44:47], s74 offen offset:0 sc1\n\t"
      "buffer_load_dwordx4 v[44:47], v1, s[44:47], s74 offen offset:1024 sc1\n\t"
      "buffer_load_dwordx4 v[48:51], v1, s[44:47], s74 offen offset:2048 sc1\n\t"
      "buffer_load_dwordx4 v[52:55], v1, s[44:47], s74 offen offset:3072 sc1\n\t"
      "buffer_load_dwordx4 v[72:75], v1, s[44:47], s64 offen offset:0 sc1\n\t"
      "buffer_load_dwordx4 v[76:79], v1, s[44:47], s64 offen offset:1024 sc1\n\t"
      "buffer_load_dwordx4 v[80:83], v1, s[44:47], s64 offen offset:2048 sc1\n\t"
      "buffer_load_dwordx4 v[84:87], v1, s[44:47], s64 offen offset:3072 sc1\n\t"
      "buffer_load_dwordx2 v[22:23], v20, s[44:47], s76 offen sc1\n\t"
      "s_waitcnt vmcnt(5)\n\t"
      "v_mfma_f32_16x16x32_bf16 v[120:123], v[186:189], v[40:43], v[120:123]\n\t"
      "v_mfma_f32_16x16x32_bf16 v[124:127], v[186:189], v[44:47], v[124:127]\n\t"
      "v_mfma_f32_16x16x32_bf16 v[128:131], v[186:189], v[48:51], v[128:131]\n\t"
      "v_mfma_f32_16x16x32_bf16 v[132:135], v[186:189], v[52:55], v[132:135]\n\t"
      "v_mfma_f32_16x16x32_bf16 v[136:139], v[184:187], v[40:43], v[136:139]\n\t"
      "v_mfma_f32_16x16x32_bf16 v[140:143], v[184:187], v[44:47], v[140:143]\n\t"
      "v_mfma_f32_16x16x32_bf16 v[144:147], v[184:187], v[48:51], v[144:147]\n\t"
      "v_mfma_f32_16x16x32_bf16 v[148:151], v[184:187], v[52:55], v[148:151]\n\t"
      "s_waitcnt vmcnt(1)\n\t"
      "v_mfma_f32_16x16x32_bf16 v[120:123], v[186:189], v[72:75], v[120:123]\n\t"
      "v_mfma_f32_16x16x32_bf16 v[124:127], v[186:189], v[76:79], v[124:127]\n\t"
      "v_mfma_f32_16x16x32_bf16 v[128:131], v[186:189], v[80:83], v[128:131]\n\t"
      "v_mfma_f32_16x16x32_bf16 v[132:135], v[186:189], v[84:87], v[132:135]\n\t"
      "v_mfma_f32_16x16x32_bf16 v[136:139], v[184:187], v[72:75], v[136:139]\n\t"
      "v_mfma_f32_16x16x32_bf16 v[140:143], v[184:187], v[76:79], v[140:143]\n\t"
      "v_mfma_f32_16x16x32_bf16 v[144:147], v[184:187], v[80:83], v[144:147]\n\t"
      "v_mfma_f32_16x16x32_bf16 v[148:151], v[184:187], v[84:87], v[148:151]\n\t"
      "s_waitcnt vmcnt(0)\n\t"
      "v_cmp_eq_u64 vcc, s[58:59], v[22:23]\n\t"
      "s_nop 1\n\t"
      "s_cmp_eq_u64 vcc, -1\n\t"
      "s_cbranch_scc1 .Lkm_p2_ok_%=\n\t"
      "s_mov_b32 s67, 0\n\t"
      ".Lkm_p2_spin_%=:\n\t"
      "s_sleep 1\n\t"
      "buffer_load_dwordx2 v[22:23], v20, s[44:47], s76 offen sc1\n\t"
      "s_add_u32 s67, s67, 1\n\t"
      "s_cmp_ge_u32 s67, 262144\n\t"
      "s_cbranch_scc1 .Lkm_p2_ok_%=\n\t"
      "s_waitcnt vmcnt(0)\n\t"
      "v_cmp_eq_u64 vcc, s[58:59], v[22:23]\n\t"
      "s_nop 1\n\t"
      "s_cmp_eq_u64 vcc, -1\n\t"
      "s_cbranch_scc0 .Lkm_p2_spin_%=\n\t"
      ".Lkm_p2_ok_%=:\n\t"
      "buffer_load_dwordx4 v[104:107], v1, s[44:47], s65 offen offset:0 sc1\n\t"
      "buffer_load_dwordx4 v[108:111], v1, s[44:47], s65 offen offset:1024 sc1\n\t"
      "buffer_load_dwordx4 v[112:115], v1, s[44:47], s65 offen offset:2048 sc1\n\t"
      "buffer_load_dwordx4 v[116:119], v1, s[44:47], s65 offen offset:3072 sc1\n\t"
      ".Lkm_have2_%=:\n\t"
      "s_waitcnt vmcnt(3)\n\t"
      "v_mfma_f32_16x16x32_bf16 v[120:123], v[186:189], v[104:107], v[120:123]\n\t"
      "v_mfma_f32_16x16x32_bf16 v[136:139], v[184:187], v[104:107], v[136:139]\n\t"
      "s_waitcnt vmcnt(2)\n\t"
      "v_mfma_f32_16x16x32_bf16 v[124:127], v[186:189], v[108:111], v[124:127]\n\t"
      "v_mfma_f32_16x16x32_bf16 v[140:143], v[184:187], v[108:111], v[140:143]\n\t"
      "s_waitcnt vmcnt(1)\n\t"
      "v_mfma_f32_16x16x32_bf16 v[128:131], v[186:189], v[112:115], v[128:131]\n\t"
      "v_mfma_f32_16x16x32_bf16 v[144:147], v[184:187], v[112:115], v[144:147]\n\t"
      "s_waitcnt vmcnt(0)\n\t"
      "v_mfma_f32_16x16x32_bf16 v[132:135], v[186:189], v[116:119], v[132:135]\n\t"
      "v_mfma_f32_16x16x32_bf16 v[148:151], v[184:187], v[116:119], v[148:151]\n\t"
      "v_cvt_pk_bf16_f32 v120, v120, v121\n\t"
      "v_cvt_pk_bf16_f32 v121, v122, v123\n\t"
      "v_cvt_pk_bf16_f32 v122, v136, v137\n\t"
      "v_cvt_pk_bf16_f32 v123, v138, v139\n\t"
      "ds_write_b64 v247, v[120:121] offset:0\n\t"
      "ds_write_b64 v247, v[122:123] offset:32\n\t"
      "ds_read_b128 v[24:27], v10 offset:0\n\t"
      "v_cvt_pk_bf16_f32 v124, v124, v125\n\t"
      "v_cvt_pk_bf16_f32 v125, v126, v127\n\t"
      "v_cvt_pk_bf16_f32 v126, v140, v141\n\t"
      "v_cvt_pk_bf16_f32 v127, v142, v143\n\t"
      "ds_write_b64 v247, v[124:125] offset:1024\n\t"
      "ds_write_b64 v247, v[126:127] offset:1056\n\t"
      "ds_read_b128 v[28:31], v10 offset:1024\n\t"
      "v_cvt_pk_bf16_f32 v128, v128, v129\n\t"
      "v_cvt_pk_bf16_f32 v129, v130, v131\n\t"
      "v_cvt_pk_bf16_f32 v130, v144, v145\n\t"
      "v_cvt_pk_bf16_f32 v131, v146, v147\n\t"
      "ds_write_b64 v247, v[128:129] offset:2048\n\t"
      "ds_write_b64 v247, v[130:131] offset:2080\n\t"
      "ds_read_b128 v[32:35], v10 offset:2048\n\t"
      "v_cvt_pk_bf16_f32 v132, v132, v133\n\t"
      "v_cvt_pk_bf16_f32 v133, v134, v135\n\t"
      "v_cvt_pk_bf16_f32 v134, v148, v149\n\t"
      "v_cvt_pk_bf16_f32 v135, v150, v151\n\t"
      "ds_write_b64 v247, v[132:133] offset:3072\n\t"
      "ds_write_b64 v247, v[134:135] offset:3104\n\t"
      "ds_read_b128 v[36:39], v10 offset:3072\n\t"
      "s_waitcnt lgkmcnt(9)\n\t"
      "buffer_store_dwordx4 v[24:27], v246, s[36:39], s52 offen sc1\n\t"
      "s_waitcnt lgkmcnt(6)\n\t"
      "buffer_store_dwordx4 v[28:31], v246, s[36:39], s53 offen sc1\n\t"
      "s_waitcnt lgkmcnt(3)\n\t"
      "buffer_store_dwordx4 v[32:35], v246, s[36:39], s54 offen sc1\n\t"
      "s_waitcnt lgkmcnt(0)\n\t"
      "buffer_store_dwordx4 v[36:39], v246, s[36:39], s55 offen sc1\n\t"
      "km_code_end:\n\t"
      :: "s"(lo(ws)), "s"(hi(ws)), "s"(lo(wqkv)), "s"(hi(wqkv)), "s"(lo(w1)), "s"(hi(w1)), "s"(lo(w2)), "s"(hi(w2)), "s"(lo(wo)), "s"(hi(wo)), "s"(lo(x)), "s"(hi(x)),
         "s"(lo(out)), "s"(hi(out)), "s"(w), "s"(bid), "s"(ldsb), "s"(tm), "s"(hd), "s"(kap), "s"(cb), "s"(cf), "s"(ch), "s"(cq), "s"(rb)
      : "v0", "v1", "v2", "v3", "v4", "v5", "v6", "v7", "v8", "v9", "v10", "v11", "v12", "v13", "v14", "v15", "v16", "v17", "v18", "v19", "v20", "v21", "v22", "v23", "v24", "v25", "v26", "v27", "v28", "v29", "v30", "v31", "v32", "v33", "v34", "v35", "v36", "v37", "v38", "v39", "v40", "v41", "v42", "v43", "v44", "v45", "v46", "v47", "v48", "v49", "v50", "v51", "v52", "v53", "v54", "v55", "v56", "v57", "v58", "v59", "v60", "v61", "v62", "v63", "v64", "v65", "v66", "v67", "v68", "v69", "v70", "v71", "v72", "v73", "v74", "v75", "v76", "v77", "v78", "v79", "v80", "v81", "v82", "v83", "v84", "v85", "v86", "v87", "v88", "v89", "v90", "v91", "v92", "v93", "v94", "v95", "v96", "v97", "v98", "v99", "v100", "v101", "v102", "v103", "v104", "v105", "v106", "v107", "v108", "v109", "v110", "v111", "v112", "v113", "v114", "v115", "v116", "v117", "v118", "v119", "v120", "v121", "v122", "v123", "v124", "v125", "v126", "v127", "v128", "v129", "v130", "v131", "v132", "v133", "v134", "v135", "v136", "v137", "v138", "v139", "v140", "v141", "v142", "v143", "v144", "v145", "v146", "v147", "v148", "v149", "v150", "v151", "v152", "v153", "v154", "v155", "v156", "v157", "v158", "v159", "v160", "v161", "v162", "v163", "v164", "v165", "v166", "v167", "v168", "v169", "v170", "v171", "v172", "v173", "v174", "v175", "v176", "v177", "v178", "v179", "v180", "v181", "v182", "v183", "v184", "v185", "v186", "v187", "v188", "v189", "v190", "v191", "v192", "v193", "v194", "v195", "v196", "v197", "v198", "v199", "v200", "v201", "v202", "v203", "v204", "v205", "v206", "v207", "v208", "v209", "v210", "v211", "v212", "v213", "v214", "v215", "v216", "v217", "v218", "v219", "v220", "v221", "v222", "v223", "v224", "v225", "v226", "v227", "v228", "v229", "v230", "v231", "v232", "v233", "v234", "v235", "v236", "v237", "v238", "v239", "v240", "v241", "v242", "v243", "v244", "v245", "v246", "v247", "v248", "v249", "v250", "v251", "v252", "v253", "v254", "v255", "a0", "a1", "a2", "a3", "a4", "a5", "a6", "a7", "a8", "a9", "a10", "a11", "a12", "a13", "a14", "a15", "a16", "a17", "a18", "a19", "a20", "a21", "a22", "a23", "a24", "a25", "a26", "a27", "a28", "a29", "a30", "a31", "a32", "a33", "a34", "a35", "a36", "a37", "a38", "a39", "a40", "a41", "a42", "a43", "a44", "a45", "a46", "a47", "a48", "a49", "a50", "a51", "a52", "a53", "a54", "a55", "a56", "a57", "a58", "a59", "a60", "a61", "a62", "a63", "a64", "a65", "a66", "a67", "a68", "a69", "a70", "a71", "a72", "a73", "a74", "a75", "a76", "a77", "a78", "a79", "a80", "a81", "a82", "a83", "a84", "a85", "a86", "a87", "a88", "a89", "a90", "a91", "a92", "a93", "a94", "a95", "a96", "a97", "a98", "a99", "a100", "a101", "a102", "a103", "a104", "a105", "a106", "a107", "a108", "a109", "a110", "a111", "a112", "a113", "a114", "a115", "a116", "a117", "a118", "a119", "a120", "a121", "a122", "a123", "a124", "a125", "a126", "a127", "a128", "a129", "a130", "a131", "a132", "a133", "a134", "a135", "a136", "a137", "a138", "a139", "a140", "a141", "a142", "a143", "a144", "a145", "a146", "a147", "a148", "a149", "a150", "a151", "a152", "a153", "a154", "a155", "a156", "a157", "a158", "a159", "a160", "a161", "a162", "a163", "a164", "a165", "a166", "a167", "a168", "a169", "a170", "a171", "a172", "a173", "a174", "a175", "a176", "a177", "a178", "a179", "a180", "a181", "a182", "a183", "a184", "a185", "a186", "a187", "a188", "a189", "a190", "a191", "a192", "a193", "a194", "a195", "a196", "a197", "a198", "a199", "a200", "a201", "a202", "a203", "a204", "a205", "a206", "a207", "a208", "a209", "a210", "a211", "a212", "a213", "a214", "a215", "a216", "a217", "a218", "a219", "a220", "a221", "a222", "a223", "a224", "a225", "a226", "a227", "a228", "a229", "a230", "a231", "a232", "a233", "a234", "a235", "a236", "a237", "a238", "a239", "a240", "a241", "a242", "a243", "a244", "a245", "a246", "a247", "a248", "a249", "a250", "a251", "a252", "a253", "a254", "a255", "s36", "s37", "s38", "s39", "s40", "s41", "s42", "s43", "s44", "s45", "s46", "s47", "s48", "s49", "s50", "s51", "s52", "s53", "s54", "s55", "s56", "s57", "s58", "s59", "s60", "s61", "s62", "s63", "s64", "s65", "s66", "s67", "s68", "s69", "s70", "s71", "s72", "s73", "s74", "s75", "s76", "s77", "s78", "s79", "s80", "s81", "s82", "s83", "scc", "vcc", "memory");
}


extern "C" void kernel_launch(void* const* d_in, const int* in_sizes, int n_in, void* d_out, int out_size, void* d_ws, size_t ws_size, hipStream_t stream) {
  const u16* x = (const u16*)d_in[0]; const u16* wqkv = (const u16*)d_in[1]; const u16* wo = (const u16*)d_in[2];
  const u16* w1 = (const u16*)d_in[4]; const u16* w2 = (const u16*)d_in[5];
  char* ws = (char*)d_ws;
  (void)hipFuncSetAttribute((const void*)k1_main, hipFuncAttributeMaxDynamicSharedMemorySize, MAIN_LDS);
  k1_main<<<256, 256, MAIN_LDS, stream>>>(wqkv, w1, w2, wo, x, (u16*)d_out, ws);
}
```
